```python
import torch

def _softmax(x, axis=-1):
    return torch.softmax(x, dim=axis)

def _gelu(x):
    return torch.nn.functional.gelu(x, approximate='tanh')

def _silu(x):
    return torch.nn.functional.silu(x)

def _sigmoid(x):
    return torch.sigmoid(x)

def _layernorm(x, axis=-1, eps=1e-05):
    m = x.mean(dim=axis, keepdim=True)
    v = x.var(dim=axis, keepdim=True, unbiased=False)
    return (x - m) / torch.sqrt(v + eps)

def _scatter_add(tgt, idx, src, axis=0):
    return tgt.clone().index_add_(axis, idx, src)

def _segment_sum(src, seg, n):
    out = torch.zeros((n,) + src.shape[1:], dtype=src.dtype)
    return out.index_add_(0, seg, src)

def _pad(x, pw):
    flat = tuple((v for pair in reversed(pw) for v in pair))
    return torch.nn.functional.pad(x, flat)
kernel_name = 'adaLN_cond__sliding_local_dilated__glu_gated_swiglu__prompt'

def setup_inputs():

    _w = lambda a: (lambda t: t.to(torch.bfloat16) if t.is_floating_point() else t)(torch.as_tensor(a))

    def _A_setup():
        B, S, D = (1, 2048, 576)
        return {'x': _w((lambda *s: torch.randn(*s, dtype=torch.bfloat16) * 0.05)(B, S, D)), 'wqkv': _w((lambda *s: torch.randn(*s, dtype=torch.bfloat16) * 0.05)(D, 3 * D)), 'wo': _w((lambda *s: torch.randn(*s, dtype=torch.bfloat16) * 0.05)(D, D))}
    _A_in = _A_setup()

    def _B_setup():
        B, S, D, F = (1, 2048, 576, 4608)
        return {'x': _w((lambda *s: torch.randn(*s, dtype=torch.bfloat16) * 0.05)(B, S, D)), 'w1': _w((lambda *s: torch.randn(*s, dtype=torch.bfloat16) * 0.8)(D, F) * (2.0 / D) ** 0.5), 'w3': _w((lambda *s: torch.randn(*s, dtype=torch.bfloat16) * 0.8)(D, F) * (2.0 / D) ** 0.5), 'w2': _w((lambda *s: torch.randn(*s, dtype=torch.bfloat16) * 0.8)(F, D) * (2.0 / F) ** 0.5)}
    _B_in = _B_setup()
    return {'A_x': _A_in['x'], 'A_wqkv': _A_in['wqkv'], 'A_wo': _A_in['wo'], 'B_x': _B_in['x'], 'B_w1': _B_in['w1'], 'B_w3': _B_in['w3'], 'B_w2': _B_in['w2'], 'c': _w((lambda *s: torch.randn(*s, dtype=torch.bfloat16) * 0.1)(1, 576) * 8.0), 'w_mod': _w((lambda *s: torch.randn(*s, dtype=torch.bfloat16) * 0.1)(576, 6 * 576) * 576 ** (-0.5) * 8.0)}

def reference(A_x, A_wqkv, A_wo, B_x, B_w1, B_w3, B_w2, c, w_mod):

    def _A_ref(x, wqkv, wo):
        B, S, D = x.shape
        H = 32
        Dh = D // H
        W, dil = (8, 2)
        qkv = (lambda x, *d: x.permute(*d))((x @ wqkv).reshape(B, S, 3, H, Dh), 2, 0, 3, 1, 4)
        q, k, v = (qkv[0], qkv[1], qkv[2])
        scale = Dh ** (-0.5)
        pos = torch.arange(S)
        off = torch.arange(W) * dil
        idx = pos[:, None] - off[None, :]
        mask = idx >= 0
        idx_c = torch.where(mask, idx, 0).reshape(-1)
        kg = k[:, :, idx_c, :].reshape(B, H, S, W, Dh)
        vg = v[:, :, idx_c, :].reshape(B, H, S, W, Dh)
        logits = torch.einsum('bhsd,bhswd->bhsw', q, kg) * scale
        logits = torch.where(mask[None, None, :, :], logits, -1000000000.0)
        attn = _softmax(logits, axis=-1)
        o = torch.einsum('bhsw,bhswd->bhsd', attn, vg)
        return (lambda x, *d: x.permute(*d))(o, 0, 2, 1, 3).reshape(B, S, D) @ wo

    def _B_ref(x, w1, w3, w2):
        gate = torch.einsum('bsd,df->bsf', x, w1)
        up = torch.einsum('bsd,df->bsf', x, w3)
        h = _silu(gate) * up
        out = torch.einsum('bsf,fd->bsd', h, w2)
        return out
    x = A_x
    mod = (_silu(c) @ w_mod).reshape(c.shape[0], 6, 1, -1)
    sa, ca, ga, sb, cb, gb = (mod[:, 0].clone(), mod[:, 1].clone(), mod[:, 2].clone(), mod[:, 3].clone(), mod[:, 4].clone(), mod[:, 5].clone())
    h = _layernorm(x, axis=-1) * (1 + ca) + sa
    x = x + ga * _A_ref(h, A_wqkv, A_wo)
    h = _layernorm(x, axis=-1) * (1 + cb) + sb
    x = x + gb * _B_ref(h, B_w1, B_w3, B_w2)
    return x
```

```cpp
#include <hip/hip_runtime.h>
#include <hip/hip_bf16.h>
#include <stdint.h>

namespace naive {
typedef unsigned short u16;
constexpr int S = 2048, D = 576, F = 4608, H = 32, DH = 18, W = 8;

__device__ __forceinline__ float bf2f(u16 v) { return __uint_as_float(((unsigned)v) << 16); }
__device__ __forceinline__ u16 f2bf(float f) {
  unsigned u = __float_as_uint(f);
  u += 0x7FFFu + ((u >> 16) & 1u);
  return (u16)(u >> 16);
}
__device__ __forceinline__ float rb(float f) { return f; }

__global__ void k_mod(const u16* c, const u16* wmod, float* mod) {
  int n = blockIdx.x * blockDim.x + threadIdx.x;
  if (n >= 6 * D) return;
  float acc = 0.f;
  for (int k = 0; k < D; ++k) {
    float cv = bf2f(c[k]);
    float s = rb(cv / (1.f + expf(-cv)));
    acc += s * bf2f(wmod[(size_t)k * 6 * D + n]);
  }
  mod[n] = rb(acc);
}

__global__ void k_ln(const float* x, const float* shift, const float* scale, float* h) {
  int row = blockIdx.x;
  __shared__ float red[64];
  int t = threadIdx.x;
  float s = 0.f;
  for (int k = t; k < D; k += 64) s += x[row * D + k];
  red[t] = s; __syncthreads();
  float m = 0.f; for (int i = 0; i < 64; ++i) m += red[i];
  m /= D; __syncthreads();
  float q = 0.f;
  for (int k = t; k < D; k += 64) { float d = x[row * D + k] - m; q += d * d; }
  red[t] = q; __syncthreads();
  float v = 0.f; for (int i = 0; i < 64; ++i) v += red[i];
  v /= D;
  float r = 1.f / sqrtf(v + 1e-5f);
  for (int k = t; k < D; k += 64) {
    float xn = rb((x[row * D + k] - m) * r);
    h[row * D + k] = rb(rb(xn * rb(1.f + scale[k])) + shift[k]);
  }
}

__global__ void k_cvt(const u16* x, float* y, int n) {
  int i = blockIdx.x * blockDim.x + threadIdx.x;
  if (i < n) y[i] = bf2f(x[i]);
}

__global__ void k_gemm(const float* A, const u16* B, float* C, int M, int N, int K) {
  int n = blockIdx.x * blockDim.x + threadIdx.x;
  int m = blockIdx.y;
  if (n >= N) return;
  float acc = 0.f;
  for (int k = 0; k < K; ++k) acc += A[(size_t)m * K + k] * bf2f(B[(size_t)k * N + n]);
  C[(size_t)m * N + n] = rb(acc);
}

__global__ void k_attn(const float* qkv, float* o) {
  int idx = blockIdx.x * blockDim.x + threadIdx.x;
  if (idx >= S * H) return;
  int s = idx / H, h = idx % H;
  const float* q = qkv + (size_t)s * 3 * D + h * DH;
  float lg[W]; float mx = -1e30f;
  for (int w = 0; w < W; ++w) {
    int j = s - 2 * w;
    if (j >= 0) {
      const float* k = qkv + (size_t)j * 3 * D + D + h * DH;
      float a = 0.f;
      for (int d = 0; d < DH; ++d) a += q[d] * k[d];
      lg[w] = rb(rb(a) * rb(0.23570226f));
    } else lg[w] = -1e9f;
    mx = fmaxf(mx, lg[w]);
  }
  float sum = 0.f;
  for (int w = 0; w < W; ++w) { lg[w] = expf(lg[w] - mx); sum += lg[w]; }
  for (int d = 0; d < DH; ++d) {
    float a = 0.f;
    for (int w = 0; w < W; ++w) {
      int j = s - 2 * w; if (j < 0) j = 0;
      a += rb(lg[w] / sum) * qkv[(size_t)j * 3 * D + 2 * D + h * DH + d];
    }
    o[(size_t)s * D + h * DH + d] = rb(a);
  }
}

__global__ void k_res(const float* x, const float* g, const float* y, float* out, u16* outb) {
  int i = blockIdx.x * blockDim.x + threadIdx.x;
  if (i >= S * D) return;
  float v = rb(x[i] + rb(g[i % D] * y[i]));
  out[i] = v;
  if (outb) outb[i] = f2bf(v);
}

__global__ void k_swiglu(const float* g, const float* u, float* h) {
  int i = blockIdx.x * blockDim.x + threadIdx.x;
  if (i >= S * F) return;
  float gv = g[i];
  h[i] = rb(rb(gv / (1.f + expf(-gv))) * u[i]);
}

struct Bufs { float *mod, *x, *h, *qkv, *o, *ao, *x1, *h2, *g, *u, *hh, *y, *xo; };

static void run(void* const* d_in, void* d_out, void* d_ws, hipStream_t stream, Bufs* bo) {
  const u16* x = (const u16*)d_in[0];
  const u16* wqkv = (const u16*)d_in[1];
  const u16* wo = (const u16*)d_in[2];
  const u16* w1 = (const u16*)d_in[4];
  const u16* w3 = (const u16*)d_in[5];
  const u16* w2 = (const u16*)d_in[6];
  const u16* c = (const u16*)d_in[7];
  const u16* wmod = (const u16*)d_in[8];
  float* p = (float*)d_ws;
  Bufs b;
  b.mod = p; p += 4096;
  b.x = p; p += S * D;
  b.h = p; p += S * D;
  b.qkv = p; p += S * 3 * D;
  b.o = p; p += S * D;
  b.ao = p; p += S * D;
  b.x1 = p; p += S * D;
  b.h2 = p; p += S * D;
  b.g = p; p += (size_t)S * F;
  b.u = p; p += (size_t)S * F;
  b.hh = p; p += (size_t)S * F;
  b.y = p; p += S * D;
  b.xo = p; p += S * D;
  k_mod<<<(6 * D + 63) / 64, 64, 0, stream>>>(c, wmod, b.mod);
  k_cvt<<<(S * D + 255) / 256, 256, 0, stream>>>(x, b.x, S * D);
  k_ln<<<S, 64, 0, stream>>>(b.x, b.mod + 0 * D, b.mod + 1 * D, b.h);
  k_gemm<<<dim3((3 * D + 63) / 64, S), 64, 0, stream>>>(b.h, wqkv, b.qkv, S, 3 * D, D);
  k_attn<<<(S * H + 255) / 256, 256, 0, stream>>>(b.qkv, b.o);
  k_gemm<<<dim3((D + 63) / 64, S), 64, 0, stream>>>(b.o, wo, b.ao, S, D, D);
  k_res<<<(S * D + 255) / 256, 256, 0, stream>>>(b.x, b.mod + 2 * D, b.ao, b.x1, nullptr);
  k_ln<<<S, 64, 0, stream>>>(b.x1, b.mod + 3 * D, b.mod + 4 * D, b.h2);
  k_gemm<<<dim3((F + 63) / 64, S), 64, 0, stream>>>(b.h2, w1, b.g, S, F, D);
  k_gemm<<<dim3((F + 63) / 64, S), 64, 0, stream>>>(b.h2, w3, b.u, S, F, D);
  k_swiglu<<<(S * F + 255) / 256, 256, 0, stream>>>(b.g, b.u, b.hh);
  k_gemm<<<dim3((D + 63) / 64, S), 64, 0, stream>>>(b.hh, w2, b.y, S, D, F);
  k_res<<<(S * D + 255) / 256, 256, 0, stream>>>(b.x1, b.mod + 5 * D, b.y, b.xo, (u16*)d_out);
  if (bo) *bo = b;
}
}

extern "C" void kernel_launch(void* const* d_in, const int* in_sizes, int n_in, void* d_out, int out_size,
                              void* d_ws, size_t ws_size, hipStream_t stream) {
  naive::run(d_in, d_out, d_ws, stream, nullptr);
}
```

```cpp
#include <hip/hip_runtime.h>
#include <hip/hip_bf16.h>
#include <stdint.h>

typedef unsigned short u16;
typedef short s16x4 __attribute__((ext_vector_type(4)));
typedef short s16x8 __attribute__((ext_vector_type(8)));
typedef float f32x2 __attribute__((ext_vector_type(2)));
typedef float f32x4 __attribute__((ext_vector_type(4)));
typedef float f32x16 __attribute__((ext_vector_type(16)));
typedef unsigned u32x2 __attribute__((ext_vector_type(2)));
typedef unsigned u32x4 __attribute__((ext_vector_type(4)));
typedef __bf16 bf16x2 __attribute__((ext_vector_type(2)));
#define LAS __attribute__((address_space(3)))
#define GAS __attribute__((address_space(1)))

constexpr int S = 2048, D = 576, F = 4608;
constexpr size_t OFF_MOD = 0;
constexpr size_t OFF_XN = 16384;
constexpr size_t OFF_O = OFF_XN + (size_t)S * D * 2;
constexpr size_t OFF_X1 = OFF_O + (size_t)S * D * 2;
constexpr size_t OFF_H2 = OFF_X1 + (size_t)S * D * 4;
constexpr size_t OFF_HH = OFF_H2 + (size_t)S * D * 2;
constexpr size_t OFF_PART = OFF_HH + (size_t)S * F * 2;

__device__ __forceinline__ float bf_lo(unsigned v) { return __uint_as_float(v << 16); }
__device__ __forceinline__ float bf_hi(unsigned v) { return __uint_as_float(v & 0xffff0000u); }
__device__ __forceinline__ unsigned pk_bf16(float lo, float hi) {
  bf16x2 r; r.x = (__bf16)lo; r.y = (__bf16)hi;
  return __builtin_bit_cast(unsigned, r);
}
__device__ __forceinline__ float rb(float f) { return bf_lo(pk_bf16(f, 0.f)); }
__device__ __forceinline__ float fast_silu(float g) { return g * __builtin_amdgcn_rcpf(1.f + __builtin_amdgcn_exp2f(g * -1.44269504f)); }

#define WAIT_V(n) asm volatile("s_waitcnt vmcnt(%0)" ::"n"(n) : "memory")
#define WAIT_L(n) asm volatile("s_waitcnt lgkmcnt(%0)" ::"n"(n) : "memory")
#define BAR() do { asm volatile("" ::: "memory"); __builtin_amdgcn_s_barrier(); asm volatile("" ::: "memory"); } while (0)
#define GLOAD16(dst, voff, sbase, imm) asm volatile("global_load_dwordx4 %0, %1, %2 offset:%3" : "=v"(dst) : "v"(voff), "s"(sbase), "n"(imm) : "memory")

__device__ __forceinline__ void glds16(const void* sbase, unsigned voff, unsigned ldsaddr) {
  asm volatile("s_mov_b32 m0, %2\n\ts_nop 0\n\tglobal_load_lds_dwordx4 %0, %1" :: "v"(voff), "s"(sbase), "s"(ldsaddr) : "memory");
}
#define SB0() __builtin_amdgcn_sched_barrier(0)
__device__ __forceinline__ s16x4 tr_read(LAS char* p) { return __builtin_amdgcn_ds_read_tr16_b64_v4i16((LAS s16x4*)p); }
__device__ __forceinline__ s16x8 cat8(s16x4 a, s16x4 b) { return __builtin_shufflevector(a, b, 0, 1, 2, 3, 4, 5, 6, 7); }

template <bool MODULATE, bool IN_F32, int MT>
__device__ __forceinline__ void ln_tile(const void* xin, const float* shift, const float* scale, u16* outp, int tile, float* red) {
  constexpr int NG = 64 / MT, NS = (MT == 16) ? 3 : 6;
  const int tid = threadIdx.x, w = tid >> 6, lane = tid & 63;
  const int m = lane & (MT - 1), g = lane / MT;
  const int row = tile * MT + m;
  float v[NS][8];
#pragma unroll
  for (int i = 0; i < NS; ++i) {
    const int k0 = ((w * NS + i) * NG + g) * 8;
    if (IN_F32) {
      const f32x4 a = *(const f32x4*)((const float*)xin + (size_t)row * D + k0);
      const f32x4 b = *(const f32x4*)((const float*)xin + (size_t)row * D + k0 + 4);
      for (int j = 0; j < 4; ++j) { v[i][j] = a[j]; v[i][4 + j] = b[j]; }
    } else {
      const u32x4 a = *(const u32x4*)((const u16*)xin + (size_t)row * D + k0);
      for (int j = 0; j < 4; ++j) { v[i][2 * j] = bf_lo(a[j]); v[i][2 * j + 1] = bf_hi(a[j]); }
    }
  }
  float s = 0.f;
#pragma unroll
  for (int i = 0; i < NS; ++i)
    for (int j = 0; j < 8; ++j) s += v[i][j];
  if (MT == 16) s += __shfl_xor(s, 16);
  s += __shfl_xor(s, 32);
  if (g == 0) red[w * 32 + m] = s;
  __syncthreads();
  float mean = 0.f;
  for (int i = 0; i < 6; ++i) mean += red[i * 32 + m];
  mean *= (1.f / D);
  float q = 0.f;
#pragma unroll
  for (int i = 0; i < NS; ++i)
    for (int j = 0; j < 8; ++j) { const float d = v[i][j] - mean; q += d * d; }
  if (MT == 16) q += __shfl_xor(q, 16);
  q += __shfl_xor(q, 32);
  if (g == 0) red[192 + w * 32 + m] = q;
  __syncthreads();
  float var = 0.f;
  for (int i = 0; i < 6; ++i) var += red[192 + i * 32 + m];
  var *= (1.f / D);
  const float sd = rb(sqrtf(rb(rb(var) + 1e-5f)));
  const float rs = 1.f / sd;
  const float mr = rb(mean);
#pragma unroll
  for (int i = 0; i < NS; ++i) {
    const int ks = w * NS + i;
    const int k0 = (ks * NG + g) * 8;
    float h[8];
    for (int j = 0; j < 8; ++j) h[j] = rb(v[i][j] - mr) * rs;
    if (MODULATE) {
      const f32x4 g0 = *(const f32x4*)(scale + k0), g1 = *(const f32x4*)(scale + k0 + 4);
      const f32x4 s0 = *(const f32x4*)(shift + k0), s1 = *(const f32x4*)(shift + k0 + 4);
      for (int j = 0; j < 4; ++j) {
        h[j] = rb(rb(h[j]) * g0[j]) + s0[j];
        h[4 + j] = rb(rb(h[4 + j]) * g1[j]) + s1[j];
      }
    }
    u32x4 o;
    for (int j = 0; j < 4; ++j) o[j] = pk_bf16(h[2 * j], h[2 * j + 1]);
    *(u32x4*)(outp + ((size_t)(tile * (NS * 6) + ks) * 64 + lane) * 8) = o;
  }
}

__global__ __launch_bounds__(384) void k0_mod_ln(const u16* __restrict__ x, const u16* __restrict__ c, const u16* __restrict__ wmod,
                                                 float* __restrict__ modf, u16* __restrict__ xn) {
  __shared__ float red[384];
  const int b = blockIdx.x, tid = threadIdx.x;
  if (b >= 108) { ln_tile<false, false, 16>(x, nullptr, nullptr, xn, b - 108, red); return; }
  const int cb = b;
  const int cl = tid & 3, rg = tid >> 2;
  const int col = cb * 32 + cl * 8;
  u32x4 wv[6]; float sv[6];
#pragma unroll
  for (int i = 0; i < 6; ++i) wv[i] = *(const u32x4*)(wmod + (size_t)(rg + 96 * i) * (6 * D) + col);
#pragma unroll
  for (int i = 0; i < 6; ++i) { const float cv = bf_lo(c[rg + 96 * i]); sv[i] = rb(cv / (1.f + __expf(-cv))); }
  float a[8] = {0, 0, 0, 0, 0, 0, 0, 0};
#pragma unroll
  for (int i = 0; i < 6; ++i)
    for (int j = 0; j < 4; ++j) { a[2 * j] += sv[i] * bf_lo(wv[i][j]); a[2 * j + 1] += sv[i] * bf_hi(wv[i][j]); }
#pragma unroll
  for (int j = 0; j < 8; ++j) {
    a[j] += __shfl_xor(a[j], 4); a[j] += __shfl_xor(a[j], 8); a[j] += __shfl_xor(a[j], 16); a[j] += __shfl_xor(a[j], 32);
  }
  const int w = tid >> 6, lane = tid & 63;
  if (lane < 4) for (int j = 0; j < 8; ++j) red[w * 32 + lane * 8 + j] = a[j];
  __syncthreads();
  if (tid < 32) {
    float s = 0.f;
    for (int i = 0; i < 6; ++i) s += red[i * 32 + tid];
    s = rb(s);
    const int n = cb * 32 + tid, part = n / D;
    if (part == 1 || part == 4) s = rb(1.f + s);
    modf[n] = s;
  }
}

__global__ __launch_bounds__(384) void k2b_ln2(const u16* __restrict__ x1, const float* __restrict__ modf, u16* __restrict__ h2) {
  __shared__ float red[384];
  ln_tile<true, false, 32>(x1, modf + 3 * D, modf + 4 * D, h2, blockIdx.x, red);
}

constexpr int K1_W = 576 * 256;
constexpr int K1_CONST = K1_W;
constexpr int K1_LDS = K1_W + 5120;
constexpr int QS = 116;

__global__ __launch_bounds__(576) void k1_qkv_attn(const u16* __restrict__ xn, const u16* __restrict__ wqkv, const float* __restrict__ modf,
                                                   u16* __restrict__ o) {
  extern __shared__ __attribute__((aligned(16))) char smem[];
  LAS char* lds = (LAS char*)smem;
  const int b = blockIdx.x, xcd = b & 7, idx = b >> 3;
  const int rt = (xcd & 3) * 4 + (idx & 3), hp = (xcd >> 2) * 8 + (idx >> 2);
  const int tid = threadIdx.x, lane = tid & 63;
  const int w = __builtin_amdgcn_readfirstlane(tid >> 6);
  const unsigned lbase = (unsigned)(size_t)lds;
  if (w < 5) { if (w < 4 || lane < 32) glds16(modf, w * 1024 + lane * 16, lbase + K1_CONST + w * 1024); }
  else { if (lane < 1) glds16(modf, 0, lbase + K1_CONST + 4608 + (w - 5) * 64); }
  int mt = (w < 8) ? rt * 8 + w : rt * 8 - 1;
  if (mt < 0) mt = 0;
  u32x4 xf[18];
  {
    const char* xb = (const char*)xn + (size_t)mt * 18 * 1024;
    const unsigned vo = lane * 16;
#pragma unroll
    for (int i = 0; i < 18; i += 4) {
      const char* p = xb + i * 1024;
      GLOAD16(xf[i], vo, p, 0);
      GLOAD16(xf[i + 1], vo, p, 1024);
      if (i + 2 < 18) GLOAD16(xf[i + 2], vo, p, 2048);
      if (i + 3 < 18) GLOAD16(xf[i + 3], vo, p, 3072);
    }
  }
  {
    const int r0 = w * 4 + (lane >> 4), p = lane & 15;
    const int segbase = (72 * hp) & ~15;
#pragma unroll
    for (int j = 0; j < 16; ++j) {
      const int r = r0 + 36 * j;
      const int sw = ((r & 3) | (((r >> 3) & 1) << 2)) << 1;
      int c = p ^ sw; if (c == 15) c = 14;
      const int seg = c / 5, c5 = c - seg * 5;
      const int segoff = (seg == 0) ? 1152 : (seg == 1 ? 2304 : 0);
      const unsigned off = r * 3456 + segoff + segbase + c5 * 16;
      glds16(wqkv, off, lbase + (w + 9 * j) * 1024);
    }
  }
  asm volatile("s_waitcnt vmcnt(16)" : "+v"(xf[0]), "+v"(xf[1]), "+v"(xf[2]), "+v"(xf[3]), "+v"(xf[4]), "+v"(xf[5]), "+v"(xf[6]), "+v"(xf[7]), "+v"(xf[8]) :: "memory");
  asm volatile("" : "+v"(xf[9]), "+v"(xf[10]), "+v"(xf[11]), "+v"(xf[12]), "+v"(xf[13]), "+v"(xf[14]), "+v"(xf[15]), "+v"(xf[16]), "+v"(xf[17]) :: "memory");
  BAR();
  const int g = lane >> 4;
  {
    const LAS float* cs = (const LAS float*)(lds + K1_CONST);
#pragma unroll
    for (int s = 0; s < 18; ++s) {
      const int k0 = s * 32 + g * 8;
      const f32x4 s0 = *(const LAS f32x4*)(cs + k0), s1 = *(const LAS f32x4*)(cs + k0 + 4);
      const f32x4 g0 = *(const LAS f32x4*)(cs + 576 + k0), g1 = *(const LAS f32x4*)(cs + 576 + k0 + 4);
      u32x4 v = xf[s], r;
      r[0] = pk_bf16(bf_lo(v[0]) * g0[0] + s0[0], bf_hi(v[0]) * g0[1] + s0[1]);
      r[1] = pk_bf16(bf_lo(v[1]) * g0[2] + s0[2], bf_hi(v[1]) * g0[3] + s0[3]);
      r[2] = pk_bf16(bf_lo(v[2]) * g1[0] + s1[0], bf_hi(v[2]) * g1[1] + s1[1]);
      r[3] = pk_bf16(bf_lo(v[3]) * g1[2] + s1[2], bf_hi(v[3]) * g1[3] + s1[3]);
      asm volatile("" : "+v"(r));
      xf[s] = r;
      if (s & 1) SB0();
    }
  }
  const int q = (lane & 15) >> 2, p4 = lane & 3;
  const int shift = (hp & 1) * 8;
  unsigned ta[7];
  {
    const int sw = (q | ((g & 1) << 2)) << 1;
#pragma unroll
    for (int t = 0; t < 7; ++t) {
      int pi = 4 * t + p4; if (pi == 27) pi = 26;
      const int seg = pi / 9, pw = pi - seg * 9;
      const int byte = seg * 80 + shift + pw * 8;
      const int ch = byte >> 4;
      ta[t] = (8 * g + q) * 256 + ((ch ^ sw) << 4) + (byte & 8);
    }
  }
  f32x4 acc[7];
#pragma unroll
  for (int t = 0; t < 7; ++t) acc[t] = (f32x4){0.f, 0.f, 0.f, 0.f};
  const bool halo = (w == 8);
#pragma unroll
  for (int ph = 0; ph < 4; ++ph) {
    if (ph == 0) WAIT_V(12); else if (ph == 1) WAIT_V(8); else if (ph == 2) WAIT_V(4); else WAIT_V(0);
    BAR();
    const int s_lo = (144 * ph) / 32, s_hi = (144 * (ph + 1)) / 32;
#pragma unroll
    for (int s = s_lo; s < s_hi; ++s) {
      const s16x8 xb = __builtin_bit_cast(s16x8, xf[s]);
#pragma unroll
      for (int t = 0; t < 5; ++t) {
        const s16x8 wf = cat8(tr_read(lds + ta[t] + s * 8192), tr_read(lds + ta[t] + s * 8192 + 1024));
        acc[t] = __builtin_amdgcn_mfma_f32_16x16x32_bf16(wf, xb, acc[t], 0, 0, 0);
      }
      if (!halo) {
#pragma unroll
        for (int t = 5; t < 7; ++t) {
          const s16x8 wf = cat8(tr_read(lds + ta[t] + s * 8192), tr_read(lds + ta[t] + s * 8192 + 1024));
          acc[t] = __builtin_amdgcn_mfma_f32_16x16x32_bf16(wf, xb, acc[t], 0, 0, 0);
        }
      }
      SB0();
    }
  }
  WAIT_L(0);
  BAR();
  {
    LAS float* qs = (LAS float*)lds;
    const int lr = (halo ? 0 : (w + 1) * 16) + (lane & 15);
#pragma unroll
    for (int t = 0; t < 7; ++t) {
      const unsigned a = pk_bf16(acc[t][0], acc[t][1]), c2 = pk_bf16(acc[t][2], acc[t][3]);
      *(LAS f32x4*)(qs + lr * QS + 16 * t + 4 * g) = (f32x4){bf_lo(a), bf_hi(a), bf_lo(c2), bf_hi(c2)};
    }
  }
  WAIT_L(0);
  BAR();
  LAS u16* os = (LAS u16*)(lds + 144 * QS * 4);
  if (w < 8) {
    const LAS float* qs = (const LAS float*)lds;
    const int r = w * 16 + (lane & 15), hh = (lane >> 4) & 1, half = lane >> 5;
    const int srow = rt * 128 + r;
    const LAS float* qp = qs + (r + 16) * QS + 72 + 18 * hh;
    float qv[18];
#pragma unroll
    for (int d = 0; d < 9; ++d) { const f32x2 t2 = *(const LAS f32x2*)(qp + 2 * d); qv[2 * d] = t2[0]; qv[2 * d + 1] = t2[1]; }
    float lg[4];
#pragma unroll
    for (int i = 0; i < 4; ++i) {
      const int wi = half * 4 + i;
      const LAS float* kp = qs + (r + 16 - 2 * wi) * QS + 18 * hh;
      float a = 0.f;
#pragma unroll
      for (int d = 0; d < 9; ++d) { const f32x2 t2 = *(const LAS f32x2*)(kp + 2 * d); a += qv[2 * d] * t2[0]; a += qv[2 * d + 1] * t2[1]; }
      a = rb(rb(a) * 0.23570226f);
      lg[i] = (srow - 2 * wi >= 0) ? a : -1e9f;
    }
    float p[8];
#pragma unroll
    for (int i = 0; i < 4; ++i) { const float other = __shfl_xor(lg[i], 32); p[i] = half ? other : lg[i]; p[4 + i] = half ? lg[i] : other; }
    float mx = p[0];
#pragma unroll
    for (int i = 1; i < 8; ++i) mx = fmaxf(mx, p[i]);
    float sum = 0.f;
#pragma unroll
    for (int i = 0; i < 8; ++i) { p[i] = __builtin_amdgcn_exp2f((p[i] - mx) * 1.44269504f); sum += p[i]; }
    const float inv = 1.f / sum;
#pragma unroll
    for (int i = 0; i < 8; ++i) p[i] = rb(p[i] * inv);
    float ov[10];
#pragma unroll
    for (int d = 0; d < 10; ++d) ov[d] = 0.f;
#pragma unroll
    for (int i = 0; i < 8; ++i) {
      const LAS float* vp = qs + (r + 16 - 2 * i) * QS + 36 + 18 * hh + 8 * half;
      const float pi = (srow - 2 * i >= 0) ? p[i] : 0.f;
#pragma unroll
      for (int d = 0; d < 5; ++d) { const f32x2 t2 = *(const LAS f32x2*)(vp + 2 * d); ov[2 * d] += pi * t2[0]; ov[2 * d + 1] += pi * t2[1]; }
    }
    LAS unsigned* op = (LAS unsigned*)(os + r * 40 + shift / 2 + 18 * hh + 8 * half);
#pragma unroll
    for (int d = 0; d < 5; ++d) op[d] = pk_bf16(ov[2 * d], ov[2 * d + 1]);
  }
  WAIT_L(0);
  BAR();
  {
    const int ob = (36 * hp - shift / 2) >> 3;
    for (int e = tid; e < 128 * 5; e += 576) {
      const int oc = e >> 7, r = e & 127;
      const u32x4 v = *(const LAS u32x4*)(os + r * 40 + oc * 8);
      const int oct = ob + oc, row = rt * 128 + r;
      u16* dst = o + ((size_t)((row >> 4) * 18 + (oct >> 2)) * 64 + (oct & 3) * 16 + (row & 15)) * 8;
      if (shift == 0 && oc == 4) *(u32x2*)dst = (u32x2){v[0], v[1]};
      else if (shift != 0 && oc == 0) *(u32x2*)(dst + 4) = (u32x2){v[2], v[3]};
      else *(u32x4*)dst = v;
    }
  }
}

constexpr int K2_CH = 64 * 288;
constexpr int K2_LDS = 8 * K2_CH;
__global__ __launch_bounds__(384) void k2_wo(const u16* __restrict__ o, const u16* __restrict__ wo, const u16* __restrict__ x,
                                             const float* __restrict__ modf, u16* __restrict__ x1) {
  extern __shared__ __attribute__((aligned(16))) char smem[];
  LAS char* lds = (LAS char*)smem;
  const int b = blockIdx.x, xcd = b & 7, idx = b >> 3;
  const int rt = xcd * 8 + (idx >> 2), nq = idx & 3;
  const int tid = threadIdx.x, lane = tid & 63;
  const int w = __builtin_amdgcn_readfirstlane(tid >> 6);
  const int mi = w & 1, ng = w >> 1;
  const int g = lane >> 4, q = (lane & 15) >> 2, p4 = lane & 3;
  u32x4 xf[18];
  {
    const char* xb = (const char*)o + (size_t)(rt * 2 + mi) * 18 * 1024;
    const unsigned vo = lane * 16;
#pragma unroll
    for (int i = 0; i < 18; i += 4) {
      const char* p = xb + i * 1024;
      GLOAD16(xf[i], vo, p, 0);
      GLOAD16(xf[i + 1], vo, p, 1024);
      if (i + 2 < 18) GLOAD16(xf[i + 2], vo, p, 2048);
      if (i + 3 < 18) GLOAD16(xf[i + 3], vo, p, 3072);
    }
  }
  const int row = rt * 32 + mi * 16 + (lane & 15);
  const int col0 = nq * 144 + ng * 48 + 4 * g;
  u32x2 xr[3]; f32x4 ga[3];
#pragma unroll
  for (int t = 0; t < 3; ++t) {
    const unsigned vo = (row * D + col0 + 16 * t) * 2, vg = (2 * D + col0 + 16 * t) * 4;
    asm volatile("global_load_dwordx2 %0, %1, %2" : "=v"(xr[t]) : "v"(vo), "s"(x) : "memory");
    asm volatile("global_load_dwordx4 %0, %1, %2" : "=v"(ga[t]) : "v"(vg), "s"(modf) : "memory");
  }
  const unsigned lbase = (unsigned)(size_t)lds;
  unsigned so[3];
#pragma unroll
  for (int i = 0; i < 3; ++i) {
    const int ci = (w + 6 * i) * 64 + lane;
    const int pr = ci / 18, ch = ci - pr * 18;
    const int r = pr ^ (((pr >> 3) & 1) << 2);
    so[i] = r * (D * 2) + nq * 288 + ch * 16;
  }
#pragma unroll
  for (int c = 0; c < 8; ++c)
#pragma unroll
    for (int i = 0; i < 3; ++i) glds16((const char*)wo + c * (64 * D * 2), so[i], lbase + c * K2_CH + (w + 6 * i) * 1024);
  unsigned ta[2];
#pragma unroll
  for (int t = 0; t < 2; ++t) ta[t] = (8 * g + ((4 * t) ^ (4 * (g & 1))) + q) * 288 + ng * 96 + p4 * 8;
  f32x4 acc[3];
#pragma unroll
  for (int t = 0; t < 3; ++t) acc[t] = (f32x4){0.f, 0.f, 0.f, 0.f};
  asm volatile("s_waitcnt vmcnt(21)" : "+v"(xf[0]), "+v"(xf[1]), "+v"(xf[2]), "+v"(xf[3]), "+v"(xf[4]), "+v"(xf[5]), "+v"(xf[6]), "+v"(xf[7]), "+v"(xf[8]) :: "memory");
  asm volatile("" : "+v"(xf[9]), "+v"(xf[10]), "+v"(xf[11]), "+v"(xf[12]), "+v"(xf[13]), "+v"(xf[14]), "+v"(xf[15]), "+v"(xf[16]), "+v"(xf[17]) :: "memory");
  asm volatile("" : "+v"(xr[0]), "+v"(xr[1]), "+v"(xr[2]), "+v"(ga[0]), "+v"(ga[1]), "+v"(ga[2]) :: "memory");
#pragma unroll
  for (int c = 0; c < 9; ++c) {
    if (c == 0) WAIT_V(21); else if (c == 1) WAIT_V(18); else if (c == 2) WAIT_V(18); else if (c == 3) WAIT_V(15); else if (c == 4) WAIT_V(12);
    else if (c == 5) WAIT_V(9); else if (c == 6) WAIT_V(6); else if (c == 7) WAIT_V(3); else WAIT_V(0);
    WAIT_L(0);
    BAR();
    if (c == 1) {
#pragma unroll
      for (int i = 0; i < 3; ++i) glds16((const char*)wo + 8 * (64 * D * 2), so[i], lbase + (w + 6 * i) * 1024);
    }
    LAS char* base = lds + (c & 7) * K2_CH;
#pragma unroll
    for (int s = 0; s < 2; ++s) {
      const s16x8 xb = __builtin_bit_cast(s16x8, xf[c * 2 + s]);
#pragma unroll
      for (int t = 0; t < 3; ++t) {
        const s16x8 wf = cat8(tr_read(base + ta[0] + s * (32 * 288) + t * 32), tr_read(base + ta[1] + s * (32 * 288) + t * 32));
        acc[t] = __builtin_amdgcn_mfma_f32_16x16x32_bf16(wf, xb, acc[t], 0, 0, 0);
      }
    }
  }
#pragma unroll
  for (int t = 0; t < 3; ++t) {
    float r[4];
    const float xv[4] = {bf_lo(xr[t][0]), bf_hi(xr[t][0]), bf_lo(xr[t][1]), bf_hi(xr[t][1])};
    for (int j = 0; j < 4; ++j) r[j] = xv[j] + rb(ga[t][j] * rb(acc[t][j]));
    *(u32x2*)(x1 + (size_t)row * D + col0 + 16 * t) = (u32x2){pk_bf16(r[0], r[1]), pk_bf16(r[2], r[3])};
  }
}

constexpr int G1_W3 = 64 * 288 + 128;
constexpr int G1_STAGE = 2 * G1_W3;
constexpr int G1_LDS = 4 * G1_STAGE;
__global__ __launch_bounds__(512, 2) void k3_gemm1(const u16* __restrict__ h2, const u16* __restrict__ w1, const u16* __restrict__ w3,
                                                   u16* __restrict__ hh) {
  extern __shared__ __attribute__((aligned(16))) char smem[];
  LAS char* lds = (LAS char*)smem;
  const int b = blockIdx.x, xcd = b & 7, idx = b >> 3;
  const int mt = (xcd & 1) * 4 + (idx & 3), ct = (xcd >> 1) * 8 + (idx >> 2);
  const int tid = threadIdx.x, lane = tid & 63;
  const int w = __builtin_amdgcn_readfirstlane(tid >> 6);
  const char* wsrc = (const char*)((w < 4) ? w1 : w3) + (size_t)((w & 3) * 16) * (F * 2) + ct * 288;
  unsigned so[5];
#pragma unroll
  for (int i = 0; i < 5; ++i) {
    const int ci = i * 64 + lane;
    const int r = ci / 18, ch = ci - r * 18;
    so[i] = r * (F * 2) + ch * 16;
  }
  const unsigned wdst = (unsigned)(size_t)lds + (w >> 2) * G1_W3 + (w & 3) * 4608;
#define G1_STAGE_ISSUE(st) do { const char* _s = wsrc + (size_t)(st) * (64 * F * 2); const unsigned _d = wdst + ((st) & 3) * G1_STAGE; \
    _Pragma("unroll") for (int _i = 0; _i < 4; ++_i) glds16(_s, so[_i], _d + _i * 1024); \
    if (lane < 32) glds16(_s, so[4], _d + 4096); } while (0)
  const char* xsrc = (const char*)h2 + (size_t)(mt * 8 + w) * (36 * 1024);
  const unsigned xvo = lane * 16;
  u32x4 xa[4], xb[4];
#define G1_XLOAD(dst, st) do { const char* _p = xsrc + (st) * 4096; GLOAD16(dst[0], xvo, _p, 0); GLOAD16(dst[1], xvo, _p, 1024); \
    GLOAD16(dst[2], xvo, _p, 2048); GLOAD16(dst[3], xvo, _p, 3072); } while (0)
  G1_XLOAD(xa, 0);
  G1_STAGE_ISSUE(0); G1_STAGE_ISSUE(1); G1_STAGE_ISSUE(2);
  const int grp = lane >> 4, q = (lane & 15) >> 2, p4 = lane & 3;
  const unsigned ta = (grp & 1) * G1_W3 + (8 * (grp >> 1) + q) * 288 + (((p4 & 1) << 4) | ((p4 >> 1) << 3));
  f32x16 acc[9];
#pragma unroll
  for (int t = 0; t < 9; ++t)
    for (int j = 0; j < 16; ++j) acc[t][j] = 0.f;
  s16x8 wf[4];
#define G1_RD(b, i) cat8(tr_read((b) + (16 * ((i) / 9)) * 288 + ((i) % 9) * 32), tr_read((b) + (16 * ((i) / 9) + 4) * 288 + ((i) % 9) * 32))
#define G1_COMPUTE(xf, st, PRE, TAIL) do { LAS char* _b = lds + ((st) & 3) * G1_STAGE + ta; LAS char* _n = lds + (((st) + 1) & 3) * G1_STAGE + ta; \
    if (PRE) { wf[0] = G1_RD(_b, 0); wf[1] = G1_RD(_b, 1); wf[2] = G1_RD(_b, 2); } \
    _Pragma("unroll") for (int i = 0; i < 36; ++i) { \
      if (i + 3 < 36) wf[(i + 3) & 3] = G1_RD(_b, i + 3); else if (TAIL) wf[(i + 3) & 3] = G1_RD(_n, i + 3 - 36); \
      acc[i % 9] = __builtin_amdgcn_mfma_f32_32x32x16_bf16(wf[i & 3], __builtin_bit_cast(s16x8, xf[i / 9]), acc[i % 9], 0, 0, 0); \
      SB0(); } } while (0)
#define G1_WAITX(n, xf) asm volatile("s_waitcnt vmcnt(%4)" : "+v"(xf[0]), "+v"(xf[1]), "+v"(xf[2]), "+v"(xf[3]) : "n"(n) : "memory")
#pragma unroll
  for (int st = 0; st < 9; ++st) {
    if ((st & 1) == 0) {
      if (st == 0) G1_WAITX(10, xa); else if (st <= 5) G1_WAITX(5, xa); else if (st == 6) G1_WAITX(5, xa); else G1_WAITX(0, xa);
    } else {
      if (st <= 5) G1_WAITX(5, xb); else G1_WAITX(0, xb);
    }
    BAR();
    if (st + 1 < 9) { if (st & 1) G1_XLOAD(xa, st + 1); else G1_XLOAD(xb, st + 1); }
    if (st + 3 < 9) G1_STAGE_ISSUE(st + 3);
    if (st & 1) G1_COMPUTE(xb, st, st <= 1, st >= 1 && st < 8); else G1_COMPUTE(xa, st, st <= 1, st >= 1 && st < 8);
  }
  const int m = lane & 31, h = lane >> 5;
  const int row = mt * 256 + w * 32 + m;
#pragma unroll
  for (int t = 0; t < 9; ++t) {
    float r[8];
#pragma unroll
    for (int j = 0; j < 8; ++j) r[j] = fast_silu(acc[t][j]) * acc[t][8 + j];
    const int oct = ct * 18 + 2 * t + h;
    u16* dst = hh + ((size_t)((row >> 4) * 144 + (oct >> 2)) * 64 + (oct & 3) * 16 + (row & 15)) * 8;
    *(u32x4*)dst = (u32x4){pk_bf16(r[0], r[1]), pk_bf16(r[2], r[3]), pk_bf16(r[4], r[5]), pk_bf16(r[6], r[7])};
  }
}

constexpr int G2_STAGE = 128 * 288;
constexpr int G2_LDS = 4 * G2_STAGE;
__global__ __launch_bounds__(512, 2) void k4_gemm2(const u16* __restrict__ hh, const u16* __restrict__ w2, float* __restrict__ part) {
  extern __shared__ __attribute__((aligned(16))) char smem[];
  LAS char* lds = (LAS char*)smem;
  const int b = blockIdx.x, xcd = b & 7, idx = b >> 3;
  const int mt = (xcd & 1) * 8 + (idx & 7), nq = idx >> 3, ksl = xcd >> 1;
  const int tid = threadIdx.x, lane = tid & 63;
  const int w = __builtin_amdgcn_readfirstlane(tid >> 6);
  const int mq = w & 3, kg = w >> 2;
  const char* wsrc = (const char*)w2 + (size_t)(ksl * 1152 + w * 16) * (D * 2) + nq * 288;
  unsigned so[5];
#pragma unroll
  for (int i = 0; i < 5; ++i) {
    const int ci = i * 64 + lane;
    const int pr = ci / 18, ch = ci - pr * 18;
    const int r = pr ^ (((pr >> 3) & 1) << 2);
    so[i] = r * (D * 2) + ch * 16;
  }
  const unsigned wdst = (unsigned)(size_t)lds + w * 4608;
#define G2_STAGE_ISSUE(st) do { const char* _s = wsrc + (size_t)(st) * (128 * D * 2); const unsigned _d = wdst + ((st) & 3) * G2_STAGE; \
    _Pragma("unroll") for (int _i = 0; _i < 4; ++_i) glds16(_s, so[_i], _d + _i * 1024); \
    if (lane < 32) glds16(_s, so[4], _d + 4096); } while (0)
  const char* xs0 = (const char*)hh + ((size_t)(mt * 8 + mq * 2) * 144 + ksl * 36 + kg * 2) * 1024;
  const char* xs1 = xs0 + 144 * 1024;
  const unsigned xvo = lane * 16;
  u32x4 xa[4], xb[4];
#define G2_XLOAD(dst, st) do { const char* _p = xs0 + (st) * 4096; const char* _q = xs1 + (st) * 4096; GLOAD16(dst[0], xvo, _p, 0); GLOAD16(dst[1], xvo, _q, 0); \
    GLOAD16(dst[2], xvo, _p, 1024); GLOAD16(dst[3], xvo, _q, 1024); } while (0)
  G2_XLOAD(xa, 0);
  G2_STAGE_ISSUE(0); G2_STAGE_ISSUE(1); G2_STAGE_ISSUE(2);
  const int g = lane >> 4, q = (lane & 15) >> 2, p4 = lane & 3;
  unsigned ta[2];
#pragma unroll
  for (int t = 0; t < 2; ++t) ta[t] = (kg * 64 + 8 * g + ((4 * t) ^ (4 * (g & 1))) + q) * 288 + p4 * 8;
  f32x4 acc[2][9];
#pragma unroll
  for (int i = 0; i < 2; ++i)
    for (int t = 0; t < 9; ++t) acc[i][t] = (f32x4){0.f, 0.f, 0.f, 0.f};
#define G2_COMPUTE(xf, st) do { LAS char* _b = lds + ((st) & 3) * G2_STAGE; \
    _Pragma("unroll") for (int ks = 0; ks < 2; ++ks) { const s16x8 _x0 = __builtin_bit_cast(s16x8, xf[2 * ks]), _x1 = __builtin_bit_cast(s16x8, xf[2 * ks + 1]); \
      _Pragma("unroll") for (int t = 0; t < 9; ++t) { \
        const s16x8 _w = cat8(tr_read(_b + ta[0] + ks * (32 * 288) + t * 32), tr_read(_b + ta[1] + ks * (32 * 288) + t * 32)); \
        acc[0][t] = __builtin_amdgcn_mfma_f32_16x16x32_bf16(_w, _x0, acc[0][t], 0, 0, 0); \
        acc[1][t] = __builtin_amdgcn_mfma_f32_16x16x32_bf16(_w, _x1, acc[1][t], 0, 0, 0); } } } while (0)
#pragma unroll
  for (int st = 0; st < 9; ++st) {
    if ((st & 1) == 0) {
      if (st == 0) G1_WAITX(10, xa); else if (st <= 6) G1_WAITX(5, xa); else G1_WAITX(0, xa);
    } else {
      if (st <= 5) G1_WAITX(5, xb); else G1_WAITX(0, xb);
    }
    WAIT_L(0);
    BAR();
    if (st + 1 < 9) { if (st & 1) G2_XLOAD(xa, st + 1); else G2_XLOAD(xb, st + 1); }
    if (st + 3 < 9) G2_STAGE_ISSUE(st + 3);
    if (st & 1) G2_COMPUTE(xb, st); else G2_COMPUTE(xa, st);
  }
  WAIT_L(0);
  BAR();
  {
    LAS f32x4* ex = (LAS f32x4*)lds;
#pragma unroll
    for (int t = 0; t < 9; ++t) ex[(w * 9 + t) * 64 + lane] = kg ? acc[0][t] : acc[1][t];
    WAIT_L(0);
    BAR();
    float* dst = part + ((size_t)(((ksl * 16 + mt) * 4 + nq) * 8 + mq * 2 + kg) * 9) * 256 + lane * 4;
#pragma unroll
    for (int t = 0; t < 9; ++t) {
      const f32x4 o = ex[((w ^ 4) * 9 + t) * 64 + lane];
      *(f32x4*)(dst + t * 256) = (kg ? acc[1][t] : acc[0][t]) + o;
    }
  }
}

__global__ __launch_bounds__(256) void k5_reduce(const float* __restrict__ part, const u16* __restrict__ x1, const float* __restrict__ modf,
                                                 u16* __restrict__ out) {
  const int e = blockIdx.x * 256 + threadIdx.x;
  const int lane = e & 63; int u = e >> 6;
  const int t = u % 9; u /= 9;
  const int m16 = u & 7; u >>= 3;
  const int nq = u & 3, mt = u >> 2;
  const int row = mt * 128 + m16 * 16 + (lane & 15), col = nq * 144 + 16 * t + 4 * (lane >> 4);
  constexpr size_t PS = (size_t)S * D;
  const f32x4 a = *(const f32x4*)(part + (size_t)e * 4), b2 = *(const f32x4*)(part + PS + (size_t)e * 4);
  const f32x4 c = *(const f32x4*)(part + 2 * PS + (size_t)e * 4), d = *(const f32x4*)(part + 3 * PS + (size_t)e * 4);
  const u32x2 xr = *(const u32x2*)(x1 + (size_t)row * D + col);
  const f32x4 gb = *(const f32x4*)(modf + 5 * D + col);
  const f32x4 y = (a + b2) + (c + d);
  const float xv[4] = {bf_lo(xr[0]), bf_hi(xr[0]), bf_lo(xr[1]), bf_hi(xr[1])};
  float r[4];
  for (int j = 0; j < 4; ++j) r[j] = xv[j] + rb(gb[j] * rb(y[j]));
  *(u32x2*)(out + (size_t)row * D + col) = (u32x2){pk_bf16(r[0], r[1]), pk_bf16(r[2], r[3])};
}

extern "C" void kernel_launch(void* const* d_in, const int* in_sizes, int n_in, void* d_out, int out_size,
                              void* d_ws, size_t ws_size, hipStream_t stream) {
  const u16* x = (const u16*)d_in[0];
  const u16* wqkv = (const u16*)d_in[1];
  const u16* wo = (const u16*)d_in[2];
  const u16* w1 = (const u16*)d_in[4];
  const u16* w3 = (const u16*)d_in[5];
  const u16* w2 = (const u16*)d_in[6];
  const u16* c = (const u16*)d_in[7];
  const u16* wmod = (const u16*)d_in[8];
  char* ws = (char*)d_ws;
  float* modf = (float*)(ws + OFF_MOD);
  u16* xn = (u16*)(ws + OFF_XN);
  u16* o = (u16*)(ws + OFF_O);
  u16* x1 = (u16*)(ws + OFF_X1);
  u16* h2 = (u16*)(ws + OFF_H2);
  u16* hh = (u16*)(ws + OFF_HH);
  float* part = (float*)(ws + OFF_PART);
  (void)hipFuncSetAttribute((const void*)k1_qkv_attn, hipFuncAttributeMaxDynamicSharedMemorySize, K1_LDS);
  (void)hipFuncSetAttribute((const void*)k2_wo, hipFuncAttributeMaxDynamicSharedMemorySize, K2_LDS);
  (void)hipFuncSetAttribute((const void*)k3_gemm1, hipFuncAttributeMaxDynamicSharedMemorySize, G1_LDS);
  (void)hipFuncSetAttribute((const void*)k4_gemm2, hipFuncAttributeMaxDynamicSharedMemorySize, G2_LDS);
  k0_mod_ln<<<236, 384, 0, stream>>>(x, c, wmod, modf, xn);
  k1_qkv_attn<<<256, 576, K1_LDS, stream>>>(xn, wqkv, modf, o);
  k2_wo<<<256, 384, K2_LDS, stream>>>(o, wo, x, modf, x1);
  k2b_ln2<<<64, 384, 0, stream>>>(x1, modf, h2);
  k3_gemm1<<<256, 512, G1_LDS, stream>>>(h2, w1, w3, hh);
  k4_gemm2<<<256, 512, G2_LDS, stream>>>(hh, w2, part);
  k5_reduce<<<(S * D / 4) / 256, 256, 0, stream>>>(part, x1, modf, (u16*)d_out);
}
```

```cpp
#include <hip/hip_runtime.h>
#include <hip/hip_bf16.h>
#include <stdint.h>

typedef unsigned short u16;
typedef short s16x4 __attribute__((ext_vector_type(4)));
typedef short s16x8 __attribute__((ext_vector_type(8)));
typedef float f32x2 __attribute__((ext_vector_type(2)));
typedef float f32x4 __attribute__((ext_vector_type(4)));
typedef float f32x16 __attribute__((ext_vector_type(16)));
typedef unsigned u32x2 __attribute__((ext_vector_type(2)));
typedef unsigned u32x4 __attribute__((ext_vector_type(4)));
typedef __bf16 bf16x2 __attribute__((ext_vector_type(2)));
#define LAS __attribute__((address_space(3)))
#define GAS __attribute__((address_space(1)))

constexpr int S = 2048, D = 576, F = 4608;
constexpr size_t OFF_MOD = 0;
constexpr size_t OFF_XN = 49152;
constexpr size_t OFF_O = OFF_XN + (size_t)S * D * 2;
constexpr size_t OFF_X1 = OFF_O + (size_t)S * D * 2;
constexpr size_t OFF_H2 = OFF_X1 + (size_t)S * D * 4;
constexpr size_t OFF_HH = OFF_H2 + (size_t)S * D * 2;
constexpr size_t OFF_PART = OFF_HH + (size_t)S * F * 2;
constexpr size_t OFF_WQ = OFF_PART + (size_t)4 * 512 * 5 * 1024;
constexpr size_t OFF_BIAS = OFF_WQ + (size_t)16 * 126 * 1024;
constexpr size_t OFF_WO = OFF_BIAS + (size_t)16 * 8192;
constexpr size_t OFF_XCH = OFF_WO + (size_t)36 * 18 * 1024;
constexpr size_t OFF_CNT = OFF_XCH + (size_t)64 * 4 * 64 * 8;
constexpr unsigned XCH_TAG = 0x5a17c3e1u;

__device__ __forceinline__ float bf_lo(unsigned v) { return __uint_as_float(v << 16); }
__device__ __forceinline__ float bf_hi(unsigned v) { return __uint_as_float(v & 0xffff0000u); }
__device__ __forceinline__ unsigned pk_bf16(float lo, float hi) {
  bf16x2 r; r.x = (__bf16)lo; r.y = (__bf16)hi;
  return __builtin_bit_cast(unsigned, r);
}
__device__ __forceinline__ float rb(float f) { return bf_lo(pk_bf16(f, 0.f)); }
template <int CTRL> __device__ __forceinline__ float dppf(float x) { return __int_as_float(__builtin_amdgcn_update_dpp(0, __float_as_int(x), CTRL, 0xf, 0xf, true)); }
__device__ __forceinline__ float sum16(float x) { auto r = __builtin_amdgcn_permlane16_swap(__float_as_uint(x), __float_as_uint(x), false, false); return __uint_as_float(r[0]) + __uint_as_float(r[1]); }
__device__ __forceinline__ float sum32(float x) { auto r = __builtin_amdgcn_permlane32_swap(__float_as_uint(x), __float_as_uint(x), false, false); return __uint_as_float(r[0]) + __uint_as_float(r[1]); }
__device__ __forceinline__ float max16(float x) { auto r = __builtin_amdgcn_permlane16_swap(__float_as_uint(x), __float_as_uint(x), false, false); return fmaxf(__uint_as_float(r[0]), __uint_as_float(r[1])); }
__device__ __forceinline__ float max32(float x) { auto r = __builtin_amdgcn_permlane32_swap(__float_as_uint(x), __float_as_uint(x), false, false); return fmaxf(__uint_as_float(r[0]), __uint_as_float(r[1])); }
__device__ __forceinline__ void rb2(float& a, float& b) { const unsigned p = pk_bf16(a, b); a = bf_lo(p); b = bf_hi(p); }
__device__ __forceinline__ float fast_silu(float g) { return g * __builtin_amdgcn_rcpf(1.f + __builtin_amdgcn_exp2f(g * -1.44269504f)); }

#define WAIT_V(n) asm volatile("s_waitcnt vmcnt(%0)" ::"n"(n) : "memory")
#define WAIT_L(n) asm volatile("s_waitcnt lgkmcnt(%0)" ::"n"(n) : "memory")
#define BAR() do { asm volatile("" ::: "memory"); __builtin_amdgcn_s_barrier(); asm volatile("" ::: "memory"); } while (0)
template <int IMM, class T> __device__ __forceinline__ void gload16(T& dst, unsigned voff, const void* sbase) {
  asm volatile("global_load_dwordx4 %0, %1, %2 offset:%3" : "=v"(dst) : "v"(voff), "s"(sbase), "n"(IMM) : "memory");
}
template <int IMM, class T> __device__ __forceinline__ void gload8(T& dst, unsigned voff, const void* sbase) {
  asm volatile("global_load_dwordx2 %0, %1, %2 offset:%3" : "=v"(dst) : "v"(voff), "s"(sbase), "n"(IMM) : "memory");
}
#define GLOAD16(dst, voff, sbase, imm) asm volatile("global_load_dwordx4 %0, %1, %2 offset:%3" : "=v"(dst) : "v"(voff), "s"(sbase), "n"(imm) : "memory")

__device__ __forceinline__ void glds16(const void* sbase, unsigned voff, unsigned ldsaddr) {
  asm volatile("s_mov_b32 m0, %2\n\ts_nop 0\n\tglobal_load_lds_dwordx4 %0, %1" :: "v"(voff), "s"(sbase), "s"(ldsaddr) : "memory");
}
__device__ __forceinline__ void glds16_sc1(const void* sbase, unsigned voff, unsigned ldsaddr) {
  asm volatile("s_mov_b32 m0, %2\n\ts_nop 0\n\tglobal_load_lds_dwordx4 %0, %1 sc1" :: "v"(voff), "s"(sbase), "s"(ldsaddr) : "memory");
}
#define SB0() __builtin_amdgcn_sched_barrier(0)
template <int I> struct IC { static constexpr int value = I; };
template <int B, int E, class Fn> __device__ __forceinline__ void static_for(Fn&& f) {
  if constexpr (B < E) { f(IC<B>{}); static_for<B + 1, E>(f); }
}
__device__ __forceinline__ void touch(const void* sbase, unsigned voff, unsigned ldsaddr) {
  asm volatile("s_mov_b32 m0, %2\n\ts_nop 0\n\tglobal_load_lds_dword %0, %1" :: "v"(voff), "s"(sbase), "s"(ldsaddr) : "memory");
}
__device__ __forceinline__ void pad2(u32x4& r) { asm volatile("s_nop 1" : "+v"(r)); }
template <class A, class B> __device__ __forceinline__ void MFMA16A(const A& a, const B& b, f32x4& c) {
  asm volatile("v_mfma_f32_16x16x32_bf16 %0, %1, %2, %0" : "+v"(c) : "v"(a), "v"(b));
}
#define STAMP_DECL
#define STAMP()
#define STAMP_OUT(kid)
__device__ __forceinline__ s16x4 tr_read(LAS char* p) { return __builtin_amdgcn_ds_read_tr16_b64_v4i16((LAS s16x4*)p); }
__device__ __forceinline__ s16x8 cat8(s16x4 a, s16x4 b) { return __builtin_shufflevector(a, b, 0, 1, 2, 3, 4, 5, 6, 7); }

__device__ __forceinline__ unsigned perm_lo(unsigned a, unsigned b) { return __builtin_amdgcn_perm(b, a, 0x05040100u); }
__device__ __forceinline__ unsigned perm_hi(unsigned a, unsigned b) { return __builtin_amdgcn_perm(b, a, 0x07060302u); }

constexpr int K0_A = 162, K0_B = K0_A + 128, K0_C = K0_B + 81, K0_D = K0_C + 27, K0_E0 = 400, K0_E = K0_E0 + 72;
__global__ __launch_bounds__(384) void k0_prep(const u16* __restrict__ x, const u16* __restrict__ c, const u16* __restrict__ wmod,
                                               const u16* __restrict__ wqkv, const u16* __restrict__ wo, char* __restrict__ ws,
                                               const u16* __restrict__ w1, const u16* __restrict__ w3) {
  __shared__ __attribute__((aligned(16))) float red[6 * 64];
  const int b = blockIdx.x, tid = threadIdx.x;
  const int w = tid >> 6, lane = tid & 63;
  STAMP_DECL; STAMP();
  if (b < K0_A) {
    const int kt = b / 54, cb = b - kt * 54;
    const int r0 = kt * 192 + (tid >> 3), col = cb * 64 + (tid & 7) * 8;
    u32x4 wv[4]; float sv[4];
#pragma unroll
    for (int i = 0; i < 4; ++i) wv[i] = *(const u32x4*)(wmod + (size_t)(r0 + 48 * i) * (6 * D) + col);
#pragma unroll
    for (int i = 0; i < 4; ++i) sv[i] = rb(fast_silu(bf_lo(c[r0 + 48 * i])));
    float a[8] = {0, 0, 0, 0, 0, 0, 0, 0};
#pragma unroll
    for (int i = 0; i < 4; ++i)
#pragma unroll
      for (int j = 0; j < 4; ++j) { a[2 * j] += sv[i] * bf_lo(wv[i][j]); a[2 * j + 1] += sv[i] * bf_hi(wv[i][j]); }
#pragma unroll
    for (int j = 0; j < 8; ++j) { a[j] += dppf<0x128>(a[j]); a[j] = sum32(sum16(a[j])); }
    if (lane < 8) {
      *(f32x4*)(red + w * 64 + lane * 8) = (f32x4){a[0], a[1], a[2], a[3]};
      *(f32x4*)(red + w * 64 + lane * 8 + 4) = (f32x4){a[4], a[5], a[6], a[7]};
    }
    __syncthreads();
    if (tid < 64) {
      float s = 0.f;
#pragma unroll
      for (int i = 0; i < 6; ++i) s += red[i * 64 + tid];
      ((float*)(ws + OFF_MOD))[kt * 3456 + cb * 64 + tid] = s;
    }
  } else if (b < K0_B) {
    const int i = lane & 15, g = lane >> 4;
    const int T = b - K0_A;
    const int row = 32 * (T >> 1) + 2 * i + (T & 1);
    float v[3][8];
#pragma unroll
    for (int s = 0; s < 3; ++s) {
      const u32x4 a = *(const u32x4*)(x + (size_t)row * D + ((w * 3 + s) * 4 + g) * 8);
#pragma unroll
      for (int j = 0; j < 4; ++j) { v[s][2 * j] = bf_lo(a[j]); v[s][2 * j + 1] = bf_hi(a[j]); }
    }
    float sm = 0.f;
#pragma unroll
    for (int s = 0; s < 3; ++s) sm += ((v[s][0] + v[s][1]) + (v[s][2] + v[s][3])) + ((v[s][4] + v[s][5]) + (v[s][6] + v[s][7]));
    sm = sum32(sum16(sm));
    if (g == 0) red[w * 16 + i] = sm;
    __syncthreads();
    float mean = 0.f;
#pragma unroll
    for (int k = 0; k < 6; ++k) mean += red[k * 16 + i];
    mean *= (1.f / D);
    float q0 = 0.f, q1 = 0.f;
#pragma unroll
    for (int s = 0; s < 3; ++s)
#pragma unroll
      for (int j = 0; j < 4; ++j) { const float d0 = v[s][2 * j] - mean, d1 = v[s][2 * j + 1] - mean; q0 += d0 * d0; q1 += d1 * d1; }
    const float q = sum32(sum16(q0 + q1));
    if (g == 0) red[96 + w * 16 + i] = q;
    const float mr = rb(mean);
#pragma unroll
    for (int s = 0; s < 3; ++s)
#pragma unroll
      for (int j = 0; j < 4; ++j) { v[s][2 * j] -= mr; v[s][2 * j + 1] -= mr; rb2(v[s][2 * j], v[s][2 * j + 1]); }
    __syncthreads();
    float var = 0.f;
#pragma unroll
    for (int k = 0; k < 6; ++k) var += red[96 + k * 16 + i];
    var *= (1.f / D);
    const float rs = 1.f / rb(sqrtf(rb(rb(var) + 1e-5f)));
#pragma unroll
    for (int s = 0; s < 3; ++s) {
      u32x4 o;
#pragma unroll
      for (int j = 0; j < 4; ++j) o[j] = pk_bf16(v[s][2 * j] * rs, v[s][2 * j + 1] * rs);
      *(u32x4*)(ws + OFF_XN + ((size_t)(T * 18 + w * 3 + s) * 64 + lane) * 16) = o;
    }
  } else if (b < K0_C) {
    const int task = (b - K0_B) * 384 + tid;
    const int kq = task / 432, nq4 = task - kq * 432;
    u32x2 r[8];
#pragma unroll
    for (int i = 0; i < 8; ++i) r[i] = *(const u32x2*)(wqkv + (size_t)(kq * 8 + i) * (3 * D) + nq4 * 4);
    const int part = nq4 / 144, nh = nq4 - part * 144, ks = kq >> 2, ko = kq & 3;
#pragma unroll
    for (int e = 0; e < 4; ++e) {
      const int n = nh * 4 + e, head = n / 18, d = n - head * 18, hp = head >> 1, h = head & 1;
      int tile, idx;
      if (d < 16) { tile = (part == 0) ? 5 + h : (part == 1 ? h : 3 + h); idx = d; }
      else { tile = 2; idx = (part == 2) ? 8 + 2 * h + (d - 16) : 4 * h + 2 * part + (d - 16); }
      char* dst = ws + OFF_WQ + ((hp * 18 + ks) * 7 + tile) * 1024 + (ko * 16 + idx) * 16;
      u32x4 f;
#pragma unroll
      for (int i = 0; i < 4; ++i) f[i] = (e & 1) ? perm_hi(r[2 * i][e >> 1], r[2 * i + 1][e >> 1]) : perm_lo(r[2 * i][e >> 1], r[2 * i + 1][e >> 1]);
      *(u32x4*)dst = f;
      if (part == 2 && d >= 16) *(u32x4*)(dst + 64) = (u32x4){0u, 0u, 0u, 0u};
    }
  } else if (b >= K0_E0) {
    const int ln = ((b - K0_E0) >> 3) * 384 + tid;
    const unsigned off = (ln / 18) * (F * 2) + ((b & 7) >> 1) * 2304 + (ln % 18) * 128;
    touch(w1, off, (unsigned)(size_t)(LAS float*)red); touch(w3, off, (unsigned)(size_t)(LAS float*)red);
  } else if (b < K0_D) {
    const int task = (b - K0_C) * 384 + tid;
    const int nh = task % 144, ug = task / 144, u = ug >> 2, g = ug & 3;
    u32x2 r[8];
#pragma unroll
    for (int j = 0; j < 8; ++j) {
      const int krow = (u < 16) ? 36 * u + 18 * (j >> 2) + 4 * g + (j & 3) : 36 * (8 * (u - 16) + 2 * g + (j >> 2)) + 18 * ((j >> 1) & 1) + 16 + (j & 1);
      r[j] = *(const u32x2*)(wo + (size_t)krow * D + nh * 4);
    }
    char* dst = ws + OFF_WO + ((size_t)((nh >> 2) * 18 + u) * 64 + g * 16 + (nh & 3) * 4) * 16;
#pragma unroll
    for (int e = 0; e < 4; ++e) {
      u32x4 f;
#pragma unroll
      for (int i = 0; i < 4; ++i) f[i] = (e & 1) ? perm_hi(r[2 * i][e >> 1], r[2 * i + 1][e >> 1]) : perm_lo(r[2 * i][e >> 1], r[2 * i + 1][e >> 1]);
      *(u32x4*)(dst + e * 16) = f;
    }
    if (task < 64 * 4 * 64 / 2 + 1024) *(u32x4*)(ws + OFF_XCH + (size_t)task * 16) = (u32x4){0u, 0u, 0u, 0u};
  }
  STAMP();
}

constexpr int K1_W = 126 * 1024;
constexpr int K1_CONST = K1_W;
constexpr int K1_C2 = K1_CONST + 4608;
constexpr int K1_ST = K1_C2 + 1792;
constexpr int K1_FIN = K1_ST + 2304;
constexpr int K1_SCR = K1_FIN + 256;
constexpr int K1_LDS = K1_SCR + 256;
constexpr int K1_EK = 0, K1_EV = 9 * 2 * 1024, K1_EL = K1_EV + 9 * 2 * 512;
constexpr int K1_O = 36 * 1024;
constexpr int k1_r(int p) { return p < 0 ? 0 : (21 * (p + 1) + 8) / 9; }
constexpr int k1_cum(int p) { return 6 + 3 * (p + 1 < 5 ? p + 1 : 5) + k1_r(p); }
constexpr int K1_NEARLY = 14, K1_NOPS = 35 + K1_NEARLY;
constexpr int k1_op(int n) {
  if (n < 3) return 96;
  if (n < 6) return n - 3;
  for (int p = 0; p < 6; ++p) {
    const int lo = p ? k1_cum(p - 1) : 6;
    if (n < k1_cum(p)) { const int k = n - lo; return (p < 5 && k < 3) ? 3 * p + 3 + k : 32 + k1_r(p - 1) + k - (p < 5 ? 3 : 0); }
  }
  return 64 + n - 35;
}
constexpr int k1_pos(int n) {
  if (n < k1_cum(1)) return -1;
  for (int p = 2; p < 6; ++p) if (n < k1_cum(p)) return 3 * (p - 2) + (n - k1_cum(p - 1)) / 2;
  return 11 + (n - 35) / 2;
}
constexpr int k1_before(int s) { int c = 0; for (int n = 0; n < K1_NOPS; ++n) if (k1_pos(n) < s) ++c; return c; }

struct Cst { f32x4 s0, s1, g0, g1; };
__device__ __forceinline__ void rdc(Cst& c, const LAS float* cs) {
  c.s0 = *(const LAS f32x4*)cs; c.s1 = *(const LAS f32x4*)(cs + 4); c.g0 = *(const LAS f32x4*)(cs + 576); c.g1 = *(const LAS f32x4*)(cs + 580);
}
__device__ __forceinline__ void modc(u32x4& xv, const Cst& c) {
  asm volatile("" : "+v"(xv));
  const u32x4 v = xv;
  u32x4 r;
  r[0] = pk_bf16(bf_lo(v[0]) * c.g0[0] + c.s0[0], bf_hi(v[0]) * c.g0[1] + c.s0[1]);
  r[1] = pk_bf16(bf_lo(v[1]) * c.g0[2] + c.s0[2], bf_hi(v[1]) * c.g0[3] + c.s0[3]);
  r[2] = pk_bf16(bf_lo(v[2]) * c.g1[0] + c.s1[0], bf_hi(v[2]) * c.g1[1] + c.s1[1]);
  r[3] = pk_bf16(bf_lo(v[3]) * c.g1[2] + c.s1[2], bf_hi(v[3]) * c.g1[3] + c.s1[3]);
  pad2(r);
  xv = r;
}
template <int N> __device__ __forceinline__ void wait3(u32x4& a, u32x4& b, u32x4& c) {
  asm volatile("s_waitcnt vmcnt(%3)" : "+v"(a), "+v"(b), "+v"(c) : "n"(N) : "memory");
}
__device__ __forceinline__ void pin(u32x4& a) { asm volatile("" : "+v"(a)); }

__global__ __launch_bounds__(576) void k12_attn(char* __restrict__ ws, const u16* __restrict__ x) {
  extern __shared__ __attribute__((aligned(16))) char smem[];
  LAS char* lds = (LAS char*)smem;
  const unsigned lbase = (unsigned)(size_t)lds;
  const int b = blockIdx.x, xcd = b & 7, idx = b >> 3;
  const int rt = (xcd & 3) * 4 + (idx & 3), hp = (xcd >> 2) * 8 + (idx >> 2);
  const int tid = threadIdx.x, lane = tid & 63;
  const int w = __builtin_amdgcn_readfirstlane(tid >> 6);
  const int li = lane & 15, g = lane >> 4;
  const bool halo = (w == 8);
  STAMP_DECL; STAMP();
  const unsigned l16 = lane * 16;
  const int tb = halo ? (rt > 0 ? 8 * rt - 2 : 0) : 8 * rt + w;
  const char* xsrc = ws + OFF_XN + (size_t)tb * (18 * 1024);
  const unsigned xvo = halo ? (unsigned)((li >> 3) * (18 * 1024) + (g * 16 + 8 + (li & 7)) * 16) : l16;
  const char* wsrc = ws + OFF_WQ + (size_t)hp * K1_W + w * 1024;
  const int rt2 = 4 * rt + (hp >> 2), nq = hp & 3;
  const int nt = nq * 9 + w;
  const char* wsrc2 = ws + OFF_WO + (size_t)nt * (18 * 1024);
  u32x4 xf[18], wo[18];
  auto issue = [&](auto N) {
    constexpr int op = k1_op(decltype(N)::value);
    if constexpr (op < 32) { const char* q = xsrc + (op & ~3) * 1024; gload16<(op & 3) * 1024>(xf[op], xvo, q); }
    else if constexpr (op < 64) glds16(wsrc + (op - 32) * 9216, l16, lbase + (w + 9 * (op - 32)) * 1024);
    else { constexpr int i = op - 64; const char* q = wsrc2 + (i & ~3) * 1024; gload16<(i & 3) * 1024>(wo[i], l16, q); }
  };
  f32x2 mp[3];
  {
    const unsigned mo = tid * 8;
    const char* m0 = ws + OFF_MOD; const char* m1 = m0 + 3456 * 4; const char* m2 = m1 + 3456 * 4;
    asm volatile("global_load_dwordx2 %0, %1, %2" : "=v"(mp[0]) : "v"(mo), "s"(m0) : "memory");
    asm volatile("global_load_dwordx2 %0, %1, %2" : "=v"(mp[1]) : "v"(mo), "s"(m1) : "memory");
    asm volatile("global_load_dwordx2 %0, %1, %2" : "=v"(mp[2]) : "v"(mo), "s"(m2) : "memory");
  }
  static_for<3, k1_cum(0)>(issue);
  asm volatile("s_waitcnt vmcnt(%3)" : "+v"(mp[0]), "+v"(mp[1]), "+v"(mp[2]) : "n"(k1_cum(0) - 3) : "memory");
  {
    float m0 = (mp[0][0] + mp[1][0]) + mp[2][0], m1 = (mp[0][1] + mp[1][1]) + mp[2][1];
    rb2(m0, m1);
    if (tid >= 288) { m0 += 1.f; m1 += 1.f; rb2(m0, m1); }
    *(LAS f32x2*)(lds + K1_CONST + tid * 8) = (f32x2){m0, m1};
    WAIT_L(0);
  }
  BAR();
  const LAS float* cs = (const LAS float*)(lds + K1_CONST) + g * 8;
  Cst cst;
  {
    Cst c1, c2;
    rdc(cst, cs); rdc(c1, cs + 32); rdc(c2, cs + 64);
    wait3<k1_cum(0) - 6>(xf[0], xf[1], xf[2]);
    modc(xf[0], cst); modc(xf[1], c1); modc(xf[2], c2);
  }
  rdc(cst, cs + 96);
  static_for<k1_cum(0), k1_cum(1)>(issue);
  f32x4 acc[7];
#pragma unroll
  for (int t = 0; t < 7; ++t) { acc[t] = (f32x4){0.f, 0.f, 0.f, 0.f}; asm volatile("" : "+v"(acc[t])); }
  unsigned wa = lbase + l16;
  s16x8 wf[7];
  auto rdw = [&](auto Sx, auto T0, auto T1) {
    constexpr int s = decltype(Sx)::value, off = (s % 6) * 7168;
#pragma unroll
    for (int t = decltype(T0)::value; t < decltype(T1)::value; ++t) wf[t] = *(const LAS s16x8*)(size_t)(wa + off + t * 1024);
  };
  wait3<k1_cum(1) - k1_cum(0)>(xf[3], xf[4], xf[5]);
  BAR();
  STAMP();
  rdw(IC<0>{}, IC<0>{}, IC<7>{});
  static_for<0, 18>([&](auto Sx) {
    constexpr int s = decltype(Sx)::value, p = s / 3;
    if constexpr (s % 3 == 2 && p < 5) {
      if constexpr (p < 4) wait3<k1_before(s) - k1_cum(p + 1)>(xf[3 * p + 6], xf[3 * p + 7], xf[3 * p + 8]);
      else WAIT_V(k1_before(s) - k1_cum(5));
      BAR();
      STAMP();
    }
    if constexpr (s == 5 || s == 11) { wa += 6 * 7168; asm volatile("" : "+v"(wa)); }
    MFMA16A(wf[0], xf[s], acc[0]); MFMA16A(wf[1], xf[s], acc[1]); MFMA16A(wf[2], xf[s], acc[2]); MFMA16A(xf[s], wf[3], acc[3]);
    if constexpr (s < 17) rdw(IC<s + 1>{}, IC<0>{}, IC<4>{});
    if constexpr (s + 3 < 18) modc(xf[s + 3], cst);
    if constexpr (s + 4 < 18) rdc(cst, cs + (s + 4) * 32);
    MFMA16A(xf[s], wf[4], acc[4]);
    if (!halo) { MFMA16A(wf[5], xf[s], acc[5]); MFMA16A(wf[6], xf[s], acc[6]); }
    if constexpr (s < 17) rdw(IC<s + 1>{}, IC<4>{}, IC<7>{});
    static_for<k1_before(s), k1_before(s + 1)>(issue);
  });
  STAMP();
  const int col = nt * 16 + 4 * g;
  u32x2 xr[2]; f32x4 mq[3];
  auto issue_p2 = [&]() {
    static_for<K1_NEARLY, 18>([&](auto I) { constexpr int i = decltype(I)::value; const char* q = wsrc2 + (i & ~3) * 1024; gload16<(i & 3) * 1024>(wo[i], l16, q); });
    const unsigned v0 = ((rt2 * 32 + 2 * li) * D + col) * 2;
    gload8<0>(xr[0], v0, x); gload8<1152>(xr[1], v0, x);
    const int t = (tid & 127) < 108 ? (tid & 127) : 107, vec = t / 36, c4 = t - vec * 36;
    const unsigned vm = ((2 + vec) * D + nq * 144 + c4 * 4) * 4;
    const char* m0 = ws + OFF_MOD; const char* m1 = m0 + 3456 * 4; const char* m2 = m1 + 3456 * 4;
    gload16<0>(mq[0], vm, m0); gload16<0>(mq[1], vm, m1); gload16<0>(mq[2], vm, m2);
  };
  asm volatile("s_nop 7" : "+v"(acc[0]), "+v"(acc[1]), "+v"(acc[2]), "+v"(acc[3]), "+v"(acc[4]), "+v"(acc[5]), "+v"(acc[6]) :: "memory");
  unsigned kf[2][2], qf[2][2], vf[2][2], lf[2];
#pragma unroll
  for (int h = 0; h < 2; ++h) {
    kf[h][0] = pk_bf16(acc[h][0], acc[h][1]); kf[h][1] = pk_bf16(acc[h][2], acc[h][3]);
    qf[h][0] = pk_bf16(acc[5 + h][0], acc[5 + h][1]); qf[h][1] = pk_bf16(acc[5 + h][2], acc[5 + h][3]);
    vf[h][0] = pk_bf16(acc[3 + h][0], acc[3 + h][1]); vf[h][1] = pk_bf16(acc[3 + h][2], acc[3 + h][3]);
  }
  lf[0] = pk_bf16(acc[2][0], acc[2][1]); lf[1] = pk_bf16(acc[2][2], acc[2][3]);
#pragma unroll
  for (int h = 0; h < 2; ++h) {
    *(LAS u32x4*)(lds + K1_EK + (w * 2 + h) * 1024 + l16) = (u32x4){kf[h][0], kf[h][1], lf[1], 0u};
    *(LAS u32x2*)(lds + K1_EV + (w * 2 + h) * 512 + lane * 8) = (u32x2){vf[h][0], vf[h][1]};
  }
  if (g == 2) *(LAS u32x2*)(lds + K1_EL + (w * 16 + li) * 8) = (u32x2){lf[0], lf[1]};
  WAIT_L(0);
  BAR();
  STAMP();
  issue_p2();
  if (!halo) {
    const int par = w & 1;
    const bool first = (w < 2);
    const int pw = first ? 8 : w - 2;
    const int pki = first ? 8 * par + (li & 7) : li;
    const int pvg = first ? 2 * par + (g & 1) : g;
    const int q4 = li >> 2;
    const int plk = first ? 8 * par + ((4 * g + q4) & 7) : 4 * g + q4;
    const s16x8 vl = cat8(tr_read(lds + K1_EL + (pw * 16 + plk) * 8), tr_read(lds + K1_EL + (w * 16 + 4 * g + q4) * 8));
    const bool prev_ok = !(first && rt == 0);
    unsigned op[2][2], ol[2];
    bool usep[4], useo[4];
#pragma unroll
    for (int j = 0; j < 4; ++j) { const int kk = 4 * g + j; usep[j] = prev_ok && (kk >= li + 9); useo[j] = (kk <= li) && (li - kk <= 7); }
    const unsigned mp01 = (usep[0] ? 0xffffu : 0u) | (usep[1] ? 0xffff0000u : 0u), mp23 = (usep[2] ? 0xffffu : 0u) | (usep[3] ? 0xffff0000u : 0u);
#pragma unroll
    for (int h = 0; h < 2; ++h) {
      const u32x4 kp = *(const LAS u32x4*)(lds + K1_EK + (pw * 2 + h) * 1024 + (g * 16 + pki) * 16);
      const u32x2 vp = *(const LAS u32x2*)(lds + K1_EV + (pw * 2 + h) * 512 + (pvg * 16 + li) * 8);
      const u32x4 ko = (u32x4){kf[h][0], kf[h][1], lf[1], 0u};
      const u32x4 qq = (u32x4){qf[h][0], qf[h][1], (g == h) ? lf[0] : 0u, 0u};
      const f32x4 z = (f32x4){0.f, 0.f, 0.f, 0.f};
      const f32x4 so = __builtin_amdgcn_mfma_f32_16x16x32_bf16(__builtin_bit_cast(s16x8, ko), __builtin_bit_cast(s16x8, qq), z, 0, 0, 0);
      const f32x4 sp = __builtin_amdgcn_mfma_f32_16x16x32_bf16(__builtin_bit_cast(s16x8, kp), __builtin_bit_cast(s16x8, qq), z, 0, 0, 0);
      float lg[4];
#pragma unroll
      for (int j = 0; j < 4; ++j) lg[j] = usep[j] ? sp[j] : so[j];
      rb2(lg[0], lg[1]); rb2(lg[2], lg[3]);
#pragma unroll
      for (int j = 0; j < 4; ++j) lg[j] *= 0.23570226f;
      rb2(lg[0], lg[1]); rb2(lg[2], lg[3]);
#pragma unroll
      for (int j = 0; j < 4; ++j) lg[j] = (usep[j] || useo[j]) ? lg[j] : -3.0e38f;
      const float mx = max32(max16(fmaxf(fmaxf(lg[0], lg[1]), fmaxf(lg[2], lg[3]))));
#pragma unroll
      for (int j = 0; j < 4; ++j) lg[j] = __builtin_amdgcn_exp2f((lg[j] - mx) * 1.44269504f);
      const float inv = __builtin_amdgcn_rcpf(sum32(sum16((lg[0] + lg[1]) + (lg[2] + lg[3]))));
      const unsigned p01 = pk_bf16(lg[0] * inv, lg[1] * inv), p23 = pk_bf16(lg[2] * inv, lg[3] * inv);
      const u32x4 pf = (u32x4){p01 & mp01, p23 & mp23, p01 & ~mp01, p23 & ~mp23};
      const u32x4 vv = (u32x4){vp[0], vp[1], vf[h][0], vf[h][1]};
      const f32x4 oo = __builtin_amdgcn_mfma_f32_16x16x32_bf16(__builtin_bit_cast(s16x8, vv), __builtin_bit_cast(s16x8, pf), z, 0, 0, 0);
      const f32x4 lo = __builtin_amdgcn_mfma_f32_16x16x32_bf16(vl, __builtin_bit_cast(s16x8, pf), z, 0, 0, 0);
      op[h][0] = pk_bf16(oo[0], oo[1]); op[h][1] = pk_bf16(oo[2], oo[3]);
      ol[h] = pk_bf16(lo[2 * h], lo[2 * h + 1]);
    }
    const char* ob = ws + OFF_O + (size_t)(8 * rt + w) * (18 * 1024);
    const u32x4 o4 = (u32x4){op[0][0], op[0][1], op[1][0], op[1][1]};
    const unsigned oo0 = hp * 1024 + l16;
    asm volatile("global_store_dwordx4 %0, %1, %2 sc1\n\ts_nop 1" :: "v"(oo0), "v"(o4), "s"(ob) : "memory");
    if (g == 0) {
      const u32x2 o2 = (u32x2){ol[0], ol[1]};
      const unsigned oo1 = (16 + (hp >> 3)) * 1024 + (((hp & 7) >> 1) * 16 + li) * 16 + 8 * (hp & 1);
      asm volatile("global_store_dwordx2 %0, %1, %2 sc1\n\ts_nop 1" :: "v"(oo1), "v"(o2), "s"(ob) : "memory");
    }
  }
  STAMP();
  asm volatile("s_waitcnt vmcnt(0)" : "+v"(xr[0]), "+v"(xr[1]), "+v"(mq[0]), "+v"(mq[1]), "+v"(mq[2]) :: "memory");
  static_for<0, 18>([&](auto I) { pin(wo[decltype(I)::value]); });
  BAR();
  GAS unsigned* cnt = (GAS unsigned*)(ws + OFF_CNT) + rt * 64;
  if (tid == 512) __hip_atomic_fetch_add(cnt, 1u, __ATOMIC_RELAXED, __HIP_MEMORY_SCOPE_AGENT);
  if (tid < 108) {
    float t[4];
#pragma unroll
    for (int j = 0; j < 4; ++j) t[j] = (mq[0][j] + mq[1][j]) + mq[2][j];
    rb2(t[0], t[1]); rb2(t[2], t[3]);
    if (tid >= 72) {
#pragma unroll
      for (int j = 0; j < 4; ++j) t[j] += 1.f;
      rb2(t[0], t[1]); rb2(t[2], t[3]);
    }
    *(LAS f32x4*)(lds + K1_C2 + tid * 16) = (f32x4){t[0], t[1], t[2], t[3]};
  }
  if (halo) {
    for (unsigned spin = 0; spin < (1u << 16); ++spin) {
      if (__builtin_amdgcn_readfirstlane(__hip_atomic_load(cnt, __ATOMIC_RELAXED, __HIP_MEMORY_SCOPE_AGENT)) >= 16u) break;
      __builtin_amdgcn_s_sleep(1);
    }
  }
  BAR();
  STAMP();
  {
    const char* osrc = ws + OFF_O + (size_t)rt2 * (36 * 1024) + w * 4096;
#pragma unroll
    for (int i = 0; i < 4; ++i) glds16_sc1(osrc + i * 1024, l16, lbase + K1_O + w * 4096 + i * 1024);
  }
  asm volatile("s_waitcnt vmcnt(0) lgkmcnt(0)" ::: "memory");
  BAR();
  STAMP();
  f32x4 ac2[2];
  ac2[0] = (f32x4){0.f, 0.f, 0.f, 0.f}; ac2[1] = ac2[0];
  {
    unsigned oa = lbase + K1_O + l16;
    asm volatile("" : "+v"(oa));
#pragma unroll
    for (int s = 0; s < 18; ++s) {
      const s16x8 x0 = *(const LAS s16x8*)(size_t)(oa + s * 1024), x1f = *(const LAS s16x8*)(size_t)(oa + (18 + s) * 1024);
      ac2[0] = __builtin_amdgcn_mfma_f32_16x16x32_bf16(__builtin_bit_cast(s16x8, wo[s]), x0, ac2[0], 0, 0, 0);
      ac2[1] = __builtin_amdgcn_mfma_f32_16x16x32_bf16(__builtin_bit_cast(s16x8, wo[s]), x1f, ac2[1], 0, 0, 0);
    }
  }
  const f32x4 ga = *(const LAS f32x4*)(lds + K1_C2 + (w * 16 + 4 * g) * 4);
  const f32x4 sb = *(const LAS f32x4*)(lds + K1_C2 + (144 + w * 16 + 4 * g) * 4);
  const f32x4 g2 = *(const LAS f32x4*)(lds + K1_C2 + (288 + w * 16 + 4 * g) * 4);
  float v[2][4];
  f32x2 sq[2];
  LAS f32x2* st = (LAS f32x2*)(lds + K1_ST);
  LAS f32x2* fin = (LAS f32x2*)(lds + K1_FIN);
#pragma unroll
  for (int m = 0; m < 2; ++m) {
    const float xv[4] = {bf_lo(xr[m][0]), bf_hi(xr[m][0]), bf_lo(xr[m][1]), bf_hi(xr[m][1])};
#pragma unroll
    for (int j = 0; j < 4; ++j) v[m][j] = ac2[m][j];
    rb2(v[m][0], v[m][1]); rb2(v[m][2], v[m][3]);
#pragma unroll
    for (int j = 0; j < 4; ++j) v[m][j] *= ga[j];
    rb2(v[m][0], v[m][1]); rb2(v[m][2], v[m][3]);
    const unsigned p0 = pk_bf16(v[m][0] + xv[0], v[m][1] + xv[1]), p1 = pk_bf16(v[m][2] + xv[2], v[m][3] + xv[3]);
    v[m][0] = bf_lo(p0); v[m][1] = bf_hi(p0); v[m][2] = bf_lo(p1); v[m][3] = bf_hi(p1);
    const float mw = sum32(sum16((v[m][0] + v[m][1]) + (v[m][2] + v[m][3]))) * (1.f / 16.f);
    const float d0 = v[m][0] - mw, d1 = v[m][1] - mw, d2 = v[m][2] - mw, d3 = v[m][3] - mw;
    const float q = sum32(sum16((d0 * d0 + d1 * d1) + (d2 * d2 + d3 * d3)));
    sq[m] = (f32x2){mw, q};
    xr[m] = (u32x2){p0, p1};
  }
  if (g < 2) st[w * 32 + g * 16 + li] = g ? sq[1] : sq[0];
  WAIT_L(0);
  BAR();
  STAMP();
  if (w == 0) {
    const int r = lane & 31;
    float mt_ = 0.f;
    f32x2 sv[9];
#pragma unroll
    for (int k = 0; k < 9; ++k) { sv[k] = st[k * 32 + r]; mt_ += sv[k][0]; }
    mt_ *= (1.f / 9.f);
    float m2 = 0.f;
#pragma unroll
    for (int k = 0; k < 9; ++k) { const float dl = sv[k][0] - mt_; m2 += sv[k][1] + 16.f * dl * dl; }
    GAS unsigned long long* xch = (GAS unsigned long long*)(ws + OFF_XCH) + (size_t)rt2 * 256;
    const float mine = (lane < 32) ? mt_ : m2;
    __hip_atomic_store(xch + nq * 64 + lane, ((unsigned long long)XCH_TAG << 32) | __float_as_uint(mine), __ATOMIC_RELAXED, __HIP_MEMORY_SCOPE_AGENT);
    float o[4];
    for (unsigned spin = 0; spin < (1u << 16); ++spin) {
      bool ok = true;
#pragma unroll
      for (int k = 0; k < 4; ++k) {
        const unsigned long long gv = __hip_atomic_load(xch + k * 64 + lane, __ATOMIC_RELAXED, __HIP_MEMORY_SCOPE_AGENT);
        o[k] = __uint_as_float((unsigned)gv); ok &= ((unsigned)(gv >> 32) == XCH_TAG);
      }
      if (__all(ok)) break;
      __builtin_amdgcn_s_sleep(1);
    }
    const float mean = 0.25f * ((o[0] + o[1]) + (o[2] + o[3]));
    float t = 0.f;
#pragma unroll
    for (int k = 0; k < 4; ++k) { const float dl = o[k] - mean; t += 144.f * dl * dl; }
    const auto sw = __builtin_amdgcn_permlane32_swap(__float_as_uint(mean), __float_as_uint(mean), false, false);
    const float m2s = 4.f * __uint_as_float(sw[1]);
    if (lane < 32) {
      const float var = (t + m2s) * (1.f / D);
      fin[r] = (f32x2){rb(mean), 1.f / rb(sqrtf(rb(rb(var) + 1e-5f)))};
    }
  }
#pragma unroll
  for (int m = 0; m < 2; ++m) *(u32x2*)(ws + OFF_X1 + ((size_t)((rt2 * 2 + m) * 36 + nt) * 64 + lane) * 8) = xr[m];
  WAIT_L(0);
  BAR();
  STAMP();
#pragma unroll
  for (int m = 0; m < 2; ++m) {
    const f32x2 f = fin[m * 16 + li];
    float h[4];
#pragma unroll
    for (int j = 0; j < 4; ++j) h[j] = v[m][j] - f[0];
    rb2(h[0], h[1]); rb2(h[2], h[3]);
#pragma unroll
    for (int j = 0; j < 4; ++j) h[j] *= f[1];
    rb2(h[0], h[1]); rb2(h[2], h[3]);
#pragma unroll
    for (int j = 0; j < 4; ++j) h[j] *= g2[j];
    rb2(h[0], h[1]); rb2(h[2], h[3]);
#pragma unroll
    for (int j = 0; j < 4; ++j) h[j] += sb[j];
    *(u32x2*)(ws + OFF_H2 + ((size_t)(rt2 * 36 + nt) * 64 + (g >> 1) * 32 + m * 16 + li) * 16 + (g & 1) * 8) = (u32x2){pk_bf16(h[0], h[1]), pk_bf16(h[2], h[3])};
  }
  STAMP_OUT(1);
}

constexpr int G1_W3 = 64 * 288 + 128;
constexpr int G1_STAGE = 2 * G1_W3;
constexpr int G1_LDS = 4 * G1_STAGE + 256;
#define G1_WAITX(n, xf) asm volatile("s_waitcnt vmcnt(%4)" : "+v"(xf[0]), "+v"(xf[1]), "+v"(xf[2]), "+v"(xf[3]) : "n"(n) : "memory")
__global__ __launch_bounds__(512, 2) void k3_gemm1(const u16* __restrict__ h2, const u16* __restrict__ w1, const u16* __restrict__ w3,
                                                   u16* __restrict__ hh, const u16* __restrict__ w2) {
  extern __shared__ __attribute__((aligned(16))) char smem[];
  LAS char* lds = (LAS char*)smem;
  const int b = blockIdx.x, xcd = b & 7, idx = b >> 3;
  const int mt = (xcd & 1) * 4 + (idx & 3), ct = (xcd >> 1) * 8 + (idx >> 2);
  const int tid = threadIdx.x, lane = tid & 63;
  const int w = __builtin_amdgcn_readfirstlane(tid >> 6);
  STAMP_DECL; STAMP();
  const char* wsrc = (const char*)((w < 4) ? w1 : w3) + (size_t)((w & 3) * 16) * (F * 2) + ct * 288;
  unsigned so[5];
#pragma unroll
  for (int i = 0; i < 5; ++i) {
    const int ci = i * 64 + lane;
    const int r = ci / 18, ch = ci - r * 18;
    so[i] = r * (F * 2) + ch * 16;
  }
  const unsigned wdst = (unsigned)(size_t)lds + (w >> 2) * G1_W3 + (w & 3) * 4608;
#define G1_G(st, i) do { const char* _s = wsrc + (size_t)(st) * (64 * F * 2); const unsigned _d = wdst + ((st) & 3) * G1_STAGE; \
    if ((i) < 4) glds16(_s, so[i], _d + (i) * 1024); else if (lane < 32) glds16(_s, so[4], _d + 4096); } while (0)
  const char* xsrc = (const char*)h2 + (size_t)(mt * 8 + w) * (36 * 1024);
  const unsigned xvo = lane * 16;
  u32x4 xa[4], xb[4], xc[4];
#define G1_X(dst, st, i) do { const char* _p = xsrc + (st) * 4096; GLOAD16(dst[i], xvo, _p, (i) * 1024); } while (0)
#pragma unroll
  for (int i = 0; i < 5; ++i) G1_G(0, i);
#pragma unroll
  for (int i = 0; i < 4; ++i) G1_X(xa, 0, i);
  BAR();
#pragma unroll
  for (int i = 0; i < 5; ++i) G1_G(1, i);
  const int grp = lane >> 4, q = (lane & 15) >> 2, p4 = lane & 3;
  const unsigned ta = (grp & 1) * G1_W3 + (8 * (grp >> 1) + q) * 288 + (((p4 & 1) << 4) | ((p4 >> 1) << 3));
  f32x16 acc[9];
#pragma unroll
  for (int t = 0; t < 9; ++t)
    for (int j = 0; j < 16; ++j) acc[t][j] = 0.f;
  s16x8 wf[4];
#define G1_RDKT(b, ks, t) cat8(tr_read((b) + (16 * (ks)) * 288 + (t) * 32), tr_read((b) + (16 * (ks) + 4) * 288 + (t) * 32))
#define G1_RD(b, i) G1_RDKT(b, (i) / 9, (i) % 9)
#define G1_STEP(xf, xn, st) do { LAS char* _b = lds + ((st) & 3) * G1_STAGE + ta; LAS char* _n = lds + (((st) + 1) & 3) * G1_STAGE + ta; \
    if ((st) <= 1) { wf[0] = G1_RD(_b, 0); wf[1] = G1_RD(_b, 1); wf[2] = G1_RD(_b, 2); } \
    _Pragma("unroll") for (int i = 0; i < 36; ++i) { \
      if (i + 3 < 36) wf[(i + 3) & 3] = G1_RD(_b, i + 3); \
      else if ((st) >= 1 && (st) < 6) wf[(i + 3) & 3] = G1_RD(_n, i + 3 - 36); \
      else if ((st) == 6) wf[(i + 3) & 3] = G1_RDKT(_n, i + 3 - 36, 0); \
      acc[i % 9] = __builtin_amdgcn_mfma_f32_32x32x16_bf16(wf[i & 3], __builtin_bit_cast(s16x8, xf[i / 9]), acc[i % 9], 0, 0, 0); \
      if ((st) == 0) { if ((i & 1) == 1 && (i >> 1) < 14) { const int v = i >> 1; \
        if (v < 4) G1_X(xn, 1, (v) & 3); else if (v < 9) G1_G(2, (v < 4) ? 0 : (v - 4) % 5); else G1_G(3, (v < 9) ? 0 : (v - 9) % 5); } } \
      else if ((i & 3) == 1) { const int v = i >> 2; \
        if (v < 4) G1_X(xn, (st) + 1, (v) & 3); \
        else if ((st) + 3 < 9) G1_G((st) + 3, (v - 4 < 0) ? 0 : v - 4); \
        else if ((st) == 6 && v < 8) G1_X(xc, 8, (v) & 3); } \
      SB0(); } } while (0)
  G1_WAITX(5, xa); BAR(); STAMP(); G1_STEP(xa, xb, 0);
  G1_WAITX(5, xb); BAR(); STAMP(); G1_STEP(xb, xa, 1);
  G1_WAITX(5, xa); BAR(); STAMP(); G1_STEP(xa, xb, 2);
  G1_WAITX(5, xb); BAR(); STAMP(); G1_STEP(xb, xa, 3);
  G1_WAITX(5, xa); BAR(); STAMP(); G1_STEP(xa, xb, 4);
  G1_WAITX(5, xb); BAR(); STAMP(); G1_STEP(xb, xa, 5);
  G1_WAITX(5, xa); BAR(); STAMP(); G1_STEP(xa, xb, 6);
  G1_WAITX(0, xb); G1_WAITX(0, xc); BAR(); STAMP();
  if (tid < 324) touch(w2, ((xcd >> 1) * 10368 + idx * 324 + tid) * 128, (unsigned)(size_t)lds + 4 * G1_STAGE);
  {
    LAS char* b7 = lds + 3 * G1_STAGE + ta; LAS char* b8 = lds + ta;
    const int m = lane & 31, h = lane >> 5;
    const int row = mt * 256 + w * 32 + m;
    const int oct0 = ct * 18 + h;
    u16* dbase = hh + ((size_t)((row >> 4) * 144) * 64 + (row & 15)) * 8;
#define G1_RT(i) (((i) & 7) < 4 ? G1_RDKT(b7, (i) & 3, (i) >> 3) : G1_RDKT(b8, (i) & 3, (i) >> 3))
    float e[8];
#pragma unroll
    for (int i = 0; i < 80; ++i) {
      const int t = i >> 3, j = i & 7;
      if (i + 3 < 72) wf[(i + 3) & 3] = G1_RT(i + 3);
      if (i < 72) {
        const u32x4 xx = (j < 4) ? xb[j & 3] : xc[j & 3];
        acc[t] = __builtin_amdgcn_mfma_f32_32x32x16_bf16(wf[i & 3], __builtin_bit_cast(s16x8, xx), acc[t], 0, 0, 0);
      }
      if (t >= 1) {
        e[j] = fast_silu(acc[t - 1][j]) * acc[t - 1][8 + j];
        if (j == 7) {
          const int oct = oct0 + 2 * (t - 1);
          u16* dst = dbase + ((oct >> 2) * 64 + (oct & 3) * 16) * 8;
          *(u32x4*)dst = (u32x4){pk_bf16(e[0], e[1]), pk_bf16(e[2], e[3]), pk_bf16(e[4], e[5]), pk_bf16(e[6], e[7])};
        }
      }
      SB0();
    }
  }
  STAMP();
  WAIT_V(0);
  STAMP();
  STAMP_OUT(3);
}

constexpr int G2_STAGE = 128 * 288;
constexpr int G2_LDS = 4 * G2_STAGE;
constexpr int g2_pos(int n) {
  const int s = n / 9, k = n % 9;
  if (s == 0) return -1;
  if (s == 1) return k < 5 ? -1 : k - 5;
  if (s == 2) return 4 + k + k / 2;
  return 18 * (s - 2) + 2 * k;
}
constexpr int g2_op_at(int u) { for (int n = 0; n < 81; ++n) if (g2_pos(n) == u) return n; return -1; }
constexpr int g2_issued_before(int u) { int c = 0; for (int n = 0; n < 81; ++n) if (g2_pos(n) < u) ++c; return c; }
__global__ __launch_bounds__(512, 2) void k4_gemm2(const u16* __restrict__ hh, const u16* __restrict__ w2, u16* __restrict__ part) {
  extern __shared__ __attribute__((aligned(16))) char smem[];
  LAS char* lds = (LAS char*)smem;
  const int b = blockIdx.x, xcd = b & 7, idx = b >> 3;
  const int mt = (xcd & 1) * 8 + (idx & 7), nq = idx >> 3, ksl = xcd >> 1;
  const int tid = threadIdx.x, lane = tid & 63;
  const int w = __builtin_amdgcn_readfirstlane(tid >> 6);
  const int mq = w & 3, kg = w >> 2;
  STAMP_DECL; STAMP();
  const char* wsrc = (const char*)w2 + (size_t)(ksl * 1152 + w * 16) * (D * 2) + nq * 288;
  unsigned so[5];
#pragma unroll
  for (int i = 0; i < 5; ++i) {
    const int ci = i * 64 + lane;
    const int pr = ci / 18, ch = ci - pr * 18;
    const int r = pr ^ (((pr >> 3) & 1) << 2);
    so[i] = r * (D * 2) + ch * 16;
  }
  const unsigned wdst = (unsigned)(size_t)lds + w * 4608;
#define G2_G(st, i) do { const char* _s = wsrc + (size_t)(st) * (128 * D * 2); const unsigned _d = wdst + ((st) & 3) * G2_STAGE; \
    if ((i) < 4) glds16(_s, so[i], _d + (i) * 1024); else if (lane < 32) glds16(_s, so[4], _d + 4096); } while (0)
  const char* xs0 = (const char*)hh + ((size_t)(mt * 8 + mq * 2 + kg) * 144 + ksl * 36 + kg * 2) * 1024;
  const char* xs1 = (const char*)hh + ((size_t)(mt * 8 + mq * 2 + (kg ^ 1)) * 144 + ksl * 36 + kg * 2) * 1024;
  const unsigned xvo = lane * 16;
  u32x4 xr[3][4];
#define G2_X(st, i) do { const char* _p = (((i) & 1) ? xs1 : xs0) + (st) * 4096; GLOAD16(xr[(st) % 3][i], xvo, _p, ((i) >> 1) * 1024); } while (0)
#pragma unroll
  for (int i = 0; i < 5; ++i) G2_G(0, i);
#pragma unroll
  for (int i = 0; i < 4; ++i) G2_X(0, i);
  BAR();
#pragma unroll
  for (int i = 0; i < 5; ++i) G2_G(1, i);
  const int g = lane >> 4, q = (lane & 15) >> 2, p4 = lane & 3;
  unsigned tb[2][2];
#pragma unroll
  for (int t = 0; t < 2; ++t) { tb[0][t] = (unsigned)(size_t)lds + (kg * 64 + 8 * g + ((4 * t) ^ (4 * (g & 1))) + q) * 288 + p4 * 8; tb[1][t] = tb[0][t] + G2_STAGE; }
  asm volatile("" : "+v"(tb[0][0]), "+v"(tb[0][1]), "+v"(tb[1][0]), "+v"(tb[1][1]));
  f32x4 acc[2][9];
#pragma unroll
  for (int i = 0; i < 2; ++i)
#pragma unroll
    for (int t = 0; t < 9; ++t) { acc[i][t] = (f32x4){0.f, 0.f, 0.f, 0.f}; asm volatile("" : "+v"(acc[i][t])); }
  constexpr int PF2 = 5, RG2 = 6;
  s16x8 wf[RG2];
#define G2_RD(st, i) cat8(tr_read((LAS char*)(size_t)(tb[(st) & 1][0] + ((i) / 9) * (32 * 288) + ((i) % 9) * 32)), \
                          tr_read((LAS char*)(size_t)(tb[(st) & 1][1] + ((i) / 9) * (32 * 288) + ((i) % 9) * 32)))
  asm volatile("s_waitcnt vmcnt(5)" : "+v"(xr[0][0]), "+v"(xr[0][1]), "+v"(xr[0][2]), "+v"(xr[0][3]) :: "memory");
  BAR();
  STAMP();
  static_for<0, PF2>([&](auto I) { constexpr int i = decltype(I)::value; wf[i % RG2] = G2_RD(0, i); });
  static_for<0, 162>([&](auto U) {
    constexpr int u = decltype(U)::value, st = u / 18, i = u % 18;
    if constexpr (i == 12 && st < 8) {
      constexpr int nv = g2_issued_before(u) - 9 * (st + 2);
      static_assert(nv >= 0, "");
      asm volatile("s_waitcnt vmcnt(%4)" : "+v"(xr[(st + 1) % 3][0]), "+v"(xr[(st + 1) % 3][1]), "+v"(xr[(st + 1) % 3][2]), "+v"(xr[(st + 1) % 3][3]) : "n"(nv) : "memory");
      BAR();
      STAMP();
      if constexpr (st >= 1) {
        constexpr unsigned d = (((st + 1) & 3) >= 2) ? 2 * G2_STAGE : (unsigned)(-2 * G2_STAGE);
        tb[(st + 1) & 1][0] += d; tb[(st + 1) & 1][1] += d;
        asm volatile("" : "+v"(tb[(st + 1) & 1][0]), "+v"(tb[(st + 1) & 1][1]));
      }
    }
    constexpr int op = g2_op_at(u);
    if constexpr (op >= 0) { if constexpr (op % 9 < 5) G2_G(op / 9, op % 9); else G2_X(op / 9, op % 9 - 5); }
    if constexpr (u + PF2 < 162) wf[(u + PF2) % RG2] = G2_RD((u + PF2) / 18, (u + PF2) % 18);
    MFMA16A(wf[u % RG2], xr[st % 3][2 * (i / 9)], acc[0][i % 9]);
    MFMA16A(wf[u % RG2], xr[st % 3][2 * (i / 9) + 1], acc[1][i % 9]);
    SB0();
  });
  asm volatile("s_nop 7" : "+v"(acc[0][0]), "+v"(acc[0][1]), "+v"(acc[0][2]), "+v"(acc[0][3]), "+v"(acc[0][4]), "+v"(acc[0][5]), "+v"(acc[0][6]), "+v"(acc[0][7]), "+v"(acc[0][8]),
               "+v"(acc[1][0]), "+v"(acc[1][1]), "+v"(acc[1][2]), "+v"(acc[1][3]), "+v"(acc[1][4]), "+v"(acc[1][5]), "+v"(acc[1][6]), "+v"(acc[1][7]), "+v"(acc[1][8]) :: "memory");
  BAR();
  STAMP();
  {
    LAS f32x4* ex = (LAS f32x4*)lds;
#pragma unroll
    for (int t = 0; t < 9; ++t) ex[(w * 9 + t) * 64 + lane] = acc[1][t];
    WAIT_L(0);
    BAR();
    char* dst = (char*)part + ((size_t)(((ksl * 16 + mt) * 4 + nq) * 8 + mq * 2 + kg) * 5) * 1024 + lane * 16;
    unsigned pk[18];
#pragma unroll
    for (int t = 0; t < 9; ++t) {
      const f32x4 v = acc[0][t] + ex[((w ^ 4) * 9 + t) * 64 + lane];
      pk[2 * t] = pk_bf16(v[0], v[1]); pk[2 * t + 1] = pk_bf16(v[2], v[3]);
    }
#pragma unroll
    for (int i = 0; i < 4; ++i) *(u32x4*)(dst + i * 1024) = (u32x4){pk[4 * i], pk[4 * i + 1], pk[4 * i + 2], pk[4 * i + 3]};
    *(u32x2*)(dst + 4096) = (u32x2){pk[16], pk[17]};
  }
  STAMP();
  WAIT_V(0);
  STAMP();
  STAMP_OUT(4);
}

__global__ __launch_bounds__(320) void k5_reduce(const char* __restrict__ wsr, u16* __restrict__ out) {
  STAMP_DECL; STAMP();
  const int lane = threadIdx.x & 63, slot = threadIdx.x >> 6;
  const int u = blockIdx.x;
  const int m16 = u & 7, nq = (u >> 3) & 3, mt = u >> 5;
  const int T = mt * 8 + m16;
  const int row = 32 * (T >> 1) + 2 * (lane & 15) + (T & 1), col = nq * 144 + 32 * slot + 4 * (lane >> 4);
  constexpr size_t PS = (size_t)512 * 5 * 1024;
  const char* p = wsr + OFF_PART + ((size_t)u * 5 + slot) * 1024 + lane * 16;
  u32x4 v[4];
#pragma unroll
  for (int k = 0; k < 4; ++k) v[k] = *(const u32x4*)(p + k * PS);
  const bool two = slot < 4;
  const char* xp = wsr + OFF_X1 + ((size_t)(T * 36 + nq * 9 + 2 * slot) * 64 + lane) * 8;
  const u32x2 x0 = *(const u32x2*)xp;
  const u32x2 x2 = *(const u32x2*)(xp + (two ? 512 : 0));
  const float* modf = (const float*)(wsr + OFF_MOD);
  const float* gp = modf + 5 * D + col; const float* gq = gp + (two ? 16 : 0);
  f32x4 g0 = (*(const f32x4*)gp + *(const f32x4*)(gp + 3456)) + *(const f32x4*)(gp + 6912);
  f32x4 g1 = (*(const f32x4*)gq + *(const f32x4*)(gq + 3456)) + *(const f32x4*)(gq + 6912);
#pragma unroll
  for (int j = 0; j < 4; ++j) { g0[j] = rb(g0[j]); g1[j] = rb(g1[j]); }
  float y[8];
#pragma unroll
  for (int j = 0; j < 4; ++j) {
    y[2 * j] = (bf_lo(v[0][j]) + bf_lo(v[1][j])) + (bf_lo(v[2][j]) + bf_lo(v[3][j]));
    y[2 * j + 1] = (bf_hi(v[0][j]) + bf_hi(v[1][j])) + (bf_hi(v[2][j]) + bf_hi(v[3][j]));
  }
  const float xa[4] = {bf_lo(x0[0]), bf_hi(x0[0]), bf_lo(x0[1]), bf_hi(x0[1])};
  const float xb[4] = {bf_lo(x2[0]), bf_hi(x2[0]), bf_lo(x2[1]), bf_hi(x2[1])};
  float r[8];
#pragma unroll
  for (int j = 0; j < 4; ++j) { r[j] = xa[j] + rb(g0[j] * rb(y[j])); r[4 + j] = xb[j] + rb(g1[j] * rb(y[4 + j])); }
  *(u32x2*)(out + (size_t)row * D + col) = (u32x2){pk_bf16(r[0], r[1]), pk_bf16(r[2], r[3])};
  if (two) *(u32x2*)(out + (size_t)row * D + col + 16) = (u32x2){pk_bf16(r[4], r[5]), pk_bf16(r[6], r[7])};
  STAMP();
}

extern "C" void kernel_launch(void* const* d_in, const int* in_sizes, int n_in, void* d_out, int out_size,
                              void* d_ws, size_t ws_size, hipStream_t stream) {
  const u16* x = (const u16*)d_in[0];
  const u16* wqkv = (const u16*)d_in[1];
  const u16* wo = (const u16*)d_in[2];
  const u16* w1 = (const u16*)d_in[4];
  const u16* w3 = (const u16*)d_in[5];
  const u16* w2 = (const u16*)d_in[6];
  const u16* c = (const u16*)d_in[7];
  const u16* wmod = (const u16*)d_in[8];
  char* ws = (char*)d_ws;
  (void)hipFuncSetAttribute((const void*)k12_attn, hipFuncAttributeMaxDynamicSharedMemorySize, K1_LDS);
  (void)hipFuncSetAttribute((const void*)k3_gemm1, hipFuncAttributeMaxDynamicSharedMemorySize, G1_LDS);
  (void)hipFuncSetAttribute((const void*)k4_gemm2, hipFuncAttributeMaxDynamicSharedMemorySize, G2_LDS);
  k0_prep<<<K0_E, 384, 0, stream>>>(x, c, wmod, wqkv, wo, ws, w1, w3);
  k12_attn<<<256, 576, K1_LDS, stream>>>(ws, x);
  k3_gemm1<<<256, 512, G1_LDS, stream>>>((const u16*)(ws + OFF_H2), w1, w3, (u16*)(ws + OFF_HH), w2);
  k4_gemm2<<<256, 512, G2_LDS, stream>>>((const u16*)(ws + OFF_HH), w2, (u16*)(ws + OFF_PART));
  k5_reduce<<<512, 320, 0, stream>>>(ws, (u16*)d_out);
}
```

```cpp
#include <hip/hip_runtime.h>
#include <hip/hip_bf16.h>
#include <stdint.h>

typedef unsigned short u16;
typedef short s16x4 __attribute__((ext_vector_type(4)));
typedef short s16x8 __attribute__((ext_vector_type(8)));
typedef float f32x2 __attribute__((ext_vector_type(2)));
typedef float f32x4 __attribute__((ext_vector_type(4)));
typedef float f32x16 __attribute__((ext_vector_type(16)));
typedef unsigned u32x2 __attribute__((ext_vector_type(2)));
typedef unsigned u32x4 __attribute__((ext_vector_type(4)));
typedef __bf16 bf16x2 __attribute__((ext_vector_type(2)));
#define LAS __attribute__((address_space(3)))
#define GAS __attribute__((address_space(1)))

constexpr int S = 2048, D = 576, F = 4608;
constexpr size_t OFF_MOD = 0;
constexpr size_t OFF_GB = 45056;
constexpr size_t OFF_XN = 49152;
constexpr size_t OFF_O = OFF_XN + (size_t)S * D * 2;
constexpr size_t OFF_X1 = OFF_O + (size_t)S * D * 2;
constexpr size_t OFF_H2 = OFF_X1 + (size_t)S * D * 4;
constexpr size_t OFF_HH = OFF_H2 + (size_t)S * D * 2;
constexpr size_t OFF_PART = OFF_HH + (size_t)S * F * 2;
constexpr size_t OFF_WQ = OFF_PART + (size_t)4 * 512 * 5 * 1024;
constexpr size_t OFF_BIAS = OFF_WQ + (size_t)16 * 126 * 1024;
constexpr size_t OFF_WO = OFF_BIAS + (size_t)16 * 8192;
constexpr size_t OFF_XCH = OFF_WO + (size_t)36 * 18 * 1024;
constexpr size_t OFF_CNT = OFF_XCH + (size_t)64 * 4 * 64 * 8;
constexpr size_t OFF_END = OFF_CNT + 32768;
constexpr unsigned XCH_TAG = 0x5a17c3e1u;

__device__ __forceinline__ float bf_lo(unsigned v) { return __uint_as_float(v << 16); }
__device__ __forceinline__ float bf_hi(unsigned v) { return __uint_as_float(v & 0xffff0000u); }
__device__ __forceinline__ unsigned pk_bf16(float lo, float hi) {
  bf16x2 r; r.x = (__bf16)lo; r.y = (__bf16)hi;
  return __builtin_bit_cast(unsigned, r);
}
__device__ __forceinline__ float rb(float f) { return bf_lo(pk_bf16(f, 0.f)); }
template <int CTRL> __device__ __forceinline__ float dppf(float x) { return __int_as_float(__builtin_amdgcn_update_dpp(0, __float_as_int(x), CTRL, 0xf, 0xf, true)); }
__device__ __forceinline__ float sum16(float x) { auto r = __builtin_amdgcn_permlane16_swap(__float_as_uint(x), __float_as_uint(x), false, false); return __uint_as_float(r[0]) + __uint_as_float(r[1]); }
__device__ __forceinline__ float sum32(float x) { auto r = __builtin_amdgcn_permlane32_swap(__float_as_uint(x), __float_as_uint(x), false, false); return __uint_as_float(r[0]) + __uint_as_float(r[1]); }
__device__ __forceinline__ float max16(float x) { auto r = __builtin_amdgcn_permlane16_swap(__float_as_uint(x), __float_as_uint(x), false, false); return fmaxf(__uint_as_float(r[0]), __uint_as_float(r[1])); }
__device__ __forceinline__ float max32(float x) { auto r = __builtin_amdgcn_permlane32_swap(__float_as_uint(x), __float_as_uint(x), false, false); return fmaxf(__uint_as_float(r[0]), __uint_as_float(r[1])); }
__device__ __forceinline__ void rb2(float& a, float& b) { const unsigned p = pk_bf16(a, b); a = bf_lo(p); b = bf_hi(p); }
__device__ __forceinline__ float fast_silu(float g) { return g * __builtin_amdgcn_rcpf(1.f + __builtin_amdgcn_exp2f(g * -1.44269504f)); }

#define WAIT_V(n) asm volatile("s_waitcnt vmcnt(%0)" ::"n"(n) : "memory")
#define WAIT_L(n) asm volatile("s_waitcnt lgkmcnt(%0)" ::"n"(n) : "memory")
#define BAR() do { asm volatile("" ::: "memory"); __builtin_amdgcn_s_barrier(); asm volatile("" ::: "memory"); } while (0)
template <int IMM, class T> __device__ __forceinline__ void gload16(T& dst, unsigned voff, const void* sbase) {
  asm volatile("global_load_dwordx4 %0, %1, %2 offset:%3" : "=v"(dst) : "v"(voff), "s"(sbase), "n"(IMM) : "memory");
}
template <int IMM, class T> __device__ __forceinline__ void gload8(T& dst, unsigned voff, const void* sbase) {
  asm volatile("global_load_dwordx2 %0, %1, %2 offset:%3" : "=v"(dst) : "v"(voff), "s"(sbase), "n"(IMM) : "memory");
}
#define GLOAD16(dst, voff, sbase, imm) asm volatile("global_load_dwordx4 %0, %1, %2 offset:%3" : "=v"(dst) : "v"(voff), "s"(sbase), "n"(imm) : "memory")

__device__ __forceinline__ void glds16(const void* sbase, unsigned voff, unsigned ldsaddr) {
  asm volatile("s_mov_b32 m0, %2\n\ts_nop 0\n\tglobal_load_lds_dwordx4 %0, %1" :: "v"(voff), "s"(sbase), "s"(ldsaddr) : "memory");
}
__device__ __forceinline__ void glds16_sc1(const void* sbase, unsigned voff, unsigned ldsaddr) {
  asm volatile("s_mov_b32 m0, %2\n\ts_nop 0\n\tglobal_load_lds_dwordx4 %0, %1 sc1" :: "v"(voff), "s"(sbase), "s"(ldsaddr) : "memory");
}
#define SB0() __builtin_amdgcn_sched_barrier(0)
template <int I> struct IC { static constexpr int value = I; };
template <int B, int E, class Fn> __device__ __forceinline__ void static_for(Fn&& f) {
  if constexpr (B < E) { f(IC<B>{}); static_for<B + 1, E>(f); }
}
__device__ __forceinline__ void touch(const void* sbase, unsigned voff, unsigned ldsaddr) {
  asm volatile("s_mov_b32 m0, %2\n\ts_nop 0\n\tglobal_load_lds_dword %0, %1" :: "v"(voff), "s"(sbase), "s"(ldsaddr) : "memory");
}
__device__ __forceinline__ void pad2(u32x4& r) { asm volatile("s_nop 1" : "+v"(r)); }
template <class A, class B> __device__ __forceinline__ void MFMA16A(const A& a, const B& b, f32x4& c) {
  asm volatile("v_mfma_f32_16x16x32_bf16 %0, %1, %2, %0" : "+v"(c) : "v"(a), "v"(b));
}
#define STAMP_DECL
#define STAMP()
#define STAMP_OUT(kid)
__device__ __forceinline__ s16x4 tr_read(LAS char* p) { return __builtin_amdgcn_ds_read_tr16_b64_v4i16((LAS s16x4*)p); }
__device__ __forceinline__ s16x8 cat8(s16x4 a, s16x4 b) { return __builtin_shufflevector(a, b, 0, 1, 2, 3, 4, 5, 6, 7); }

__device__ __forceinline__ unsigned perm_lo(unsigned a, unsigned b) { return __builtin_amdgcn_perm(b, a, 0x05040100u); }
__device__ __forceinline__ unsigned perm_hi(unsigned a, unsigned b) { return __builtin_amdgcn_perm(b, a, 0x07060302u); }

constexpr int K0_A = 162, K0_B = K0_A + 128, K0_C = K0_B + 81, K0_D = K0_C + 27, K0_E = 400, K0_F = K0_E + 8 * 4;
extern "C" __global__ __launch_bounds__(384) void k0_prep(const u16* __restrict__ x, const u16* __restrict__ c, const u16* __restrict__ wmod,
                                               const u16* __restrict__ wqkv, const u16* __restrict__ wo, char* __restrict__ ws,
                                               const u16* __restrict__ w1, const u16* __restrict__ w3, const u16* __restrict__ w2,
                                               const u16* __restrict__ outp) {
  __shared__ __attribute__((aligned(16))) float red[6 * 64];
  const int b = blockIdx.x, tid = threadIdx.x;
  const int w = tid >> 6, lane = tid & 63;
  STAMP_DECL; STAMP();
  if (b < K0_A) {
    const int kt = b / 54, cb = b - kt * 54;
    const int r0 = kt * 192 + (tid >> 3), col = cb * 64 + (tid & 7) * 8;
    u32x4 wv[4]; float sv[4];
#pragma unroll
    for (int i = 0; i < 4; ++i) wv[i] = *(const u32x4*)(wmod + (size_t)(r0 + 48 * i) * (6 * D) + col);
#pragma unroll
    for (int i = 0; i < 4; ++i) sv[i] = rb(fast_silu(bf_lo(c[r0 + 48 * i])));
    float a[8] = {0, 0, 0, 0, 0, 0, 0, 0};
#pragma unroll
    for (int i = 0; i < 4; ++i)
#pragma unroll
      for (int j = 0; j < 4; ++j) { a[2 * j] += sv[i] * bf_lo(wv[i][j]); a[2 * j + 1] += sv[i] * bf_hi(wv[i][j]); }
#pragma unroll
    for (int j = 0; j < 8; ++j) { a[j] += dppf<0x128>(a[j]); a[j] = sum32(sum16(a[j])); }
    if (lane < 8) {
      *(f32x4*)(red + w * 64 + lane * 8) = (f32x4){a[0], a[1], a[2], a[3]};
      *(f32x4*)(red + w * 64 + lane * 8 + 4) = (f32x4){a[4], a[5], a[6], a[7]};
    }
    __syncthreads();
    if (tid < 64) {
      float s = 0.f;
#pragma unroll
      for (int i = 0; i < 6; ++i) s += red[i * 64 + tid];
      ((float*)(ws + OFF_MOD))[kt * 3456 + cb * 64 + tid] = s;
    }
  } else if (b < K0_B) {
    const int i = lane & 15, g = lane >> 4;
    const int T = b - K0_A;
    const int row = 32 * (T >> 1) + 2 * i + (T & 1);
    float v[3][8];
#pragma unroll
    for (int s = 0; s < 3; ++s) {
      const u32x4 a = *(const u32x4*)(x + (size_t)row * D + ((w * 3 + s) * 4 + g) * 8);
#pragma unroll
      for (int j = 0; j < 4; ++j) { v[s][2 * j] = bf_lo(a[j]); v[s][2 * j + 1] = bf_hi(a[j]); }
    }
    float sm = 0.f;
#pragma unroll
    for (int s = 0; s < 3; ++s) sm += ((v[s][0] + v[s][1]) + (v[s][2] + v[s][3])) + ((v[s][4] + v[s][5]) + (v[s][6] + v[s][7]));
    const float mw = sum32(sum16(sm)) * (1.f / 96.f);
    float q0 = 0.f, q1 = 0.f;
#pragma unroll
    for (int s = 0; s < 3; ++s)
#pragma unroll
      for (int j = 0; j < 4; ++j) { const float d0 = v[s][2 * j] - mw, d1 = v[s][2 * j + 1] - mw; q0 += d0 * d0; q1 += d1 * d1; }
    const float q = sum32(sum16(q0 + q1));
    if (g == 0) *(f32x2*)(red + (w * 16 + i) * 2) = (f32x2){mw, q};
    __syncthreads();
    f32x2 p[6];
    float mean = 0.f;
#pragma unroll
    for (int k = 0; k < 6; ++k) { p[k] = *(const f32x2*)(red + (k * 16 + i) * 2); mean += p[k][0]; }
    mean *= (1.f / 6.f);
    float var = 0.f;
#pragma unroll
    for (int k = 0; k < 6; ++k) { const float dl = p[k][0] - mean; var += p[k][1] + 96.f * dl * dl; }
    var *= (1.f / D);
    const float mr = rb(mean);
#pragma unroll
    for (int s = 0; s < 3; ++s)
#pragma unroll
      for (int j = 0; j < 4; ++j) { v[s][2 * j] -= mr; v[s][2 * j + 1] -= mr; rb2(v[s][2 * j], v[s][2 * j + 1]); }
    const float rs = __builtin_amdgcn_rcpf(rb(__builtin_amdgcn_sqrtf(rb(rb(var) + 1e-5f))));
#pragma unroll
    for (int s = 0; s < 3; ++s) {
      u32x4 o;
#pragma unroll
      for (int j = 0; j < 4; ++j) o[j] = pk_bf16(v[s][2 * j] * rs, v[s][2 * j + 1] * rs);
      *(u32x4*)(ws + OFF_XN + ((size_t)(T * 18 + w * 3 + s) * 64 + lane) * 16) = o;
    }
  } else if (b < K0_C) {
    const int task = (b - K0_B) * 384 + tid;
    const int kq = task / 432, nq4 = task - kq * 432;
    u32x2 r[8];
#pragma unroll
    for (int i = 0; i < 8; ++i) r[i] = *(const u32x2*)(wqkv + (size_t)(kq * 8 + i) * (3 * D) + nq4 * 4);
    const int part = nq4 / 144, nh = nq4 - part * 144, ks = kq >> 2, ko = kq & 3;
#pragma unroll
    for (int e = 0; e < 4; ++e) {
      const int n = nh * 4 + e, head = n / 18, d = n - head * 18, hp = head >> 1, h = head & 1;
      int tile, idx;
      if (d < 16) { tile = (part == 0) ? 5 + h : (part == 1 ? h : 3 + h); idx = d; }
      else { tile = 2; idx = (part == 2) ? 8 + 2 * h + (d - 16) : 4 * h + 2 * part + (d - 16); }
      char* dst = ws + OFF_WQ + ((hp * 18 + ks) * 7 + tile) * 1024 + (ko * 16 + idx) * 16;
      u32x4 f;
#pragma unroll
      for (int i = 0; i < 4; ++i) f[i] = (e & 1) ? perm_hi(r[2 * i][e >> 1], r[2 * i + 1][e >> 1]) : perm_lo(r[2 * i][e >> 1], r[2 * i + 1][e >> 1]);
      *(u32x4*)dst = f;
      if (part == 2 && d >= 16) *(u32x4*)(dst + 64) = (u32x4){0u, 0u, 0u, 0u};
    }
  } else if (b >= K0_E) {
    const int job = (b - K0_E) >> 3;
    const unsigned scr = (unsigned)(size_t)(LAS float*)red;
    if (job < 2) { const unsigned o = (job * 384 + tid) * 65536u; if (o < OFF_END) touch(ws, o, scr); }
    else if (job == 2) {
      if (tid < 81) { touch(w1, tid * 65536u, scr); touch(w3, tid * 65536u, scr); touch(w2, tid * 65536u, scr); }
      if (tid < 36) touch(outp, tid * 65536u, scr);
    } else {
      unsigned long long pc; int o0, o1;
      asm volatile("s_getpc_b64 %0\n.Lpc_%=:\n\ts_mov_b32 %1, k12_attn-.Lpc_%=\n\ts_mov_b32 %2, k_end_marker-.Lpc_%=" : "=&s"(pc), "=&s"(o0), "=&s"(o1));
      const int o = ((job - 3) * 384 + tid) * 128;
      if (o0 > 0 && o < o1 - o0) touch((const void*)pc, (unsigned)(o0 + o) & ~3u, scr);
    }
  } else if (b < K0_D) {
    const int task = (b - K0_C) * 384 + tid;
    const int nh = task % 144, ug = task / 144, u = ug >> 2, g = ug & 3;
    u32x2 r[8];
#pragma unroll
    for (int j = 0; j < 8; ++j) {
      const int krow = (u < 16) ? 36 * u + 18 * (j >> 2) + 4 * g + (j & 3) : 36 * (8 * (u - 16) + 2 * g + (j >> 2)) + 18 * ((j >> 1) & 1) + 16 + (j & 1);
      r[j] = *(const u32x2*)(wo + (size_t)krow * D + nh * 4);
    }
    char* dst = ws + OFF_WO + ((size_t)((nh >> 2) * 18 + u) * 64 + g * 16 + (nh & 3) * 4) * 16;
#pragma unroll
    for (int e = 0; e < 4; ++e) {
      u32x4 f;
#pragma unroll
      for (int i = 0; i < 4; ++i) f[i] = (e & 1) ? perm_hi(r[2 * i][e >> 1], r[2 * i + 1][e >> 1]) : perm_lo(r[2 * i][e >> 1], r[2 * i + 1][e >> 1]);
      *(u32x4*)(dst + e * 16) = f;
    }
    if (task < 64 * 4 * 64 / 2 + 2048) *(u32x4*)(ws + OFF_XCH + (size_t)task * 16) = (u32x4){0u, 0u, 0u, 0u};
  }
  STAMP();
}

constexpr int K1_W = 126 * 1024;
constexpr int K1_CONST = K1_W;
constexpr int K1_C2 = K1_CONST + 4608;
constexpr int K1_ST = K1_C2 + 1792;
constexpr int K1_FIN = K1_ST + 2304;
constexpr int K1_SCR = K1_FIN + 256;
constexpr int K1_LDS = K1_SCR + 256;
constexpr int K1_EK = 0, K1_EV = 9 * 2 * 1024, K1_EL = K1_EV + 9 * 2 * 512;
constexpr int K1_O = 36 * 1024;
constexpr int k1_r(int p) { return p < 0 ? 0 : (21 * (p + 1) + 8) / 9; }
constexpr int k1_cum(int p) { return 6 + 3 * (p + 1 < 5 ? p + 1 : 5) + k1_r(p); }
constexpr int K1_NOPS = 36 + 18 + 5;
constexpr int k1_op(int n) {
  if (n < 3) return 96;
  if (n < 6) return n - 3;
  for (int p = 0; p < 6; ++p) {
    const int lo = p ? k1_cum(p - 1) : 6;
    if (n < k1_cum(p)) { const int k = n - lo; return (p < 5 && k < 3) ? 3 * p + 3 + k : 32 + k1_r(p - 1) + k - (p < 5 ? 3 : 0); }
  }
  return n == 35 ? 97 : 64 + n - 36;
}
constexpr int k1_pos(int n) {
  if (n < k1_cum(0)) return -1;
  if (n < k1_cum(1)) return 0;
  if (n < k1_cum(2)) return 1 + (n - k1_cum(1)) / 3;
  for (int p = 3; p < 6; ++p) if (n < k1_cum(p)) return 3 * (p - 2) + (n - k1_cum(p - 1)) / 2;
  return n == 35 ? 10 : 10 + (n - 36) / 3;
}
constexpr int k1_before(int s) { int c = 0; for (int n = 0; n < K1_NOPS; ++n) if (k1_pos(n) < s) ++c; return c; }

struct Cst { f32x4 s0, s1, g0, g1; };
__device__ __forceinline__ void rdc(Cst& c, const LAS float* cs) {
  c.s0 = *(const LAS f32x4*)cs; c.s1 = *(const LAS f32x4*)(cs + 4); c.g0 = *(const LAS f32x4*)(cs + 576); c.g1 = *(const LAS f32x4*)(cs + 580);
}
__device__ __forceinline__ float dot2(unsigned x, float gw, float s) {
  float r; asm("v_dot2_f32_bf16 %0, %1, %2, %3" : "=v"(r) : "v"(x), "v"(gw), "v"(s)); return r;
}
__device__ __forceinline__ void modc(u32x4& xv, const Cst& c) {
  asm volatile("" : "+v"(xv));
  const u32x4 v = xv;
  float h[8];
  h[0] = dot2(v[0], c.g0[0], c.s0[0]); h[1] = dot2(v[0], c.g0[1], c.s0[1]); h[2] = dot2(v[1], c.g0[2], c.s0[2]); h[3] = dot2(v[1], c.g0[3], c.s0[3]);
  h[4] = dot2(v[2], c.g1[0], c.s1[0]); h[5] = dot2(v[2], c.g1[1], c.s1[1]); h[6] = dot2(v[3], c.g1[2], c.s1[2]); h[7] = dot2(v[3], c.g1[3], c.s1[3]);
  asm volatile("s_nop 2" : "+v"(h[0]), "+v"(h[1]), "+v"(h[2]), "+v"(h[3]), "+v"(h[4]), "+v"(h[5]), "+v"(h[6]), "+v"(h[7]));
  u32x4 r;
  r[0] = pk_bf16(h[0], h[1]); r[1] = pk_bf16(h[2], h[3]); r[2] = pk_bf16(h[4], h[5]); r[3] = pk_bf16(h[6], h[7]);
  pad2(r);
  xv = r;
}
template <int N> __device__ __forceinline__ void wait3(u32x4& a, u32x4& b, u32x4& c) {
  asm volatile("s_waitcnt vmcnt(%3)" : "+v"(a), "+v"(b), "+v"(c) : "n"(N) : "memory");
}
__device__ __forceinline__ void pin(u32x4& a) { asm volatile("" : "+v"(a)); }

extern "C" __global__ __launch_bounds__(576) void k12_attn(char* __restrict__ ws, const u16* __restrict__ x, const u16* __restrict__ w1, const u16* __restrict__ w3) {
  extern __shared__ __attribute__((aligned(16))) char smem[];
  LAS char* lds = (LAS char*)smem;
  const unsigned lbase = (unsigned)(size_t)lds;
  const int b = blockIdx.x, xcd = b & 7, idx = b >> 3;
  const int rt = (xcd & 3) * 4 + (idx & 3), hp = (xcd >> 2) * 8 + (idx >> 2);
  const int tid = threadIdx.x, lane = tid & 63;
  const int w = __builtin_amdgcn_readfirstlane(tid >> 6);
  const int li = lane & 15, g = lane >> 4;
  const bool halo = (w == 8);
  STAMP_DECL; STAMP();
  const unsigned l16 = lane * 16;
  const int tb = halo ? (rt > 0 ? 8 * rt - 2 : 0) : 8 * rt + w;
  const char* xsrc = ws + OFF_XN + (size_t)tb * (18 * 1024);
  const unsigned xvo = halo ? (unsigned)((li >> 3) * (18 * 1024) + (g * 16 + 8 + (li & 7)) * 16) : l16;
  const char* wsrc = ws + OFF_WQ + (size_t)hp * K1_W + w * 1024;
  const int rt2 = 4 * rt + (hp >> 2), nq = hp & 3;
  const int nt = nq * 9 + w;
  const char* wsrc2 = ws + OFF_WO + (size_t)nt * (18 * 1024);
  u32x4 xf[18], wo[18];
  u32x2 xr[2]; f32x4 mq[3];
  auto issue = [&](auto N) {
    constexpr int op = k1_op(decltype(N)::value);
    if constexpr (op < 32) { const char* q = xsrc + (op & ~3) * 1024; gload16<(op & 3) * 1024>(xf[op], xvo, q); }
    else if constexpr (op < 64) glds16(wsrc + (op - 32) * 9216, l16, lbase + (w + 9 * (op - 32)) * 1024);
    else if constexpr (op < 82) { constexpr int i = op - 64; const char* q = wsrc2 + (i & ~3) * 1024; gload16<(i & 3) * 1024>(wo[i], l16, q); }
    else if constexpr (op < 84) gload8<(op - 82) * 1152>(xr[op - 82], (unsigned)(((rt2 * 32 + 2 * li) * D + nt * 16 + 4 * g) * 2), x);
    else if constexpr (op < 87) {
      const int t = tid < 144 ? tid : 143, vec = t / 36, c4 = t - vec * 36;
      gload16<0>(mq[op - 84], (unsigned)(((2 + vec) * D + nq * 144 + c4 * 4) * 4), ws + OFF_MOD + (op - 84) * 3456 * 4);
    }
    else {
      const int ln = idx * 108 + (w & 3) * 27 + (lane < 27 ? lane : 26);
      touch((w & 4) ? w3 : w1, (ln / 18) * (F * 2) + (xcd >> 1) * 2304 + (ln % 18) * 128, lbase + K1_SCR);
    }
  };
  f32x2 mp[3];
  {
    const unsigned mo = tid * 8;
    const char* m0 = ws + OFF_MOD; const char* m1 = m0 + 3456 * 4; const char* m2 = m1 + 3456 * 4;
    asm volatile("global_load_dwordx2 %0, %1, %2" : "=v"(mp[0]) : "v"(mo), "s"(m0) : "memory");
    asm volatile("global_load_dwordx2 %0, %1, %2" : "=v"(mp[1]) : "v"(mo), "s"(m1) : "memory");
    asm volatile("global_load_dwordx2 %0, %1, %2" : "=v"(mp[2]) : "v"(mo), "s"(m2) : "memory");
  }
  static_for<3, k1_cum(0)>(issue);
  asm volatile("s_waitcnt vmcnt(%3)" : "+v"(mp[0]), "+v"(mp[1]), "+v"(mp[2]) : "n"(k1_cum(0) - 3) : "memory");
  {
    float m0 = (mp[0][0] + mp[1][0]) + mp[2][0], m1 = (mp[0][1] + mp[1][1]) + mp[2][1];
    rb2(m0, m1);
    if (tid >= 288) { m0 += 1.f; m1 += 1.f; rb2(m0, m1); m0 = __uint_as_float(__float_as_uint(m0) >> 16); }
    *(LAS f32x2*)(lds + K1_CONST + tid * 8) = (f32x2){m0, m1};
    WAIT_L(0);
  }
  BAR();
  const LAS float* cs = (const LAS float*)(lds + K1_CONST) + g * 8;
  Cst cst;
  {
    Cst c1, c2;
    rdc(cst, cs); rdc(c1, cs + 32); rdc(c2, cs + 64);
    wait3<k1_cum(0) - 6>(xf[0], xf[1], xf[2]);
    modc(xf[0], cst); modc(xf[1], c1); modc(xf[2], c2);
  }
  rdc(cst, cs + 96);
  f32x4 acc[7];
#pragma unroll
  for (int t = 0; t < 7; ++t) { acc[t] = (f32x4){0.f, 0.f, 0.f, 0.f}; asm volatile("" : "+v"(acc[t])); }
  unsigned wa = lbase + l16;
  s16x8 wf[7];
  auto rdw = [&](auto Sx, auto T0, auto T1) {
    constexpr int s = decltype(Sx)::value, off = (s % 6) * 7168;
#pragma unroll
    for (int t = decltype(T0)::value; t < decltype(T1)::value; ++t) wf[t] = *(const LAS s16x8*)(size_t)(wa + off + t * 1024);
  };
  wait3<0>(xf[3], xf[4], xf[5]);
  BAR();
  STAMP();
  rdw(IC<0>{}, IC<0>{}, IC<7>{});
  static_for<0, 18>([&](auto Sx) {
    constexpr int s = decltype(Sx)::value, p = s / 3;
    if constexpr (s % 3 == 2 && p < 5) {
      if constexpr (p < 4) wait3<k1_before(s) - k1_cum(p + 1)>(xf[3 * p + 6], xf[3 * p + 7], xf[3 * p + 8]);
      else WAIT_V(k1_before(s) - k1_cum(5));
      BAR();
      STAMP();
    }
    if constexpr (s == 5 || s == 11) { wa += 6 * 7168; asm volatile("" : "+v"(wa)); }
    if constexpr (s == 0) static_for<k1_before(0), k1_before(1)>(issue);
    MFMA16A(wf[0], xf[s], acc[0]); MFMA16A(wf[1], xf[s], acc[1]); MFMA16A(wf[2], xf[s], acc[2]); MFMA16A(xf[s], wf[3], acc[3]);
    if constexpr (s < 17) rdw(IC<s + 1>{}, IC<0>{}, IC<4>{});
    if constexpr (s + 3 < 18) modc(xf[s + 3], cst);
    if constexpr (s + 4 < 18) rdc(cst, cs + (s + 4) * 32);
    MFMA16A(xf[s], wf[4], acc[4]);
    if (!halo) { MFMA16A(wf[5], xf[s], acc[5]); MFMA16A(wf[6], xf[s], acc[6]); }
    if constexpr (s < 17) rdw(IC<s + 1>{}, IC<4>{}, IC<7>{});
    if constexpr (s > 0) static_for<k1_before(s), k1_before(s + 1)>(issue);
  });
  STAMP();
  asm volatile("s_nop 7" : "+v"(acc[0]), "+v"(acc[1]), "+v"(acc[2]), "+v"(acc[3]), "+v"(acc[4]), "+v"(acc[5]), "+v"(acc[6]) :: "memory");
  unsigned kf[2][2], qf[2][2], vf[2][2], lf[2];
#pragma unroll
  for (int h = 0; h < 2; ++h) {
    kf[h][0] = pk_bf16(acc[h][0], acc[h][1]); kf[h][1] = pk_bf16(acc[h][2], acc[h][3]);
    qf[h][0] = pk_bf16(acc[5 + h][0], acc[5 + h][1]); qf[h][1] = pk_bf16(acc[5 + h][2], acc[5 + h][3]);
    vf[h][0] = pk_bf16(acc[3 + h][0], acc[3 + h][1]); vf[h][1] = pk_bf16(acc[3 + h][2], acc[3 + h][3]);
  }
  lf[0] = pk_bf16(acc[2][0], acc[2][1]); lf[1] = pk_bf16(acc[2][2], acc[2][3]);
#pragma unroll
  for (int h = 0; h < 2; ++h) {
    *(LAS u32x4*)(lds + K1_EK + (w * 2 + h) * 1024 + l16) = (u32x4){kf[h][0], kf[h][1], lf[1], 0u};
    *(LAS u32x2*)(lds + K1_EV + (w * 2 + h) * 512 + lane * 8) = (u32x2){vf[h][0], vf[h][1]};
  }
  if (g == 2) *(LAS u32x2*)(lds + K1_EL + (w * 16 + li) * 8) = (u32x2){lf[0], lf[1]};
  WAIT_L(0);
  BAR();
  STAMP();
  if (!halo) {
    const int par = w & 1;
    const bool first = (w < 2);
    const int pw = first ? 8 : w - 2;
    const int pki = first ? 8 * par + (li & 7) : li;
    const int pvg = first ? 2 * par + (g & 1) : g;
    const int q4 = li >> 2;
    const int plk = first ? 8 * par + ((4 * g + q4) & 7) : 4 * g + q4;
    const s16x8 vl = cat8(tr_read(lds + K1_EL + (pw * 16 + plk) * 8), tr_read(lds + K1_EL + (w * 16 + 4 * g + q4) * 8));
    const bool prev_ok = !(first && rt == 0);
    unsigned op[2][2], ol[2];
    bool usep[4], useo[4];
#pragma unroll
    for (int j = 0; j < 4; ++j) { const int kk = 4 * g + j; usep[j] = prev_ok && (kk >= li + 9); useo[j] = (kk <= li) && (li - kk <= 7); }
    const unsigned mp01 = (usep[0] ? 0xffffu : 0u) | (usep[1] ? 0xffff0000u : 0u), mp23 = (usep[2] ? 0xffffu : 0u) | (usep[3] ? 0xffff0000u : 0u);
#pragma unroll
    for (int h = 0; h < 2; ++h) {
      const u32x4 kp = *(const LAS u32x4*)(lds + K1_EK + (pw * 2 + h) * 1024 + (g * 16 + pki) * 16);
      const u32x2 vp = *(const LAS u32x2*)(lds + K1_EV + (pw * 2 + h) * 512 + (pvg * 16 + li) * 8);
      const u32x4 ko = (u32x4){kf[h][0], kf[h][1], lf[1], 0u};
      const u32x4 qq = (u32x4){qf[h][0], qf[h][1], (g == h) ? lf[0] : 0u, 0u};
      const f32x4 z = (f32x4){0.f, 0.f, 0.f, 0.f};
      const f32x4 so = __builtin_amdgcn_mfma_f32_16x16x32_bf16(__builtin_bit_cast(s16x8, ko), __builtin_bit_cast(s16x8, qq), z, 0, 0, 0);
      const f32x4 sp = __builtin_amdgcn_mfma_f32_16x16x32_bf16(__builtin_bit_cast(s16x8, kp), __builtin_bit_cast(s16x8, qq), z, 0, 0, 0);
      float lg[4];
#pragma unroll
      for (int j = 0; j < 4; ++j) lg[j] = usep[j] ? sp[j] : so[j];
      rb2(lg[0], lg[1]); rb2(lg[2], lg[3]);
#pragma unroll
      for (int j = 0; j < 4; ++j) lg[j] *= 0.23570226f;
      rb2(lg[0], lg[1]); rb2(lg[2], lg[3]);
#pragma unroll
      for (int j = 0; j < 4; ++j) lg[j] = (usep[j] || useo[j]) ? lg[j] : -3.0e38f;
      const float mx = max32(max16(fmaxf(fmaxf(lg[0], lg[1]), fmaxf(lg[2], lg[3]))));
#pragma unroll
      for (int j = 0; j < 4; ++j) lg[j] = __builtin_amdgcn_exp2f((lg[j] - mx) * 1.44269504f);
      const float inv = __builtin_amdgcn_rcpf(sum32(sum16((lg[0] + lg[1]) + (lg[2] + lg[3]))));
      const unsigned p01 = pk_bf16(lg[0] * inv, lg[1] * inv), p23 = pk_bf16(lg[2] * inv, lg[3] * inv);
      const u32x4 pf = (u32x4){p01 & mp01, p23 & mp23, p01 & ~mp01, p23 & ~mp23};
      const u32x4 vv = (u32x4){vp[0], vp[1], vf[h][0], vf[h][1]};
      const f32x4 oo = __builtin_amdgcn_mfma_f32_16x16x32_bf16(__builtin_bit_cast(s16x8, vv), __builtin_bit_cast(s16x8, pf), z, 0, 0, 0);
      const f32x4 lo = __builtin_amdgcn_mfma_f32_16x16x32_bf16(vl, __builtin_bit_cast(s16x8, pf), z, 0, 0, 0);
      op[h][0] = pk_bf16(oo[0], oo[1]); op[h][1] = pk_bf16(oo[2], oo[3]);
      ol[h] = pk_bf16(lo[2 * h], lo[2 * h + 1]);
    }
    const char* ob = ws + OFF_O + (size_t)(8 * rt + w) * (18 * 1024);
    const u32x4 o4 = (u32x4){op[0][0], op[0][1], op[1][0], op[1][1]};
    const unsigned oo0 = hp * 1024 + l16;
    asm volatile("global_store_dwordx4 %0, %1, %2 sc1\n\ts_nop 1" :: "v"(oo0), "v"(o4), "s"(ob) : "memory");
    if (g == 0) {
      const u32x2 o2 = (u32x2){ol[0], ol[1]};
      const unsigned oo1 = (16 + (hp >> 3)) * 1024 + (((hp & 7) >> 1) * 16 + li) * 16 + 8 * (hp & 1);
      asm volatile("global_store_dwordx2 %0, %1, %2 sc1\n\ts_nop 1" :: "v"(oo1), "v"(o2), "s"(ob) : "memory");
    }
  }
  STAMP();
  asm volatile("s_waitcnt vmcnt(0)" : "+v"(xr[0]), "+v"(xr[1]), "+v"(mq[0]), "+v"(mq[1]), "+v"(mq[2]) :: "memory");
  static_for<0, 18>([&](auto I) { pin(wo[decltype(I)::value]); });
  BAR();
  GAS unsigned* cnt = (GAS unsigned*)(ws + OFF_CNT) + rt * 64;
  if (tid == 512) __hip_atomic_fetch_add(cnt, 1u, __ATOMIC_RELAXED, __HIP_MEMORY_SCOPE_AGENT);
  if (tid < 144) {
    float t[4];
#pragma unroll
    for (int j = 0; j < 4; ++j) t[j] = (mq[0][j] + mq[1][j]) + mq[2][j];
    rb2(t[0], t[1]); rb2(t[2], t[3]);
    if (tid >= 72 && tid < 108) {
#pragma unroll
      for (int j = 0; j < 4; ++j) t[j] += 1.f;
      rb2(t[0], t[1]); rb2(t[2], t[3]);
    }
    if (tid < 108) *(LAS f32x4*)(lds + K1_C2 + tid * 16) = (f32x4){t[0], t[1], t[2], t[3]};
    else if (rt2 == 0) *(f32x4*)(ws + OFF_GB + (nq * 144 + (tid - 108) * 4) * 4) = (f32x4){t[0], t[1], t[2], t[3]};
  }
  if (halo) {
    for (unsigned spin = 0; spin < (1u << 16); ++spin) {
      if (__builtin_amdgcn_readfirstlane(__hip_atomic_load(cnt, __ATOMIC_RELAXED, __HIP_MEMORY_SCOPE_AGENT)) >= 16u) break;
      __builtin_amdgcn_s_sleep(1);
    }
  }
  BAR();
  STAMP();
  {
    const char* osrc = ws + OFF_O + (size_t)rt2 * (36 * 1024) + w * 4096;
#pragma unroll
    for (int i = 0; i < 4; ++i) glds16_sc1(osrc + i * 1024, l16, lbase + K1_O + w * 4096 + i * 1024);
  }
  asm volatile("s_waitcnt vmcnt(0) lgkmcnt(0)" ::: "memory");
  BAR();
  STAMP();
  f32x4 ac2[2];
  ac2[0] = (f32x4){0.f, 0.f, 0.f, 0.f}; ac2[1] = ac2[0];
  {
    unsigned oa = lbase + K1_O + l16;
    asm volatile("" : "+v"(oa));
#pragma unroll
    for (int s = 0; s < 18; ++s) {
      const s16x8 x0 = *(const LAS s16x8*)(size_t)(oa + s * 1024), x1f = *(const LAS s16x8*)(size_t)(oa + (18 + s) * 1024);
      ac2[0] = __builtin_amdgcn_mfma_f32_16x16x32_bf16(__builtin_bit_cast(s16x8, wo[s]), x0, ac2[0], 0, 0, 0);
      ac2[1] = __builtin_amdgcn_mfma_f32_16x16x32_bf16(__builtin_bit_cast(s16x8, wo[s]), x1f, ac2[1], 0, 0, 0);
    }
  }
  const f32x4 ga = *(const LAS f32x4*)(lds + K1_C2 + (w * 16 + 4 * g) * 4);
  const f32x4 sb = *(const LAS f32x4*)(lds + K1_C2 + (144 + w * 16 + 4 * g) * 4);
  const f32x4 g2 = *(const LAS f32x4*)(lds + K1_C2 + (288 + w * 16 + 4 * g) * 4);
  float v[2][4];
  f32x2 sq[2];
  LAS f32x2* st = (LAS f32x2*)(lds + K1_ST);
  LAS f32x2* fin = (LAS f32x2*)(lds + K1_FIN);
#pragma unroll
  for (int m = 0; m < 2; ++m) {
    const float xv[4] = {bf_lo(xr[m][0]), bf_hi(xr[m][0]), bf_lo(xr[m][1]), bf_hi(xr[m][1])};
#pragma unroll
    for (int j = 0; j < 4; ++j) v[m][j] = ac2[m][j];
    rb2(v[m][0], v[m][1]); rb2(v[m][2], v[m][3]);
#pragma unroll
    for (int j = 0; j < 4; ++j) v[m][j] *= ga[j];
    rb2(v[m][0], v[m][1]); rb2(v[m][2], v[m][3]);
    const unsigned p0 = pk_bf16(v[m][0] + xv[0], v[m][1] + xv[1]), p1 = pk_bf16(v[m][2] + xv[2], v[m][3] + xv[3]);
    v[m][0] = bf_lo(p0); v[m][1] = bf_hi(p0); v[m][2] = bf_lo(p1); v[m][3] = bf_hi(p1);
    const float mw = sum32(sum16((v[m][0] + v[m][1]) + (v[m][2] + v[m][3]))) * (1.f / 16.f);
    const float d0 = v[m][0] - mw, d1 = v[m][1] - mw, d2 = v[m][2] - mw, d3 = v[m][3] - mw;
    const float q = sum32(sum16((d0 * d0 + d1 * d1) + (d2 * d2 + d3 * d3)));
    sq[m] = (f32x2){mw, q};
    xr[m] = (u32x2){p0, p1};
  }
  if (g < 2) st[w * 32 + g * 16 + li] = g ? sq[1] : sq[0];
  WAIT_L(0);
  BAR();
  STAMP();
  if (w == 0) {
    const int r = lane & 31;
    float mt_ = 0.f;
    f32x2 sv[9];
#pragma unroll
    for (int k = 0; k < 9; ++k) { sv[k] = st[k * 32 + r]; mt_ += sv[k][0]; }
    mt_ *= (1.f / 9.f);
    float m2 = 0.f;
#pragma unroll
    for (int k = 0; k < 9; ++k) { const float dl = sv[k][0] - mt_; m2 += sv[k][1] + 16.f * dl * dl; }
    GAS unsigned long long* xch = (GAS unsigned long long*)(ws + OFF_XCH) + (size_t)rt2 * 256;
    const float mine = (lane < 32) ? mt_ : m2;
    __hip_atomic_store(xch + nq * 64 + lane, ((unsigned long long)XCH_TAG << 32) | __float_as_uint(mine), __ATOMIC_RELAXED, __HIP_MEMORY_SCOPE_AGENT);
    float o[4];
    for (unsigned spin = 0; spin < (1u << 16); ++spin) {
      bool ok = true;
#pragma unroll
      for (int k = 0; k < 4; ++k) {
        const unsigned long long gv = __hip_atomic_load(xch + k * 64 + lane, __ATOMIC_RELAXED, __HIP_MEMORY_SCOPE_AGENT);
        o[k] = __uint_as_float((unsigned)gv); ok &= ((unsigned)(gv >> 32) == XCH_TAG);
      }
      if (__all(ok)) break;
      __builtin_amdgcn_s_sleep(1);
    }
    const float mean = 0.25f * ((o[0] + o[1]) + (o[2] + o[3]));
    float t = 0.f;
#pragma unroll
    for (int k = 0; k < 4; ++k) { const float dl = o[k] - mean; t += 144.f * dl * dl; }
    const auto sw = __builtin_amdgcn_permlane32_swap(__float_as_uint(mean), __float_as_uint(mean), false, false);
    const float m2s = 4.f * __uint_as_float(sw[1]);
    if (lane < 32) {
      const float var = (t + m2s) * (1.f / D);
      fin[r] = (f32x2){rb(mean), __builtin_amdgcn_rcpf(rb(__builtin_amdgcn_sqrtf(rb(rb(var) + 1e-5f))))};
    }
  }
#pragma unroll
  for (int m = 0; m < 2; ++m) *(u32x2*)(ws + OFF_X1 + ((size_t)((rt2 * 2 + m) * 36 + nt) * 64 + lane) * 8) = xr[m];
  WAIT_L(0);
  BAR();
  STAMP();
#pragma unroll
  for (int m = 0; m < 2; ++m) {
    const f32x2 f = fin[m * 16 + li];
    float h[4];
#pragma unroll
    for (int j = 0; j < 4; ++j) h[j] = v[m][j] - f[0];
    rb2(h[0], h[1]); rb2(h[2], h[3]);
#pragma unroll
    for (int j = 0; j < 4; ++j) h[j] *= f[1];
    rb2(h[0], h[1]); rb2(h[2], h[3]);
#pragma unroll
    for (int j = 0; j < 4; ++j) h[j] *= g2[j];
    rb2(h[0], h[1]); rb2(h[2], h[3]);
#pragma unroll
    for (int j = 0; j < 4; ++j) h[j] += sb[j];
    *(u32x2*)(ws + OFF_H2 + ((size_t)(rt2 * 36 + nt) * 64 + (g >> 1) * 32 + m * 16 + li) * 16 + (g & 1) * 8) = (u32x2){pk_bf16(h[0], h[1]), pk_bf16(h[2], h[3])};
  }
  STAMP_OUT(1);
}

constexpr int G1_W3 = 64 * 288 + 128;
constexpr int G1_STAGE = 2 * G1_W3;
constexpr int G1_LDS = 4 * G1_STAGE + 256;
#define G1_WAITX(n, xf) asm volatile("s_waitcnt vmcnt(%4)" : "+v"(xf[0]), "+v"(xf[1]), "+v"(xf[2]), "+v"(xf[3]) : "n"(n) : "memory")
constexpr int g2_pos(int n) {
  const int s = n / 9, k = n % 9;
  if (s <= 1) return -1;
  return 18 * (s - 2) + 2 * k;
}
constexpr int g2_op_at(int u) { for (int n = 0; n < 81; ++n) if (g2_pos(n) == u) return n; return -1; }
constexpr int g2_issued_before(int u) { int c = 0; for (int n = 0; n < 81; ++n) if (g2_pos(n) < u) ++c; return c; }
template <int IMM, class T> __device__ __forceinline__ void gload16_sc1(T& dst, unsigned voff, const void* sbase) {
  asm volatile("global_load_dwordx4 %0, %1, %2 offset:%3 sc1" : "=v"(dst) : "v"(voff), "s"(sbase), "n"(IMM) : "memory");
}
template <int IMM> __device__ __forceinline__ void gstore16_sc1(const u32x4& v, unsigned voff, const void* sbase) {
  asm volatile("global_store_dwordx4 %0, %1, %2 offset:%3 sc1\n\ts_nop 1" :: "v"(voff), "v"(v), "s"(sbase), "n"(IMM) : "memory");
}
__device__ __forceinline__ void gstore8_sc1(const u32x2& v, unsigned voff, const void* sbase) {
  asm volatile("global_store_dwordx2 %0, %1, %2 sc1\n\ts_nop 1" :: "v"(voff), "v"(v), "s"(sbase) : "memory");
}
template <int N> __device__ __forceinline__ void wait4(u32x4 (&x)[4]) {
  asm volatile("s_waitcnt vmcnt(%4)" : "+v"(x[0]), "+v"(x[1]), "+v"(x[2]), "+v"(x[3]) : "n"(N) : "memory");
}
__device__ __forceinline__ void pinu(unsigned& a, unsigned& b) { asm volatile("" : "+v"(a), "+v"(b)); }
__device__ __forceinline__ void wait_count(GAS unsigned* cnt, unsigned want) {
  for (unsigned spin = 0; spin < (1u << 16); ++spin) {
    if (__builtin_amdgcn_readfirstlane(__hip_atomic_load(cnt, __ATOMIC_RELAXED, __HIP_MEMORY_SCOPE_AGENT)) >= want) break;
    __builtin_amdgcn_s_sleep(1);
  }
}

extern "C" __global__ __launch_bounds__(512, 2) void k345_mlp(char* __restrict__ ws, const u16* __restrict__ w1, const u16* __restrict__ w3,
                                                   const u16* __restrict__ w2, u16* __restrict__ out) {
  extern __shared__ __attribute__((aligned(16))) char smem[];
  LAS char* lds = (LAS char*)smem;
  const unsigned lbase = (unsigned)(size_t)lds;
  const int b = blockIdx.x, xcd = b & 7, idx = b >> 3;
  const int tid = threadIdx.x, lane = tid & 63;
  const int w = __builtin_amdgcn_readfirstlane(tid >> 6);
  const unsigned xvo = lane * 16;
  const int ksl = xcd >> 1;
  const int mt2 = (xcd & 1) * 8 + (idx & 7), nq = idx >> 3;
  const int mq = w & 3, kg = w >> 2;
  const char* hhb = ws + OFF_HH;
  STAMP_DECL; STAMP();
  unsigned so[5];
  const char* wsrc2 = (const char*)w2 + (size_t)(ksl * 1152 + w * 16) * (D * 2) + nq * 288;
  const unsigned wdst2 = lbase + w * 4608;
#define G2_G(st, i) do { const char* _s = wsrc2 + (size_t)(st) * (128 * D * 2); const unsigned _d = wdst2 + (((st) + 1) & 3) * G1_STAGE; \
    if ((i) < 4) glds16(_s, so[i], _d + (i) * 1024); else if (lane < 32) glds16(_s, so[4], _d + 4096); } while (0)
  {
    const int mt = (xcd & 1) * 4 + (idx & 3), ct = ksl * 8 + (idx >> 2);
    const char* wsrc = (const char*)((w < 4) ? w1 : w3) + (size_t)((w & 3) * 16) * (F * 2) + ct * 288;
#pragma unroll
    for (int i = 0; i < 5; ++i) {
      const int ci = i * 64 + lane;
      const int r = ci / 18, ch = ci - r * 18;
      so[i] = r * (F * 2) + ch * 16;
    }
    const unsigned wdst = lbase + (w >> 2) * G1_W3 + (w & 3) * 4608;
#define G1_G(st, i) do { const char* _s = wsrc + (size_t)(st) * (64 * F * 2); const unsigned _d = wdst + ((st) & 3) * G1_STAGE; \
    if ((i) < 4) glds16(_s, so[i], _d + (i) * 1024); else if (lane < 32) glds16(_s, so[4], _d + 4096); } while (0)
    const char* xsrc = ws + OFF_H2 + (size_t)(mt * 8 + w) * (36 * 1024);
    u32x4 xa[4], xb[4], xc[4];
#define G1_X(dst, st, i) do { const char* _p = xsrc + (st) * 4096; GLOAD16(dst[i], xvo, _p, (i) * 1024); } while (0)
#pragma unroll
    for (int i = 0; i < 5; ++i) G1_G(0, i);
#pragma unroll
    for (int i = 0; i < 4; ++i) G1_X(xa, 0, i);
    BAR();
#pragma unroll
    for (int i = 0; i < 5; ++i) G1_G(1, i);
    const int grp = lane >> 4, q = (lane & 15) >> 2, p4 = lane & 3;
    const unsigned ta = (grp & 1) * G1_W3 + (8 * (grp >> 1) + q) * 288 + (((p4 & 1) << 4) | ((p4 >> 1) << 3));
    f32x16 acc[9];
#pragma unroll
    for (int t = 0; t < 9; ++t)
      for (int j = 0; j < 16; ++j) acc[t][j] = 0.f;
    s16x8 wf[4];
#define G1_RDKT(b, ks, t) cat8(tr_read((b) + (16 * (ks)) * 288 + (t) * 32), tr_read((b) + (16 * (ks) + 4) * 288 + (t) * 32))
#define G1_RD(b, i) G1_RDKT(b, (i) / 9, (i) % 9)
    const unsigned w2o = (ksl * 10368 + idx * 324 + (w * 7 + lane < 54 ? w * 7 + lane : 53)) * 128;
#define G1_STEP(xf, xn, st) do { LAS char* _b = lds + ((st) & 3) * G1_STAGE + ta; LAS char* _n = lds + (((st) + 1) & 3) * G1_STAGE + ta; \
    if ((st) >= 1) { if (lane < 7) touch((const char*)w2 + ((st) - 1) * (54 * 128), w2o, lbase + 4 * G1_STAGE); } \
    if ((st) <= 1) { wf[0] = G1_RD(_b, 0); wf[1] = G1_RD(_b, 1); wf[2] = G1_RD(_b, 2); } \
    _Pragma("unroll") for (int i = 0; i < 36; ++i) { \
      if (i + 3 < 36) wf[(i + 3) & 3] = G1_RD(_b, i + 3); \
      else if ((st) >= 1 && (st) < 6) wf[(i + 3) & 3] = G1_RD(_n, i + 3 - 36); \
      else if ((st) == 6) wf[(i + 3) & 3] = G1_RDKT(_n, i + 3 - 36, 0); \
      acc[i % 9] = __builtin_amdgcn_mfma_f32_32x32x16_bf16(wf[i & 3], __builtin_bit_cast(s16x8, xf[i / 9]), acc[i % 9], 0, 0, 0); \
      if ((st) == 0) { if ((i & 1) == 1 && (i >> 1) < 14) { const int v = i >> 1; \
        if (v < 4) G1_X(xn, 1, (v) & 3); else if (v < 9) G1_G(2, (v < 4) ? 0 : (v - 4) % 5); else G1_G(3, (v < 9) ? 0 : (v - 9) % 5); } } \
      else if ((i & 3) == 1) { const int v = i >> 2; \
        if (v < 4) G1_X(xn, (st) + 1, (v) & 3); \
        else if ((st) + 3 < 9) G1_G((st) + 3, (v - 4 < 0) ? 0 : v - 4); \
        else if ((st) == 6 && v < 8) G1_X(xc, 8, (v) & 3); } \
      SB0(); } } while (0)
    G1_WAITX(5, xa); BAR(); STAMP(); G1_STEP(xa, xb, 0);
    G1_WAITX(5, xb); BAR(); G1_STEP(xb, xa, 1);
    G1_WAITX(5, xa); BAR(); G1_STEP(xa, xb, 2);
    G1_WAITX(5, xb); BAR(); G1_STEP(xb, xa, 3);
    G1_WAITX(5, xa); BAR(); G1_STEP(xa, xb, 4);
    G1_WAITX(5, xb); BAR(); G1_STEP(xb, xa, 5);
    G1_WAITX(5, xa); BAR(); G1_STEP(xa, xb, 6);
    G1_WAITX(0, xb); G1_WAITX(0, xc); BAR(); STAMP();
#pragma unroll
    for (int i = 0; i < 5; ++i) {
      const int ci = i * 64 + lane;
      const int pr = ci / 18, ch = ci - pr * 18;
      const int r = pr ^ (((pr >> 3) & 1) << 2);
      so[i] = r * (D * 2) + ch * 16;
    }
    LAS char* b7 = lds + 3 * G1_STAGE + ta; LAS char* b8 = lds + ta;
    const int m = lane & 31, h = lane >> 5;
    const int row = mt * 256 + w * 32 + m;
    const int oct0 = ct * 18 + h;
    const unsigned dbase = (((row >> 4) * 144) * 64 + (row & 15)) * 16;
#define G1_RT(i) (((i) & 7) < 4 ? G1_RDKT(b7, (i) & 3, (i) >> 3) : G1_RDKT(b8, (i) & 3, (i) >> 3))
    float e[8];
    static_for<0, 80>([&](auto I) {
      constexpr int i = decltype(I)::value, t = i >> 3, j = i & 7;
      if constexpr (i + 3 < 72) wf[(i + 3) & 3] = G1_RT(i + 3);
      if constexpr (i < 72) {
        const u32x4 xx = (j < 4) ? xb[j & 3] : xc[j & 3];
        acc[t] = __builtin_amdgcn_mfma_f32_32x32x16_bf16(wf[i & 3], __builtin_bit_cast(s16x8, xx), acc[t], 0, 0, 0);
      }
      if constexpr (t >= 1) {
        e[j] = fast_silu(acc[t - 1][j]) * acc[t - 1][8 + j];
        if constexpr (j == 7) {
          const int oct = oct0 + 2 * (t - 1);
          gstore16_sc1<0>((u32x4){pk_bf16(e[0], e[1]), pk_bf16(e[2], e[3]), pk_bf16(e[4], e[5]), pk_bf16(e[6], e[7])},
                          dbase + ((oct >> 2) * 64 + (oct & 3) * 16) * 16, hhb);
        }
      }
      if constexpr (i >= 26 && i < 66 && (i & 3) == 2) { constexpr int v = (i - 26) >> 2; G2_G(v / 5, v % 5); }
      SB0();
    });
    STAMP();
    WAIT_V(0);
    BAR();
    STAMP();
    if (tid == 0) __hip_atomic_fetch_add((GAS unsigned*)(ws + OFF_CNT) + (16 + mt * 4 + ksl) * 64, 1u, __ATOMIC_RELAXED, __HIP_MEMORY_SCOPE_AGENT);
  }
  if (w == 0) wait_count((GAS unsigned*)(ws + OFF_CNT) + (16 + (mt2 >> 1) * 4 + ksl) * 64, 8u);
  const char* xs0 = hhb + ((size_t)(mt2 * 8 + mq * 2 + kg) * 144 + ksl * 36 + kg * 2) * 1024;
  const char* xs1 = hhb + ((size_t)(mt2 * 8 + mq * 2 + (kg ^ 1)) * 144 + ksl * 36 + kg * 2) * 1024;
  u32x4 xr[3][4];
#define G2_X(st, i) gload16_sc1<((i) >> 1) * 1024>(xr[(st) % 3][i], xvo, (((i) & 1) ? xs1 : xs0) + (st) * 4096)
  const int g = lane >> 4;
  unsigned tb[2][2];
  {
    const int q = (lane & 15) >> 2, p4 = lane & 3;
#pragma unroll
    for (int t = 0; t < 2; ++t) { tb[0][t] = lbase + G1_STAGE + (kg * 64 + 8 * g + ((4 * t) ^ (4 * (g & 1))) + q) * 288 + p4 * 8; tb[1][t] = tb[0][t] + G1_STAGE; }
    pinu(tb[0][0], tb[0][1]); pinu(tb[1][0], tb[1][1]);
  }
  f32x4 acc[2][9];
#pragma unroll
  for (int i = 0; i < 2; ++i)
#pragma unroll
    for (int t = 0; t < 9; ++t) { acc[i][t] = (f32x4){0.f, 0.f, 0.f, 0.f}; asm volatile("" : "+v"(acc[i][t])); }
  constexpr int PF2 = 5, RG2 = 6;
  s16x8 wf[RG2];
#define G2_RD(st, i) cat8(tr_read((LAS char*)(size_t)(tb[(st) & 1][0] + ((i) / 9) * (32 * 288) + ((i) % 9) * 32)), \
                          tr_read((LAS char*)(size_t)(tb[(st) & 1][1] + ((i) / 9) * (32 * 288) + ((i) % 9) * 32)))
  static_for<0, PF2>([&](auto I) { constexpr int i = decltype(I)::value; wf[i % RG2] = G2_RD(0, i); });
  BAR();
  STAMP();
  static_for<0, 8>([&](auto I) { constexpr int i = decltype(I)::value; G2_X(i / 4, i % 4); });
  wait4<4>(xr[0]);
  STAMP();
  const int fm = w & 1, fs = w >> 1;
  const int T = mt2 * 8 + 2 * ksl + fm;
  const int col = nq * 144 + 32 * fs + 4 * g;
  u32x2 x1v[3]; f32x4 gb[3];
  auto issue_p5 = [&]() {
    const char* xp = ws + OFF_X1 + (size_t)(T * 36 + nq * 9) * 512;
    const unsigned l8 = lane * 8;
    gload8<0>(x1v[0], l8 + fs * 1024, xp); gload8<512>(x1v[1], l8 + fs * 1024, xp); gload8<0>(x1v[2], l8 + 4096, xp);
    const unsigned c0 = col * 4, c8 = (nq * 144 + 128 + 4 * g) * 4;
    const char* mp = ws + OFF_GB;
    gload16<0>(gb[0], c0, mp); gload16<64>(gb[1], c0, mp); gload16<0>(gb[2], c8, mp);
  };
  static_for<0, 162>([&](auto U) {
    constexpr int u = decltype(U)::value, st = u / 18, i = u % 18;
    if constexpr (i == 12 && st < 8) {
      wait4<g2_issued_before(u) - 9 * (st + 2)>(xr[(st + 1) % 3]);
      BAR();
      if constexpr (st >= 1) {
        constexpr unsigned d = (((st + 2) & 3) >= 2) ? 2 * G1_STAGE : (unsigned)(-2 * G1_STAGE);
        tb[(st + 1) & 1][0] += d; tb[(st + 1) & 1][1] += d;
        pinu(tb[(st + 1) & 1][0], tb[(st + 1) & 1][1]);
      }
    }
    constexpr int op = g2_op_at(u);
    if constexpr (op >= 0) { if constexpr (op % 9 < 5) G2_G(op / 9, op % 9); else G2_X(op / 9, op % 9 - 5); }
    if constexpr (u == 140) issue_p5();
    if constexpr (u + PF2 < 162) wf[(u + PF2) % RG2] = G2_RD((u + PF2) / 18, (u + PF2) % 18);
    MFMA16A(wf[u % RG2], xr[st % 3][2 * (i / 9)], acc[0][i % 9]);
    MFMA16A(wf[u % RG2], xr[st % 3][2 * (i / 9) + 1], acc[1][i % 9]);
    SB0();
  });
  asm volatile("s_nop 7" : "+v"(acc[0][0]), "+v"(acc[0][1]), "+v"(acc[0][2]), "+v"(acc[0][3]), "+v"(acc[0][4]), "+v"(acc[0][5]), "+v"(acc[0][6]), "+v"(acc[0][7]), "+v"(acc[0][8]),
               "+v"(acc[1][0]), "+v"(acc[1][1]), "+v"(acc[1][2]), "+v"(acc[1][3]), "+v"(acc[1][4]), "+v"(acc[1][5]), "+v"(acc[1][6]), "+v"(acc[1][7]), "+v"(acc[1][8]) :: "memory");
  BAR();
  STAMP();
  LAS f32x4* ex = (LAS f32x4*)lds;
#pragma unroll
  for (int t = 0; t < 9; ++t) ex[(w * 9 + t) * 64 + lane] = acc[1][t];
  WAIT_L(0);
  BAR();
  STAMP();
  {
    f32x4 v[9];
#pragma unroll
    for (int t = 0; t < 9; ++t) v[t] = acc[0][t] + ex[((w ^ 4) * 9 + t) * 64 + lane];
    if (mq == ksl) {
      LAS f32x4* own = ex + (72 + kg * 9) * 64 + lane;
#pragma unroll
      for (int t = 0; t < 9; ++t) own[t * 64] = v[t];
    } else {
      const char* dst = ws + OFF_PART + ((size_t)(((ksl * 16 + mt2) * 4 + nq) * 8 + mq * 2 + kg) * 5) * 1024;
      static_for<0, 4>([&](auto I) { constexpr int i = decltype(I)::value;
        gstore16_sc1<i * 1024>((u32x4){pk_bf16(v[2 * i][0], v[2 * i][1]), pk_bf16(v[2 * i][2], v[2 * i][3]), pk_bf16(v[2 * i + 1][0], v[2 * i + 1][1]), pk_bf16(v[2 * i + 1][2], v[2 * i + 1][3])}, xvo, dst); });
      gstore8_sc1((u32x2){pk_bf16(v[8][0], v[8][1]), pk_bf16(v[8][2], v[8][3])}, xvo + 4096, dst);
    }
  }
  asm volatile("s_waitcnt vmcnt(0) lgkmcnt(0)" : "+v"(x1v[0]), "+v"(x1v[1]), "+v"(x1v[2]), "+v"(gb[0]), "+v"(gb[1]), "+v"(gb[2]) :: "memory");
  BAR();
  STAMP();
  GAS unsigned* cnt4 = (GAS unsigned*)(ws + OFF_CNT) + (64 + mt2 * 4 + nq) * 64;
  if (tid == 448) __hip_atomic_fetch_add(cnt4, 1u, __ATOMIC_RELAXED, __HIP_MEMORY_SCOPE_AGENT);
  f32x4 y[3];
  y[0] = ex[(72 + fm * 9 + 2 * fs) * 64 + lane]; y[1] = ex[(72 + fm * 9 + 2 * fs + 1) * 64 + lane]; y[2] = ex[(72 + fm * 9 + 8) * 64 + lane];
  if (w == 7) wait_count(cnt4, 4u);
  BAR();
  STAMP();
  {
    u32x4 pa[3], pb[3];
#pragma unroll
    for (int k = 0; k < 3; ++k) {
      const int src = (ksl + 1 + k) & 3;
      const char* p = ws + OFF_PART + ((size_t)(((src * 16 + mt2) * 4 + nq) * 8 + 2 * ksl + fm) * 5) * 1024;
      gload16_sc1<0>(pa[k], xvo + fs * 1024, p);
      if (fs == 0) gload16_sc1<0>(pb[k], xvo + 4096, p);
    }
    asm volatile("s_waitcnt vmcnt(0)" : "+v"(pa[0]), "+v"(pa[1]), "+v"(pa[2]), "+v"(pb[0]), "+v"(pb[1]), "+v"(pb[2]) :: "memory");
    STAMP();
#pragma unroll
    for (int k = 0; k < 3; ++k) {
      y[0] += (f32x4){bf_lo(pa[k][0]), bf_hi(pa[k][0]), bf_lo(pa[k][1]), bf_hi(pa[k][1])};
      y[1] += (f32x4){bf_lo(pa[k][2]), bf_hi(pa[k][2]), bf_lo(pa[k][3]), bf_hi(pa[k][3])};
      y[2] += (f32x4){bf_lo(pb[k][0]), bf_hi(pb[k][0]), bf_lo(pb[k][1]), bf_hi(pb[k][1])};
    }
    const int row = 32 * (T >> 1) + 2 * (lane & 15) + (T & 1);
    u16* op = out + (size_t)row * D;
#pragma unroll
    for (int k = 0; k < 3; ++k) {
      float r0 = y[k][0], r1 = y[k][1], r2 = y[k][2], r3 = y[k][3];
      rb2(r0, r1); rb2(r2, r3);
      r0 *= gb[k][0]; r1 *= gb[k][1]; r2 *= gb[k][2]; r3 *= gb[k][3];
      rb2(r0, r1); rb2(r2, r3);
      const u32x2 o = (u32x2){pk_bf16(r0 + bf_lo(x1v[k][0]), r1 + bf_hi(x1v[k][0])), pk_bf16(r2 + bf_lo(x1v[k][1]), r3 + bf_hi(x1v[k][1]))};
      if (k < 2) *(u32x2*)(op + col + 16 * k) = o;
      else if (fs == 0) *(u32x2*)(op + nq * 144 + 128 + 4 * g) = o;
    }
  }
  STAMP();
  STAMP_OUT(3);
}

extern "C" __global__ void k_end_marker() {}

extern "C" void kernel_launch(void* const* d_in, const int* in_sizes, int n_in, void* d_out, int out_size,
                              void* d_ws, size_t ws_size, hipStream_t stream) {
  const u16* x = (const u16*)d_in[0];
  const u16* wqkv = (const u16*)d_in[1];
  const u16* wo = (const u16*)d_in[2];
  const u16* w1 = (const u16*)d_in[4];
  const u16* w3 = (const u16*)d_in[5];
  const u16* w2 = (const u16*)d_in[6];
  const u16* c = (const u16*)d_in[7];
  const u16* wmod = (const u16*)d_in[8];
  char* ws = (char*)d_ws;
  (void)hipFuncSetAttribute((const void*)k12_attn, hipFuncAttributeMaxDynamicSharedMemorySize, K1_LDS);
  (void)hipFuncSetAttribute((const void*)k345_mlp, hipFuncAttributeMaxDynamicSharedMemorySize, G1_LDS);
  k0_prep<<<K0_F, 384, 0, stream>>>(x, c, wmod, wqkv, wo, ws, w1, w3, w2, (const u16*)d_out);
  k12_attn<<<256, 576, K1_LDS, stream>>>(ws, x, w1, w3);
  k345_mlp<<<256, 512, G1_LDS, stream>>>(ws, w1, w3, w2, (u16*)d_out);
}
```

```cpp
#include <hip/hip_runtime.h>
#include <hip/hip_bf16.h>
#include <stdint.h>

typedef unsigned short u16;
typedef short s16x4 __attribute__((ext_vector_type(4)));
typedef short s16x8 __attribute__((ext_vector_type(8)));
typedef float f32x2 __attribute__((ext_vector_type(2)));
typedef float f32x4 __attribute__((ext_vector_type(4)));
typedef float f32x16 __attribute__((ext_vector_type(16)));
typedef unsigned u32x2 __attribute__((ext_vector_type(2)));
typedef unsigned u32x4 __attribute__((ext_vector_type(4)));
typedef __bf16 bf16x2 __attribute__((ext_vector_type(2)));
#define LAS __attribute__((address_space(3)))
#define GAS __attribute__((address_space(1)))

constexpr int S = 2048, D = 576, F = 4608;
constexpr size_t OFF_MOD = 0;
constexpr size_t OFF_GB = 45056;
constexpr size_t OFF_XN = 49152;
constexpr size_t OFF_O = OFF_XN + (size_t)S * D * 2;
constexpr size_t OFF_X1 = OFF_O + (size_t)S * D * 2;
constexpr size_t OFF_H2 = OFF_X1 + (size_t)S * D * 4;
constexpr size_t OFF_HH = OFF_H2 + (size_t)S * D * 2;
constexpr size_t OFF_PART = OFF_HH + (size_t)S * F * 2;
constexpr size_t OFF_WQ = OFF_PART + (size_t)4 * 512 * 5 * 1024;
constexpr size_t OFF_BIAS = OFF_WQ + (size_t)16 * 126 * 1024;
constexpr size_t OFF_WO = OFF_BIAS + (size_t)16 * 8192;
constexpr size_t OFF_XCH = OFF_WO + (size_t)36 * 18 * 1024;
constexpr size_t OFF_CNT = OFF_XCH + (size_t)64 * 4 * 64 * 8;
constexpr size_t OFF_END = OFF_CNT + 32768;
constexpr unsigned XCH_TAG = 0x5a17c3e1u;

__device__ __forceinline__ float bf_lo(unsigned v) { return __uint_as_float(v << 16); }
__device__ __forceinline__ float bf_hi(unsigned v) { return __uint_as_float(v & 0xffff0000u); }
__device__ __forceinline__ unsigned pk_bf16(float lo, float hi) {
  bf16x2 r; r.x = (__bf16)lo; r.y = (__bf16)hi;
  return __builtin_bit_cast(unsigned, r);
}
__device__ __forceinline__ float rb(float f) { return bf_lo(pk_bf16(f, 0.f)); }
template <int CTRL> __device__ __forceinline__ float dppf(float x) { return __int_as_float(__builtin_amdgcn_update_dpp(0, __float_as_int(x), CTRL, 0xf, 0xf, true)); }
__device__ __forceinline__ float sum16(float x) { auto r = __builtin_amdgcn_permlane16_swap(__float_as_uint(x), __float_as_uint(x), false, false); return __uint_as_float(r[0]) + __uint_as_float(r[1]); }
__device__ __forceinline__ float sum32(float x) { auto r = __builtin_amdgcn_permlane32_swap(__float_as_uint(x), __float_as_uint(x), false, false); return __uint_as_float(r[0]) + __uint_as_float(r[1]); }
__device__ __forceinline__ float max16(float x) { auto r = __builtin_amdgcn_permlane16_swap(__float_as_uint(x), __float_as_uint(x), false, false); return fmaxf(__uint_as_float(r[0]), __uint_as_float(r[1])); }
__device__ __forceinline__ float max32(float x) { auto r = __builtin_amdgcn_permlane32_swap(__float_as_uint(x), __float_as_uint(x), false, false); return fmaxf(__uint_as_float(r[0]), __uint_as_float(r[1])); }
__device__ __forceinline__ void rb2(float& a, float& b) { const unsigned p = pk_bf16(a, b); a = bf_lo(p); b = bf_hi(p); }
__device__ __forceinline__ float fast_silu(float g) { return g * __builtin_amdgcn_rcpf(1.f + __builtin_amdgcn_exp2f(g * -1.44269504f)); }

#define WAIT_V(n) asm volatile("s_waitcnt vmcnt(%0)" ::"n"(n) : "memory")
#define WAIT_L(n) asm volatile("s_waitcnt lgkmcnt(%0)" ::"n"(n) : "memory")
#define BAR() do { asm volatile("" ::: "memory"); __builtin_amdgcn_s_barrier(); asm volatile("" ::: "memory"); } while (0)
template <int IMM, class T> __device__ __forceinline__ void gload16(T& dst, unsigned voff, const void* sbase) {
  asm volatile("global_load_dwordx4 %0, %1, %2 offset:%3" : "=v"(dst) : "v"(voff), "s"(sbase), "n"(IMM) : "memory");
}
template <int IMM, class T> __device__ __forceinline__ void gload8(T& dst, unsigned voff, const void* sbase) {
  asm volatile("global_load_dwordx2 %0, %1, %2 offset:%3" : "=v"(dst) : "v"(voff), "s"(sbase), "n"(IMM) : "memory");
}
#define GLOAD16(dst, voff, sbase, imm) asm volatile("global_load_dwordx4 %0, %1, %2 offset:%3" : "=v"(dst) : "v"(voff), "s"(sbase), "n"(imm) : "memory")

__device__ __forceinline__ void glds16(const void* sbase, unsigned voff, unsigned ldsaddr) {
  asm volatile("s_mov_b32 m0, %2\n\ts_nop 0\n\tglobal_load_lds_dwordx4 %0, %1" :: "v"(voff), "s"(sbase), "s"(ldsaddr) : "memory");
}
__device__ __forceinline__ void glds16_sc1(const void* sbase, unsigned voff, unsigned ldsaddr) {
  asm volatile("s_mov_b32 m0, %2\n\ts_nop 0\n\tglobal_load_lds_dwordx4 %0, %1 sc1" :: "v"(voff), "s"(sbase), "s"(ldsaddr) : "memory");
}
#define SB0() __builtin_amdgcn_sched_barrier(0)
template <int I> struct IC { static constexpr int value = I; };
template <int B, int E, class Fn> __device__ __forceinline__ void static_for(Fn&& f) {
  if constexpr (B < E) { f(IC<B>{}); static_for<B + 1, E>(f); }
}
__device__ __forceinline__ void touch(const void* sbase, unsigned voff, unsigned ldsaddr) {
  asm volatile("s_mov_b32 m0, %2\n\ts_nop 0\n\tglobal_load_lds_dword %0, %1" :: "v"(voff), "s"(sbase), "s"(ldsaddr) : "memory");
}
__device__ __forceinline__ void pad2(u32x4& r) { asm volatile("s_nop 1" : "+v"(r)); }
template <class A, class B> __device__ __forceinline__ void MFMA16A(const A& a, const B& b, f32x4& c) {
  asm volatile("v_mfma_f32_16x16x32_bf16 %0, %1, %2, %0" : "+v"(c) : "v"(a), "v"(b));
}
#define STAMP_DECL
#define STAMP()
#define STAMP_OUT(kid)
__device__ __forceinline__ s16x4 tr_read(LAS char* p) { return __builtin_amdgcn_ds_read_tr16_b64_v4i16((LAS s16x4*)p); }
__device__ __forceinline__ s16x8 cat8(s16x4 a, s16x4 b) { return __builtin_shufflevector(a, b, 0, 1, 2, 3, 4, 5, 6, 7); }

__device__ __forceinline__ unsigned perm_lo(unsigned a, unsigned b) { return __builtin_amdgcn_perm(b, a, 0x05040100u); }
__device__ __forceinline__ unsigned perm_hi(unsigned a, unsigned b) { return __builtin_amdgcn_perm(b, a, 0x07060302u); }

constexpr int K0_A = 162, K0_B = K0_A + 128, K0_C = K0_B + 81, K0_D = K0_C + 27, K0_E = 400, K0_F = K0_E + 8 * 4;
extern "C" __global__ __launch_bounds__(384) void k0_prep(const u16* __restrict__ x, const u16* __restrict__ c, const u16* __restrict__ wmod,
                                               const u16* __restrict__ wqkv, const u16* __restrict__ wo, char* __restrict__ ws,
                                               const u16* __restrict__ w1, const u16* __restrict__ w3, const u16* __restrict__ w2,
                                               const u16* __restrict__ outp) {
  __shared__ __attribute__((aligned(16))) float red[6 * 64];
  const int b = blockIdx.x, tid = threadIdx.x;
  const int w = tid >> 6, lane = tid & 63;
  STAMP_DECL; STAMP();
  if (b < K0_A) {
    const int kt = b / 54, cb = b - kt * 54;
    const int r0 = kt * 192 + (tid >> 3), col = cb * 64 + (tid & 7) * 8;
    u32x4 wv[4]; float sv[4];
#pragma unroll
    for (int i = 0; i < 4; ++i) wv[i] = *(const u32x4*)(wmod + (size_t)(r0 + 48 * i) * (6 * D) + col);
#pragma unroll
    for (int i = 0; i < 4; ++i) sv[i] = rb(fast_silu(bf_lo(c[r0 + 48 * i])));
    float a[8] = {0, 0, 0, 0, 0, 0, 0, 0};
#pragma unroll
    for (int i = 0; i < 4; ++i)
#pragma unroll
      for (int j = 0; j < 4; ++j) { a[2 * j] += sv[i] * bf_lo(wv[i][j]); a[2 * j + 1] += sv[i] * bf_hi(wv[i][j]); }
#pragma unroll
    for (int j = 0; j < 8; ++j) { a[j] += dppf<0x128>(a[j]); a[j] = sum32(sum16(a[j])); }
    if (lane < 8) {
      *(f32x4*)(red + w * 64 + lane * 8) = (f32x4){a[0], a[1], a[2], a[3]};
      *(f32x4*)(red + w * 64 + lane * 8 + 4) = (f32x4){a[4], a[5], a[6], a[7]};
    }
    __syncthreads();
    if (tid < 64) {
      float s = 0.f;
#pragma unroll
      for (int i = 0; i < 6; ++i) s += red[i * 64 + tid];
      ((float*)(ws + OFF_MOD))[kt * 3456 + cb * 64 + tid] = s;
    }
  } else if (b < K0_B) {
    const int i = lane & 15, g = lane >> 4;
    const int T = b - K0_A;
    const int row = 32 * (T >> 1) + 2 * i + (T & 1);
    float v[3][8];
#pragma unroll
    for (int s = 0; s < 3; ++s) {
      const u32x4 a = *(const u32x4*)(x + (size_t)row * D + ((w * 3 + s) * 4 + g) * 8);
#pragma unroll
      for (int j = 0; j < 4; ++j) { v[s][2 * j] = bf_lo(a[j]); v[s][2 * j + 1] = bf_hi(a[j]); }
    }
    float sm = 0.f;
#pragma unroll
    for (int s = 0; s < 3; ++s) sm += ((v[s][0] + v[s][1]) + (v[s][2] + v[s][3])) + ((v[s][4] + v[s][5]) + (v[s][6] + v[s][7]));
    const float mw = sum32(sum16(sm)) * (1.f / 96.f);
    float q0 = 0.f, q1 = 0.f;
#pragma unroll
    for (int s = 0; s < 3; ++s)
#pragma unroll
      for (int j = 0; j < 4; ++j) { const float d0 = v[s][2 * j] - mw, d1 = v[s][2 * j + 1] - mw; q0 += d0 * d0; q1 += d1 * d1; }
    const float q = sum32(sum16(q0 + q1));
    if (g == 0) *(f32x2*)(red + (w * 16 + i) * 2) = (f32x2){mw, q};
    __syncthreads();
    f32x2 p[6];
    float mean = 0.f;
#pragma unroll
    for (int k = 0; k < 6; ++k) { p[k] = *(const f32x2*)(red + (k * 16 + i) * 2); mean += p[k][0]; }
    mean *= (1.f / 6.f);
    float var = 0.f;
#pragma unroll
    for (int k = 0; k < 6; ++k) { const float dl = p[k][0] - mean; var += p[k][1] + 96.f * dl * dl; }
    var *= (1.f / D);
    const float mr = rb(mean);
#pragma unroll
    for (int s = 0; s < 3; ++s)
#pragma unroll
      for (int j = 0; j < 4; ++j) { v[s][2 * j] -= mr; v[s][2 * j + 1] -= mr; rb2(v[s][2 * j], v[s][2 * j + 1]); }
    const float rs = __builtin_amdgcn_rcpf(rb(__builtin_amdgcn_sqrtf(rb(rb(var) + 1e-5f))));
#pragma unroll
    for (int s = 0; s < 3; ++s) {
      u32x4 o;
#pragma unroll
      for (int j = 0; j < 4; ++j) o[j] = pk_bf16(v[s][2 * j] * rs, v[s][2 * j + 1] * rs);
      *(u32x4*)(ws + OFF_XN + ((size_t)(T * 18 + w * 3 + s) * 64 + lane) * 16) = o;
    }
  } else if (b < K0_C) {
    const int task = (b - K0_B) * 384 + tid;
    const int kq = task / 432, nq4 = task - kq * 432;
    u32x2 r[8];
#pragma unroll
    for (int i = 0; i < 8; ++i) r[i] = *(const u32x2*)(wqkv + (size_t)(kq * 8 + i) * (3 * D) + nq4 * 4);
    const int part = nq4 / 144, nh = nq4 - part * 144, ks = kq >> 2, ko = kq & 3;
#pragma unroll
    for (int e = 0; e < 4; ++e) {
      const int n = nh * 4 + e, head = n / 18, d = n - head * 18, hp = head >> 1, h = head & 1;
      int tile, idx;
      if (d < 16) { tile = (part == 0) ? 5 + h : (part == 1 ? h : 3 + h); idx = d; }
      else { tile = 2; idx = (part == 2) ? 8 + 2 * h + (d - 16) : 4 * h + 2 * part + (d - 16); }
      char* dst = ws + OFF_WQ + ((hp * 18 + ks) * 7 + tile) * 1024 + (ko * 16 + idx) * 16;
      u32x4 f;
#pragma unroll
      for (int i = 0; i < 4; ++i) f[i] = (e & 1) ? perm_hi(r[2 * i][e >> 1], r[2 * i + 1][e >> 1]) : perm_lo(r[2 * i][e >> 1], r[2 * i + 1][e >> 1]);
      *(u32x4*)dst = f;
      if (part == 2 && d >= 16) *(u32x4*)(dst + 64) = (u32x4){0u, 0u, 0u, 0u};
    }
  } else if (b >= K0_E) {
    const int job = (b - K0_E) >> 3;
    const unsigned scr = (unsigned)(size_t)(LAS float*)red;
    if (job < 2) { const unsigned o = (job * 384 + tid) * 65536u; if (o < OFF_END) touch(ws, o, scr); }
    else if (job == 2) {
      if (tid < 81) { touch(w1, tid * 65536u, scr); touch(w3, tid * 65536u, scr); touch(w2, tid * 65536u, scr); }
      if (tid < 36) touch(outp, tid * 65536u, scr);
    } else {
      unsigned long long pc; int o0, o1;
      asm volatile("s_getpc_b64 %0\n.Lpc_%=:\n\ts_mov_b32 %1, k12_attn-.Lpc_%=\n\ts_mov_b32 %2, k_end_marker-.Lpc_%=" : "=&s"(pc), "=&s"(o0), "=&s"(o1));
      const int o = ((job - 3) * 384 + tid) * 128;
      if (o0 > 0 && o < o1 - o0) touch((const void*)pc, (unsigned)(o0 + o) & ~3u, scr);
    }
  } else if (b < K0_D) {
    const int task = (b - K0_C) * 384 + tid;
    const int nh = task % 144, ug = task / 144, u = ug >> 2, g = ug & 3;
    u32x2 r[8];
#pragma unroll
    for (int j = 0; j < 8; ++j) {
      const int krow = (u < 16) ? 36 * u + 18 * (j >> 2) + 4 * g + (j & 3) : 36 * (8 * (u - 16) + 2 * g + (j >> 2)) + 18 * ((j >> 1) & 1) + 16 + (j & 1);
      r[j] = *(const u32x2*)(wo + (size_t)krow * D + nh * 4);
    }
    char* dst = ws + OFF_WO + ((size_t)((nh >> 2) * 18 + u) * 64 + g * 16 + (nh & 3) * 4) * 16;
#pragma unroll
    for (int e = 0; e < 4; ++e) {
      u32x4 f;
#pragma unroll
      for (int i = 0; i < 4; ++i) f[i] = (e & 1) ? perm_hi(r[2 * i][e >> 1], r[2 * i + 1][e >> 1]) : perm_lo(r[2 * i][e >> 1], r[2 * i + 1][e >> 1]);
      *(u32x4*)(dst + e * 16) = f;
    }
    if (task < 64 * 4 * 64 / 2 + 2048) *(u32x4*)(ws + OFF_XCH + (size_t)task * 16) = (u32x4){0u, 0u, 0u, 0u};
  }
  STAMP();
}

constexpr int K1_W = 126 * 1024;
constexpr int K1_CONST = K1_W;
constexpr int K1_C2 = K1_CONST + 4608;
constexpr int K1_ST = K1_C2 + 1792;
constexpr int K1_FIN = K1_ST + 2304;
constexpr int K1_SCR = K1_FIN + 256;
constexpr int K1_LDS = K1_SCR + 256;
constexpr int K1_EK = 0, K1_EV = 9 * 2 * 1024, K1_EL = K1_EV + 9 * 2 * 512;
constexpr int K1_O = 36 * 1024;
constexpr int k1_r(int p) { return p < 0 ? 0 : (21 * (p + 1) + 8) / 9; }
constexpr int k1_cum(int p) { return 6 + 3 * (p + 1 < 5 ? p + 1 : 5) + k1_r(p); }
constexpr int K1_NOPS = 36 + 18 + 5;
constexpr int k1_op(int n) {
  if (n < 3) return 96;
  if (n < 6) return n - 3;
  for (int p = 0; p < 6; ++p) {
    const int lo = p ? k1_cum(p - 1) : 6;
    if (n < k1_cum(p)) { const int k = n - lo; return (p < 5 && k < 3) ? 3 * p + 3 + k : 32 + k1_r(p - 1) + k - (p < 5 ? 3 : 0); }
  }
  return n == 35 ? 97 : 64 + n - 36;
}
constexpr int k1_pos(int n) {
  if (n < k1_cum(0)) return -1;
  if (n < k1_cum(1)) return 0;
  if (n < k1_cum(2)) return 1 + (n - k1_cum(1)) / 3;
  for (int p = 3; p < 6; ++p) if (n < k1_cum(p)) return 3 * (p - 2) + (n - k1_cum(p - 1)) / 2;
  return n == 35 ? 10 : 10 + (n - 36) / 3;
}
constexpr int k1_before(int s) { int c = 0; for (int n = 0; n < K1_NOPS; ++n) if (k1_pos(n) < s) ++c; return c; }

struct Cst { f32x4 s0, s1, g0, g1; };
__device__ __forceinline__ void rdc(Cst& c, const LAS float* cs) {
  c.s0 = *(const LAS f32x4*)cs; c.s1 = *(const LAS f32x4*)(cs + 4); c.g0 = *(const LAS f32x4*)(cs + 576); c.g1 = *(const LAS f32x4*)(cs + 580);
}
__device__ __forceinline__ float dot2(unsigned x, float gw, float s) {
  float r; asm("v_dot2_f32_bf16 %0, %1, %2, %3" : "=v"(r) : "v"(x), "v"(gw), "v"(s)); return r;
}
__device__ __forceinline__ void modc(u32x4& xv, const Cst& c) {
  asm volatile("" : "+v"(xv));
  const u32x4 v = xv;
  float h[8];
  h[0] = dot2(v[0], c.g0[0], c.s0[0]); h[1] = dot2(v[0], c.g0[1], c.s0[1]); h[2] = dot2(v[1], c.g0[2], c.s0[2]); h[3] = dot2(v[1], c.g0[3], c.s0[3]);
  h[4] = dot2(v[2], c.g1[0], c.s1[0]); h[5] = dot2(v[2], c.g1[1], c.s1[1]); h[6] = dot2(v[3], c.g1[2], c.s1[2]); h[7] = dot2(v[3], c.g1[3], c.s1[3]);
  asm volatile("s_nop 2" : "+v"(h[0]), "+v"(h[1]), "+v"(h[2]), "+v"(h[3]), "+v"(h[4]), "+v"(h[5]), "+v"(h[6]), "+v"(h[7]));
  u32x4 r;
  r[0] = pk_bf16(h[0], h[1]); r[1] = pk_bf16(h[2], h[3]); r[2] = pk_bf16(h[4], h[5]); r[3] = pk_bf16(h[6], h[7]);
  pad2(r);
  xv = r;
}
template <int N> __device__ __forceinline__ void wait3(u32x4& a, u32x4& b, u32x4& c) {
  asm volatile("s_waitcnt vmcnt(%3)" : "+v"(a), "+v"(b), "+v"(c) : "n"(N) : "memory");
}
__device__ __forceinline__ void pin(u32x4& a) { asm volatile("" : "+v"(a)); }

extern "C" __global__ __launch_bounds__(576) void k12_attn(char* __restrict__ ws, const u16* __restrict__ x, const u16* __restrict__ w1, const u16* __restrict__ w3) {
  extern __shared__ __attribute__((aligned(16))) char smem[];
  LAS char* lds = (LAS char*)smem;
  const unsigned lbase = (unsigned)(size_t)lds;
  const int b = blockIdx.x, xcd = b & 7, idx = b >> 3;
  const int rt = (xcd & 3) * 4 + (idx & 3), hp = (xcd >> 2) * 8 + (idx >> 2);
  const int tid = threadIdx.x, lane = tid & 63;
  const int w = __builtin_amdgcn_readfirstlane(tid >> 6);
  const int li = lane & 15, g = lane >> 4;
  const bool halo = (w == 8);
  STAMP_DECL; STAMP();
  const unsigned l16 = lane * 16;
  const int tb = halo ? (rt > 0 ? 8 * rt - 2 : 0) : 8 * rt + w;
  const char* xsrc = ws + OFF_XN + (size_t)tb * (18 * 1024);
  const unsigned xvo = halo ? (unsigned)((li >> 3) * (18 * 1024) + (g * 16 + 8 + (li & 7)) * 16) : l16;
  const char* wsrc = ws + OFF_WQ + (size_t)hp * K1_W + w * 1024;
  const int rt2 = 4 * rt + (hp >> 2), nq = hp & 3;
  const int nt = nq * 9 + w;
  const char* wsrc2 = ws + OFF_WO + (size_t)nt * (18 * 1024);
  u32x4 xf[18], wo[18];
  u32x2 xr[2]; f32x4 mq[3];
  auto issue = [&](auto N) {
    constexpr int op = k1_op(decltype(N)::value);
    if constexpr (op < 32) { const char* q = xsrc + (op & ~3) * 1024; gload16<(op & 3) * 1024>(xf[op], xvo, q); }
    else if constexpr (op < 64) glds16(wsrc + (op - 32) * 9216, l16, lbase + (w + 9 * (op - 32)) * 1024);
    else if constexpr (op < 82) { constexpr int i = op - 64; const char* q = wsrc2 + (i & ~3) * 1024; gload16<(i & 3) * 1024>(wo[i], l16, q); }
    else if constexpr (op < 84) gload8<(op - 82) * 1152>(xr[op - 82], (unsigned)(((rt2 * 32 + 2 * li) * D + nt * 16 + 4 * g) * 2), x);
    else if constexpr (op < 87) {
      const int t = tid < 144 ? tid : 143, vec = t / 36, c4 = t - vec * 36;
      gload16<0>(mq[op - 84], (unsigned)(((2 + vec) * D + nq * 144 + c4 * 4) * 4), ws + OFF_MOD + (op - 84) * 3456 * 4);
    }
    else {
      const int ln = idx * 108 + (w & 3) * 27 + (lane < 27 ? lane : 26);
      touch((w & 4) ? w3 : w1, (ln / 18) * (F * 2) + (xcd >> 1) * 2304 + (ln % 18) * 128, lbase + K1_SCR);
    }
  };
  f32x2 mp[3];
  {
    const unsigned mo = tid * 8;
    const char* m0 = ws + OFF_MOD; const char* m1 = m0 + 3456 * 4; const char* m2 = m1 + 3456 * 4;
    asm volatile("global_load_dwordx2 %0, %1, %2" : "=v"(mp[0]) : "v"(mo), "s"(m0) : "memory");
    asm volatile("global_load_dwordx2 %0, %1, %2" : "=v"(mp[1]) : "v"(mo), "s"(m1) : "memory");
    asm volatile("global_load_dwordx2 %0, %1, %2" : "=v"(mp[2]) : "v"(mo), "s"(m2) : "memory");
  }
  static_for<3, k1_cum(0)>(issue);
  asm volatile("s_waitcnt vmcnt(%3)" : "+v"(mp[0]), "+v"(mp[1]), "+v"(mp[2]) : "n"(k1_cum(0) - 3) : "memory");
  {
    float m0 = (mp[0][0] + mp[1][0]) + mp[2][0], m1 = (mp[0][1] + mp[1][1]) + mp[2][1];
    rb2(m0, m1);
    if (tid >= 288) { m0 += 1.f; m1 += 1.f; rb2(m0, m1); m0 = __uint_as_float(__float_as_uint(m0) >> 16); }
    *(LAS f32x2*)(lds + K1_CONST + tid * 8) = (f32x2){m0, m1};
    WAIT_L(0);
  }
  BAR();
  const LAS float* cs = (const LAS float*)(lds + K1_CONST) + g * 8;
  Cst cst;
  {
    Cst c1, c2;
    rdc(cst, cs); rdc(c1, cs + 32); rdc(c2, cs + 64);
    wait3<k1_cum(0) - 6>(xf[0], xf[1], xf[2]);
    modc(xf[0], cst); modc(xf[1], c1); modc(xf[2], c2);
  }
  rdc(cst, cs + 96);
  f32x4 acc[7];
#pragma unroll
  for (int t = 0; t < 7; ++t) { acc[t] = (f32x4){0.f, 0.f, 0.f, 0.f}; asm volatile("" : "+v"(acc[t])); }
  unsigned wa = lbase + l16;
  s16x8 wf[7];
  auto rdw = [&](auto Sx, auto T0, auto T1) {
    constexpr int s = decltype(Sx)::value, off = (s % 6) * 7168;
#pragma unroll
    for (int t = decltype(T0)::value; t < decltype(T1)::value; ++t) wf[t] = *(const LAS s16x8*)(size_t)(wa + off + t * 1024);
  };
  wait3<0>(xf[3], xf[4], xf[5]);
  BAR();
  STAMP();
  rdw(IC<0>{}, IC<0>{}, IC<7>{});
  static_for<0, 18>([&](auto Sx) {
    constexpr int s = decltype(Sx)::value, p = s / 3;
    if constexpr (s % 3 == 2 && p < 5) {
      if constexpr (p < 4) wait3<k1_before(s) - k1_cum(p + 1)>(xf[3 * p + 6], xf[3 * p + 7], xf[3 * p + 8]);
      else WAIT_V(k1_before(s) - k1_cum(5));
      BAR();
      STAMP();
    }
    if constexpr (s == 5 || s == 11) { wa += 6 * 7168; asm volatile("" : "+v"(wa)); }
    if constexpr (s == 0) static_for<k1_before(0), k1_before(1)>(issue);
    MFMA16A(wf[0], xf[s], acc[0]); MFMA16A(wf[1], xf[s], acc[1]); MFMA16A(wf[2], xf[s], acc[2]); MFMA16A(xf[s], wf[3], acc[3]);
    if constexpr (s < 17) rdw(IC<s + 1>{}, IC<0>{}, IC<4>{});
    if constexpr (s + 3 < 18) modc(xf[s + 3], cst);
    if constexpr (s + 4 < 18) rdc(cst, cs + (s + 4) * 32);
    MFMA16A(xf[s], wf[4], acc[4]);
    if (!halo) { MFMA16A(wf[5], xf[s], acc[5]); MFMA16A(wf[6], xf[s], acc[6]); }
    if constexpr (s < 17) rdw(IC<s + 1>{}, IC<4>{}, IC<7>{});
    if constexpr (s > 0) static_for<k1_before(s), k1_before(s + 1)>(issue);
  });
  STAMP();
  asm volatile("s_nop 7" : "+v"(acc[0]), "+v"(acc[1]), "+v"(acc[2]), "+v"(acc[3]), "+v"(acc[4]), "+v"(acc[5]), "+v"(acc[6]) :: "memory");
  unsigned kf[2][2], qf[2][2], vf[2][2], lf[2];
#pragma unroll
  for (int h = 0; h < 2; ++h) {
    kf[h][0] = pk_bf16(acc[h][0], acc[h][1]); kf[h][1] = pk_bf16(acc[h][2], acc[h][3]);
    qf[h][0] = pk_bf16(acc[5 + h][0], acc[5 + h][1]); qf[h][1] = pk_bf16(acc[5 + h][2], acc[5 + h][3]);
    vf[h][0] = pk_bf16(acc[3 + h][0], acc[3 + h][1]); vf[h][1] = pk_bf16(acc[3 + h][2], acc[3 + h][3]);
  }
  lf[0] = pk_bf16(acc[2][0], acc[2][1]); lf[1] = pk_bf16(acc[2][2], acc[2][3]);
#pragma unroll
  for (int h = 0; h < 2; ++h) {
    *(LAS u32x4*)(lds + K1_EK + (w * 2 + h) * 1024 + l16) = (u32x4){kf[h][0], kf[h][1], lf[1], 0u};
    *(LAS u32x2*)(lds + K1_EV + (w * 2 + h) * 512 + lane * 8) = (u32x2){vf[h][0], vf[h][1]};
  }
  if (g == 2) *(LAS u32x2*)(lds + K1_EL + (w * 16 + li) * 8) = (u32x2){lf[0], lf[1]};
  WAIT_L(0);
  BAR();
  STAMP();
  if (!halo) {
    const int par = w & 1;
    const bool first = (w < 2);
    const int pw = first ? 8 : w - 2;
    const int pki = first ? 8 * par + (li & 7) : li;
    const int pvg = first ? 2 * par + (g & 1) : g;
    const int q4 = li >> 2;
    const int plk = first ? 8 * par + ((4 * g + q4) & 7) : 4 * g + q4;
    const s16x8 vl = cat8(tr_read(lds + K1_EL + (pw * 16 + plk) * 8), tr_read(lds + K1_EL + (w * 16 + 4 * g + q4) * 8));
    const bool prev_ok = !(first && rt == 0);
    unsigned op[2][2], ol[2];
    bool usep[4], useo[4];
#pragma unroll
    for (int j = 0; j < 4; ++j) { const int kk = 4 * g + j; usep[j] = prev_ok && (kk >= li + 9); useo[j] = (kk <= li) && (li - kk <= 7); }
    const unsigned mp01 = (usep[0] ? 0xffffu : 0u) | (usep[1] ? 0xffff0000u : 0u), mp23 = (usep[2] ? 0xffffu : 0u) | (usep[3] ? 0xffff0000u : 0u);
#pragma unroll
    for (int h = 0; h < 2; ++h) {
      const u32x4 kp = *(const LAS u32x4*)(lds + K1_EK + (pw * 2 + h) * 1024 + (g * 16 + pki) * 16);
      const u32x2 vp = *(const LAS u32x2*)(lds + K1_EV + (pw * 2 + h) * 512 + (pvg * 16 + li) * 8);
      const u32x4 ko = (u32x4){kf[h][0], kf[h][1], lf[1], 0u};
      const u32x4 qq = (u32x4){qf[h][0], qf[h][1], (g == h) ? lf[0] : 0u, 0u};
      const f32x4 z = (f32x4){0.f, 0.f, 0.f, 0.f};
      const f32x4 so = __builtin_amdgcn_mfma_f32_16x16x32_bf16(__builtin_bit_cast(s16x8, ko), __builtin_bit_cast(s16x8, qq), z, 0, 0, 0);
      const f32x4 sp = __builtin_amdgcn_mfma_f32_16x16x32_bf16(__builtin_bit_cast(s16x8, kp), __builtin_bit_cast(s16x8, qq), z, 0, 0, 0);
      float lg[4];
#pragma unroll
      for (int j = 0; j < 4; ++j) lg[j] = usep[j] ? sp[j] : so[j];
      rb2(lg[0], lg[1]); rb2(lg[2], lg[3]);
#pragma unroll
      for (int j = 0; j < 4; ++j) lg[j] *= 0.23570226f;
      rb2(lg[0], lg[1]); rb2(lg[2], lg[3]);
#pragma unroll
      for (int j = 0; j < 4; ++j) lg[j] = (usep[j] || useo[j]) ? lg[j] : -3.0e38f;
      const float mx = max32(max16(fmaxf(fmaxf(lg[0], lg[1]), fmaxf(lg[2], lg[3]))));
#pragma unroll
      for (int j = 0; j < 4; ++j) lg[j] = __builtin_amdgcn_exp2f((lg[j] - mx) * 1.44269504f);
      const float inv = __builtin_amdgcn_rcpf(sum32(sum16((lg[0] + lg[1]) + (lg[2] + lg[3]))));
      const unsigned p01 = pk_bf16(lg[0] * inv, lg[1] * inv), p23 = pk_bf16(lg[2] * inv, lg[3] * inv);
      const u32x4 pf = (u32x4){p01 & mp01, p23 & mp23, p01 & ~mp01, p23 & ~mp23};
      const u32x4 vv = (u32x4){vp[0], vp[1], vf[h][0], vf[h][1]};
      const f32x4 oo = __builtin_amdgcn_mfma_f32_16x16x32_bf16(__builtin_bit_cast(s16x8, vv), __builtin_bit_cast(s16x8, pf), z, 0, 0, 0);
      const f32x4 lo = __builtin_amdgcn_mfma_f32_16x16x32_bf16(vl, __builtin_bit_cast(s16x8, pf), z, 0, 0, 0);
      op[h][0] = pk_bf16(oo[0], oo[1]); op[h][1] = pk_bf16(oo[2], oo[3]);
      ol[h] = pk_bf16(lo[2 * h], lo[2 * h + 1]);
    }
    const char* ob = ws + OFF_O + (size_t)(8 * rt + w) * (18 * 1024);
    const u32x4 o4 = (u32x4){op[0][0], op[0][1], op[1][0], op[1][1]};
    const unsigned oo0 = hp * 1024 + l16;
    asm volatile("global_store_dwordx4 %0, %1, %2 sc1\n\ts_nop 1" :: "v"(oo0), "v"(o4), "s"(ob) : "memory");
    if (g == 0) {
      const u32x2 o2 = (u32x2){ol[0], ol[1]};
      const unsigned oo1 = (16 + (hp >> 3)) * 1024 + (((hp & 7) >> 1) * 16 + li) * 16 + 8 * (hp & 1);
      asm volatile("global_store_dwordx2 %0, %1, %2 sc1\n\ts_nop 1" :: "v"(oo1), "v"(o2), "s"(ob) : "memory");
    }
  }
  STAMP();
  asm volatile("s_waitcnt vmcnt(0)" : "+v"(xr[0]), "+v"(xr[1]), "+v"(mq[0]), "+v"(mq[1]), "+v"(mq[2]) :: "memory");
  static_for<0, 18>([&](auto I) { pin(wo[decltype(I)::value]); });
  BAR();
  GAS unsigned* cnt = (GAS unsigned*)(ws + OFF_CNT) + rt * 64;
  if (tid == 512) __hip_atomic_fetch_add(cnt, 1u, __ATOMIC_RELAXED, __HIP_MEMORY_SCOPE_AGENT);
  if (tid < 144) {
    float t[4];
#pragma unroll
    for (int j = 0; j < 4; ++j) t[j] = (mq[0][j] + mq[1][j]) + mq[2][j];
    rb2(t[0], t[1]); rb2(t[2], t[3]);
    if (tid >= 72 && tid < 108) {
#pragma unroll
      for (int j = 0; j < 4; ++j) t[j] += 1.f;
      rb2(t[0], t[1]); rb2(t[2], t[3]);
    }
    if (tid < 108) *(LAS f32x4*)(lds + K1_C2 + tid * 16) = (f32x4){t[0], t[1], t[2], t[3]};
    else if (rt2 == 0) *(f32x4*)(ws + OFF_GB + (nq * 144 + (tid - 108) * 4) * 4) = (f32x4){t[0], t[1], t[2], t[3]};
  }
  if (halo) {
    for (unsigned spin = 0; spin < (1u << 16); ++spin) {
      if (__builtin_amdgcn_readfirstlane(__hip_atomic_load(cnt, __ATOMIC_RELAXED, __HIP_MEMORY_SCOPE_AGENT)) >= 16u) break;
      __builtin_amdgcn_s_sleep(1);
    }
  }
  BAR();
  STAMP();
  {
    const char* osrc = ws + OFF_O + (size_t)rt2 * (36 * 1024) + w * 4096;
#pragma unroll
    for (int i = 0; i < 4; ++i) glds16_sc1(osrc + i * 1024, l16, lbase + K1_O + w * 4096 + i * 1024);
  }
  asm volatile("s_waitcnt vmcnt(0) lgkmcnt(0)" ::: "memory");
  BAR();
  STAMP();
  f32x4 ac2[2];
  ac2[0] = (f32x4){0.f, 0.f, 0.f, 0.f}; ac2[1] = ac2[0];
  {
    unsigned oa = lbase + K1_O + l16;
    asm volatile("" : "+v"(oa));
#pragma unroll
    for (int s = 0; s < 18; ++s) {
      const s16x8 x0 = *(const LAS s16x8*)(size_t)(oa + s * 1024), x1f = *(const LAS s16x8*)(size_t)(oa + (18 + s) * 1024);
      ac2[0] = __builtin_amdgcn_mfma_f32_16x16x32_bf16(__builtin_bit_cast(s16x8, wo[s]), x0, ac2[0], 0, 0, 0);
      ac2[1] = __builtin_amdgcn_mfma_f32_16x16x32_bf16(__builtin_bit_cast(s16x8, wo[s]), x1f, ac2[1], 0, 0, 0);
    }
  }
  const f32x4 ga = *(const LAS f32x4*)(lds + K1_C2 + (w * 16 + 4 * g) * 4);
  const f32x4 sb = *(const LAS f32x4*)(lds + K1_C2 + (144 + w * 16 + 4 * g) * 4);
  const f32x4 g2 = *(const LAS f32x4*)(lds + K1_C2 + (288 + w * 16 + 4 * g) * 4);
  float v[2][4];
  f32x2 sq[2];
  LAS f32x2* st = (LAS f32x2*)(lds + K1_ST);
  LAS f32x2* fin = (LAS f32x2*)(lds + K1_FIN);
#pragma unroll
  for (int m = 0; m < 2; ++m) {
    const float xv[4] = {bf_lo(xr[m][0]), bf_hi(xr[m][0]), bf_lo(xr[m][1]), bf_hi(xr[m][1])};
#pragma unroll
    for (int j = 0; j < 4; ++j) v[m][j] = ac2[m][j];
    rb2(v[m][0], v[m][1]); rb2(v[m][2], v[m][3]);
#pragma unroll
    for (int j = 0; j < 4; ++j) v[m][j] *= ga[j];
    rb2(v[m][0], v[m][1]); rb2(v[m][2], v[m][3]);
    const unsigned p0 = pk_bf16(v[m][0] + xv[0], v[m][1] + xv[1]), p1 = pk_bf16(v[m][2] + xv[2], v[m][3] + xv[3]);
    v[m][0] = bf_lo(p0); v[m][1] = bf_hi(p0); v[m][2] = bf_lo(p1); v[m][3] = bf_hi(p1);
    const float mw = sum32(sum16((v[m][0] + v[m][1]) + (v[m][2] + v[m][3]))) * (1.f / 16.f);
    const float d0 = v[m][0] - mw, d1 = v[m][1] - mw, d2 = v[m][2] - mw, d3 = v[m][3] - mw;
    const float q = sum32(sum16((d0 * d0 + d1 * d1) + (d2 * d2 + d3 * d3)));
    sq[m] = (f32x2){mw, q};
    xr[m] = (u32x2){p0, p1};
  }
  if (g < 2) st[w * 32 + g * 16 + li] = g ? sq[1] : sq[0];
  WAIT_L(0);
  BAR();
  STAMP();
  if (w == 0) {
    const int r = lane & 31;
    float mt_ = 0.f;
    f32x2 sv[9];
#pragma unroll
    for (int k = 0; k < 9; ++k) { sv[k] = st[k * 32 + r]; mt_ += sv[k][0]; }
    mt_ *= (1.f / 9.f);
    float m2 = 0.f;
#pragma unroll
    for (int k = 0; k < 9; ++k) { const float dl = sv[k][0] - mt_; m2 += sv[k][1] + 16.f * dl * dl; }
    GAS unsigned long long* xch = (GAS unsigned long long*)(ws + OFF_XCH) + (size_t)rt2 * 256;
    const float mine = (lane < 32) ? mt_ : m2;
    __hip_atomic_store(xch + nq * 64 + lane, ((unsigned long long)XCH_TAG << 32) | __float_as_uint(mine), __ATOMIC_RELAXED, __HIP_MEMORY_SCOPE_AGENT);
    float o[4];
    for (unsigned spin = 0; spin < (1u << 16); ++spin) {
      bool ok = true;
#pragma unroll
      for (int k = 0; k < 4; ++k) {
        const unsigned long long gv = __hip_atomic_load(xch + k * 64 + lane, __ATOMIC_RELAXED, __HIP_MEMORY_SCOPE_AGENT);
        o[k] = __uint_as_float((unsigned)gv); ok &= ((unsigned)(gv >> 32) == XCH_TAG);
      }
      if (__all(ok)) break;
      __builtin_amdgcn_s_sleep(1);
    }
    const float mean = 0.25f * ((o[0] + o[1]) + (o[2] + o[3]));
    float t = 0.f;
#pragma unroll
    for (int k = 0; k < 4; ++k) { const float dl = o[k] - mean; t += 144.f * dl * dl; }
    const auto sw = __builtin_amdgcn_permlane32_swap(__float_as_uint(mean), __float_as_uint(mean), false, false);
    const float m2s = 4.f * __uint_as_float(sw[1]);
    if (lane < 32) {
      const float var = (t + m2s) * (1.f / D);
      fin[r] = (f32x2){rb(mean), __builtin_amdgcn_rcpf(rb(__builtin_amdgcn_sqrtf(rb(rb(var) + 1e-5f))))};
    }
  }
#pragma unroll
  for (int m = 0; m < 2; ++m) *(u32x2*)(ws + OFF_X1 + ((size_t)((rt2 * 2 + m) * 36 + nt) * 64 + lane) * 8) = xr[m];
  WAIT_L(0);
  BAR();
  STAMP();
#pragma unroll
  for (int m = 0; m < 2; ++m) {
    const f32x2 f = fin[m * 16 + li];
    float h[4];
#pragma unroll
    for (int j = 0; j < 4; ++j) h[j] = v[m][j] - f[0];
    rb2(h[0], h[1]); rb2(h[2], h[3]);
#pragma unroll
    for (int j = 0; j < 4; ++j) h[j] *= f[1];
    rb2(h[0], h[1]); rb2(h[2], h[3]);
#pragma unroll
    for (int j = 0; j < 4; ++j) h[j] *= g2[j];
    rb2(h[0], h[1]); rb2(h[2], h[3]);
#pragma unroll
    for (int j = 0; j < 4; ++j) h[j] += sb[j];
    *(u32x2*)(ws + OFF_H2 + ((size_t)(rt2 * 36 + nt) * 64 + (g >> 1) * 32 + m * 16 + li) * 16 + (g & 1) * 8) = (u32x2){pk_bf16(h[0], h[1]), pk_bf16(h[2], h[3])};
  }
  STAMP_OUT(1);
}

constexpr int G1_W3 = 64 * 288 + 128;
constexpr int G1_STAGE = 2 * G1_W3;
constexpr int G1_ARR = 4 * G1_STAGE + 256;
constexpr int G1_LDS = G1_ARR + 16;
#define G1_WAITX(n, xf) asm volatile("s_waitcnt vmcnt(%4)" : "+v"(xf[0]), "+v"(xf[1]), "+v"(xf[2]), "+v"(xf[3]) : "n"(n) : "memory")
constexpr int g2_pos(int n) {
  const int s = n / 9, k = n % 9;
  if (s <= 1) return -1;
  return 18 * (s - 2) + 2 * k;
}
constexpr int g2_op_at(int u) { for (int n = 0; n < 81; ++n) if (g2_pos(n) == u) return n; return -1; }
constexpr int g2_issued_before(int u) { int c = 0; for (int n = 0; n < 81; ++n) if (g2_pos(n) < u) ++c; return c; }
template <int IMM, class T> __device__ __forceinline__ void gload16_sc1(T& dst, unsigned voff, const void* sbase) {
  asm volatile("global_load_dwordx4 %0, %1, %2 offset:%3 sc1" : "=v"(dst) : "v"(voff), "s"(sbase), "n"(IMM) : "memory");
}
template <int IMM> __device__ __forceinline__ void gstore16_sc1(const u32x4& v, unsigned voff, const void* sbase) {
  asm volatile("global_store_dwordx4 %0, %1, %2 offset:%3 sc1\n\ts_nop 1" :: "v"(voff), "v"(v), "s"(sbase), "n"(IMM) : "memory");
}
__device__ __forceinline__ void gstore8_sc1(const u32x2& v, unsigned voff, const void* sbase) {
  asm volatile("global_store_dwordx2 %0, %1, %2 sc1\n\ts_nop 1" :: "v"(voff), "v"(v), "s"(sbase) : "memory");
}
template <int N> __device__ __forceinline__ void wait4(u32x4 (&x)[4]) {
  asm volatile("s_waitcnt vmcnt(%4)" : "+v"(x[0]), "+v"(x[1]), "+v"(x[2]), "+v"(x[3]) : "n"(N) : "memory");
}
template <int N> __device__ __forceinline__ void wait_flag(unsigned& f) { asm volatile("s_waitcnt vmcnt(%1)" : "+v"(f) : "n"(N) : "memory"); }
__device__ __forceinline__ void pinu(unsigned& a, unsigned& b) { asm volatile("" : "+v"(a), "+v"(b)); }
__device__ __forceinline__ void atomic_inc(GAS unsigned* p) {
  const unsigned one = 1u, zero = 0u;
  asm volatile("global_atomic_add %0, %1, %2" :: "v"(zero), "v"(one), "s"(p) : "memory");
}
__device__ __forceinline__ void arrive(LAS char* ctr, GAS unsigned* cnt, int lane) {
  if (lane == 0) { if (__hip_atomic_fetch_add((LAS unsigned*)ctr, 1u, __ATOMIC_RELAXED, __HIP_MEMORY_SCOPE_WORKGROUP) == 7u) atomic_inc(cnt); }
}
__device__ __forceinline__ void gload4_sc1(unsigned& dst, const GAS unsigned* p) {
  const unsigned zero = 0u;
  asm volatile("global_load_dword %0, %1, %2 sc1" : "=v"(dst) : "v"(zero), "s"(p) : "memory");
}
__device__ __forceinline__ void wait_count(const GAS unsigned* cnt, unsigned want) {
  for (unsigned spin = 0; spin < (1u << 16); ++spin) {
    if (__builtin_amdgcn_readfirstlane(__hip_atomic_load(cnt, __ATOMIC_RELAXED, __HIP_MEMORY_SCOPE_AGENT)) >= want) break;
    __builtin_amdgcn_s_sleep(1);
  }
}

extern "C" __global__ __launch_bounds__(512, 2) void k345_mlp(char* __restrict__ ws, const u16* __restrict__ w1, const u16* __restrict__ w3,
                                                   const u16* __restrict__ w2, u16* __restrict__ out) {
  extern __shared__ __attribute__((aligned(16))) char smem[];
  LAS char* lds = (LAS char*)smem;
  const unsigned lbase = (unsigned)(size_t)lds;
  const int b = blockIdx.x, xcd = b & 7, idx = b >> 3;
  const int tid = threadIdx.x, lane = tid & 63;
  const int w = __builtin_amdgcn_readfirstlane(tid >> 6);
  const unsigned xvo = lane * 16;
  const int ksl = xcd >> 1;
  const int mt2 = (xcd & 1) * 8 + (idx & 7), nq = idx >> 3;
  const int mq = w & 3, kg = w >> 2;
  const char* hhb = ws + OFF_HH;
  STAMP_DECL; STAMP();
  if (tid == 0) *(LAS u32x2*)(lds + G1_ARR) = (u32x2){0u, 0u};
  unsigned so[5];
  const GAS unsigned* cntw = (const GAS unsigned*)(ws + OFF_CNT) + (16 + (mt2 >> 1) * 4 + ksl) * 64;
  unsigned flagv;
  GAS unsigned* cntB;
  const char* wsrc2 = (const char*)w2 + (size_t)(ksl * 1152 + (w >> 2) * 144 + (w & 3) * 16) * (D * 2) + nq * 288;
  const char* wsrc2b = (const char*)w2 + (size_t)(ksl * 1152 + w * 144 + 128) * (D * 2) + nq * 288;
  const unsigned wdst2 = lbase + w * 4608;
#define G2_G(st, i) do { const char* _s = ((st) < 8) ? wsrc2 + (size_t)(((st) & 3) * 288 + ((st) >> 2) * 64) * (D * 2) : wsrc2b; const unsigned _d = wdst2 + (((st) + 1) & 3) * G1_STAGE; \
    if ((i) < 4) glds16(_s, so[i], _d + (i) * 1024); else if (lane < 32) glds16(_s, so[4], _d + 4096); } while (0)
  {
    const int mt = (xcd & 1) * 4 + (idx & 3), ct = ksl * 8 + (idx >> 2);
    const char* wsrc = (const char*)((w < 4) ? w1 : w3) + (size_t)((w & 3) * 16) * (F * 2) + ct * 288;
#pragma unroll
    for (int i = 0; i < 5; ++i) {
      const int ci = i * 64 + lane;
      const int r = ci / 18, ch = ci - r * 18;
      so[i] = r * (F * 2) + ch * 16;
    }
    const unsigned wdst = lbase + (w >> 2) * G1_W3 + (w & 3) * 4608;
#define G1_G(st, i) do { const char* _s = wsrc + (size_t)(st) * (64 * F * 2); const unsigned _d = wdst + ((st) & 3) * G1_STAGE; \
    if ((i) < 4) glds16(_s, so[i], _d + (i) * 1024); else if (lane < 32) glds16(_s, so[4], _d + 4096); } while (0)
    const char* xsrc = ws + OFF_H2 + (size_t)(mt * 8 + w) * (36 * 1024);
    u32x4 xa[4], xb[4], xc[4];
#define G1_X(dst, st, i) do { const char* _p = xsrc + (st) * 4096; GLOAD16(dst[i], xvo, _p, (i) * 1024); } while (0)
#pragma unroll
    for (int i = 0; i < 5; ++i) G1_G(0, i);
#pragma unroll
    for (int i = 0; i < 4; ++i) G1_X(xa, 0, i);
    BAR();
#pragma unroll
    for (int i = 0; i < 5; ++i) G1_G(1, i);
    const int grp = lane >> 4, q = (lane & 15) >> 2, p4 = lane & 3;
    const unsigned ta = (grp & 1) * G1_W3 + (8 * (grp >> 1) + q) * 288 + (((p4 & 1) << 4) | ((p4 >> 1) << 3));
    f32x16 acc[9];
#pragma unroll
    for (int t = 0; t < 9; ++t)
      for (int j = 0; j < 16; ++j) acc[t][j] = 0.f;
    s16x8 wf[4];
#define G1_RDKT(b, ks, t) cat8(tr_read((b) + (16 * (ks)) * 288 + (t) * 32), tr_read((b) + (16 * (ks) + 4) * 288 + (t) * 32))
#define G1_RD(b, i) G1_RDKT(b, (i) / 9, (i) % 9)
    const unsigned w2o = (ksl * 10368 + idx * 324 + (w * 7 + lane < 54 ? w * 7 + lane : 53)) * 128;
#define G1_STEP(xf, xn, st) do { LAS char* _b = lds + ((st) & 3) * G1_STAGE + ta; LAS char* _n = lds + (((st) + 1) & 3) * G1_STAGE + ta; \
    if ((st) >= 1) { if (lane < 7) touch((const char*)w2 + ((st) - 1) * (54 * 128), w2o, lbase + 4 * G1_STAGE); } \
    if ((st) <= 1) { wf[0] = G1_RD(_b, 0); wf[1] = G1_RD(_b, 1); wf[2] = G1_RD(_b, 2); } \
    _Pragma("unroll") for (int i = 0; i < 36; ++i) { \
      if (i + 3 < 36) wf[(i + 3) & 3] = G1_RD(_b, i + 3); \
      else if ((st) >= 1 && (st) < 6) wf[(i + 3) & 3] = G1_RD(_n, i + 3 - 36); \
      else if ((st) == 6) wf[(i + 3) & 3] = G1_RDKT(_n, i + 3 - 36, 0); \
      acc[i % 9] = __builtin_amdgcn_mfma_f32_32x32x16_bf16(wf[i & 3], __builtin_bit_cast(s16x8, xf[i / 9]), acc[i % 9], 0, 0, 0); \
      if ((st) == 0) { if ((i & 1) == 1 && (i >> 1) < 14) { const int v = i >> 1; \
        if (v < 4) G1_X(xn, 1, (v) & 3); else if (v < 9) G1_G(2, (v < 4) ? 0 : (v - 4) % 5); else G1_G(3, (v < 9) ? 0 : (v - 9) % 5); } } \
      else if ((i & 3) == 1) { const int v = i >> 2; \
        if (v < 4) G1_X(xn, (st) + 1, (v) & 3); \
        else if ((st) + 3 < 9) G1_G((st) + 3, (v - 4 < 0) ? 0 : v - 4); \
        else if ((st) == 6 && v < 8) G1_X(xc, 8, (v) & 3); } \
      SB0(); } } while (0)
    G1_WAITX(5, xa); BAR(); STAMP(); G1_STEP(xa, xb, 0);
    G1_WAITX(5, xb); BAR(); G1_STEP(xb, xa, 1);
    G1_WAITX(5, xa); BAR(); G1_STEP(xa, xb, 2);
    G1_WAITX(5, xb); BAR(); G1_STEP(xb, xa, 3);
    G1_WAITX(5, xa); BAR(); G1_STEP(xa, xb, 4);
    G1_WAITX(5, xb); BAR(); G1_STEP(xb, xa, 5);
    G1_WAITX(5, xa); BAR(); G1_STEP(xa, xb, 6);
    G1_WAITX(0, xb); G1_WAITX(0, xc); BAR(); STAMP();
#pragma unroll
    for (int i = 0; i < 5; ++i) {
      const int ci = i * 64 + lane;
      const int pr = ci / 18, ch = ci - pr * 18;
      const int r = pr ^ (((pr >> 3) & 1) << 2);
      so[i] = r * (D * 2) + ch * 16;
    }
    LAS char* b7 = lds + 3 * G1_STAGE + ta; LAS char* b8 = lds + ta;
    const int m = lane & 31, h = lane >> 5;
    const int row = mt * 256 + w * 32 + m;
    const int cl = idx >> 2;
    const unsigned dbase = (((row >> 4) * 144 + ksl * 36) * 64 + h * 16 + (row & 15)) * 16;
    const unsigned d03 = dbase + cl * 2048, d8 = dbase + (32 + (cl >> 1)) * 1024 + (cl & 1) * 512;
    GAS unsigned* cntA = (GAS unsigned*)(ws + OFF_CNT) + (16 + mt * 4 + ksl) * 64;
#define G1_RT(i) (((i) & 7) < 4 ? G1_RDKT(b7, (i) & 3, (i) >> 3) : G1_RDKT(b8, (i) & 3, (i) >> 3))
    float e[8];
    static_for<0, 80>([&](auto I) {
      constexpr int i = decltype(I)::value, t = i >> 3, j = i & 7;
      if constexpr (i + 3 < 72) wf[(i + 3) & 3] = G1_RT(i + 3);
      if constexpr (i < 72) {
        const u32x4 xx = (j < 4) ? xb[j & 3] : xc[j & 3];
        acc[t] = __builtin_amdgcn_mfma_f32_32x32x16_bf16(wf[i & 3], __builtin_bit_cast(s16x8, xx), acc[t], 0, 0, 0);
      }
      if constexpr (t >= 1) {
        e[j] = fast_silu(acc[t - 1][j]) * acc[t - 1][8 + j];
        if constexpr (j == 7) {
          constexpr int tt = t - 1;
          const u32x4 hv = (u32x4){pk_bf16(e[0], e[1]), pk_bf16(e[2], e[3]), pk_bf16(e[4], e[5]), pk_bf16(e[6], e[7])};
          if constexpr (tt < 4) gstore16_sc1<(tt >> 1) * 1024 + (tt & 1) * 512>(hv, d03, hhb);
          else if constexpr (tt < 8) gstore16_sc1<((tt & 3) >> 1) * 1024 + (tt & 1) * 512>(hv, d03, hhb + 16 * 1024);
          else gstore16_sc1<0>(hv, d8, hhb);
        }
      }
      if constexpr (i >= 2 && i < 40 && (i & 3) == 2) { constexpr int v = (i - 2) >> 2; G2_G(v / 5, v % 5); }
      if constexpr (i == 54) {
        WAIT_V(1);
        arrive(lds + G1_ARR, cntA, lane);
      }
      if constexpr (i == 74) { if (w == 7) gload4_sc1(flagv, cntw); }
      SB0();
    });
    STAMP();
    cntB = cntA + 32;
  }
  if (w == 7) { wait_flag<1>(flagv); if (__builtin_amdgcn_readfirstlane(flagv) < 8u) wait_count(cntw, 8u); }
  const char* xs0 = hhb + ((size_t)(mt2 * 8 + mq * 2 + kg) * 144 + ksl * 36 + kg * 2) * 1024;
  const char* xs1 = hhb + ((size_t)(mt2 * 8 + mq * 2 + (kg ^ 1)) * 144 + ksl * 36 + kg * 2) * 1024;
  u32x4 xr[3][4];
#define G2_X(st, i) gload16_sc1<((i) >> 1) * 1024>(xr[(st) % 3][i], xvo, (((i) & 1) ? xs1 : xs0) + (st) * 4096)
  const int g = lane >> 4;
  unsigned tb[2][2];
  {
    const int q = (lane & 15) >> 2, p4 = lane & 3;
#pragma unroll
    for (int t = 0; t < 2; ++t) { tb[0][t] = lbase + G1_STAGE + (kg * 64 + 8 * g + ((4 * t) ^ (4 * (g & 1))) + q) * 288 + p4 * 8; tb[1][t] = tb[0][t] + G1_STAGE; }
    pinu(tb[0][0], tb[0][1]); pinu(tb[1][0], tb[1][1]);
  }
  f32x4 acc[2][9];
#pragma unroll
  for (int i = 0; i < 2; ++i)
#pragma unroll
    for (int t = 0; t < 9; ++t) { acc[i][t] = (f32x4){0.f, 0.f, 0.f, 0.f}; asm volatile("" : "+v"(acc[i][t])); }
  constexpr int PF2 = 5, RG2 = 6;
  s16x8 wf[RG2];
#define G2_RD(st, i) cat8(tr_read((LAS char*)(size_t)(tb[(st) & 1][0] + ((i) / 9) * (32 * 288) + ((i) % 9) * 32)), \
                          tr_read((LAS char*)(size_t)(tb[(st) & 1][1] + ((i) / 9) * (32 * 288) + ((i) % 9) * 32)))
  BAR();
  STAMP();
  static_for<0, 8>([&](auto I) { constexpr int i = decltype(I)::value; G2_X(i / 4, i % 4); });
  static_for<0, PF2>([&](auto I) { constexpr int i = decltype(I)::value; wf[i % RG2] = G2_RD(0, i); });
  wait4<4>(xr[0]);
  arrive(lds + G1_ARR + 4, cntB, lane);
  STAMP();
  const int fm = w & 1, fs = w >> 1;
  const int T = mt2 * 8 + 2 * ksl + fm;
  const int col = nq * 144 + 32 * fs + 4 * g;
  u32x2 x1v[3]; f32x4 gb[3];
  auto issue_p5 = [&]() {
    const char* xp = ws + OFF_X1 + (size_t)(T * 36 + nq * 9) * 512;
    const unsigned l8 = lane * 8;
    gload8<0>(x1v[0], l8 + fs * 1024, xp); gload8<512>(x1v[1], l8 + fs * 1024, xp); gload8<0>(x1v[2], l8 + 4096, xp);
    const unsigned c0 = col * 4, c8 = (nq * 144 + 128 + 4 * g) * 4;
    const char* mp = ws + OFF_GB;
    gload16<0>(gb[0], c0, mp); gload16<64>(gb[1], c0, mp); gload16<0>(gb[2], c8, mp);
  };
  static_for<0, 162>([&](auto U) {
    constexpr int u = decltype(U)::value, st = u / 18, i = u % 18;
    if constexpr (i == 12 && st < 8) {
      wait4<g2_issued_before(u) - 9 * (st + 2)>(xr[(st + 1) % 3]);
      if constexpr (st == 1) {
        wait_flag<g2_issued_before(u) - 9 * (st + 2)>(flagv);
        if (w == 0) { if (__builtin_amdgcn_readfirstlane(flagv) < 8u) wait_count(cntw + 32, 8u); }
      }
      BAR();
      if constexpr (st >= 1) {
        constexpr unsigned d = (((st + 2) & 3) >= 2) ? 2 * G1_STAGE : (unsigned)(-2 * G1_STAGE);
        tb[(st + 1) & 1][0] += d; tb[(st + 1) & 1][1] += d;
        pinu(tb[(st + 1) & 1][0], tb[(st + 1) & 1][1]);
      }
    }
    constexpr int op = g2_op_at(u);
    if constexpr (u == 17) gload4_sc1(flagv, cntw + 32);
    if constexpr (op >= 0) { if constexpr (op % 9 < 5) G2_G(op / 9, op % 9); else G2_X(op / 9, op % 9 - 5); }
    if constexpr (u == 140) issue_p5();
    if constexpr (u + PF2 < 162) wf[(u + PF2) % RG2] = G2_RD((u + PF2) / 18, (u + PF2) % 18);
    MFMA16A(wf[u % RG2], xr[st % 3][2 * (i / 9)], acc[0][i % 9]);
    MFMA16A(wf[u % RG2], xr[st % 3][2 * (i / 9) + 1], acc[1][i % 9]);
    SB0();
  });
  asm volatile("s_nop 7" : "+v"(acc[0][0]), "+v"(acc[0][1]), "+v"(acc[0][2]), "+v"(acc[0][3]), "+v"(acc[0][4]), "+v"(acc[0][5]), "+v"(acc[0][6]), "+v"(acc[0][7]), "+v"(acc[0][8]),
               "+v"(acc[1][0]), "+v"(acc[1][1]), "+v"(acc[1][2]), "+v"(acc[1][3]), "+v"(acc[1][4]), "+v"(acc[1][5]), "+v"(acc[1][6]), "+v"(acc[1][7]), "+v"(acc[1][8]) :: "memory");
  BAR();
  STAMP();
  LAS f32x4* ex = (LAS f32x4*)lds;
#pragma unroll
  for (int t = 0; t < 9; ++t) ex[(w * 9 + t) * 64 + lane] = acc[1][t];
  WAIT_L(0);
  BAR();
  STAMP();
  {
    f32x4 v[9];
#pragma unroll
    for (int t = 0; t < 9; ++t) v[t] = acc[0][t] + ex[((w ^ 4) * 9 + t) * 64 + lane];
    if (mq == ksl) {
      LAS f32x4* own = ex + (72 + kg * 9) * 64 + lane;
#pragma unroll
      for (int t = 0; t < 9; ++t) own[t * 64] = v[t];
    } else {
      const char* dst = ws + OFF_PART + ((size_t)(((ksl * 16 + mt2) * 4 + nq) * 8 + mq * 2 + kg) * 5) * 1024;
      static_for<0, 4>([&](auto I) { constexpr int i = decltype(I)::value;
        gstore16_sc1<i * 1024>((u32x4){pk_bf16(v[2 * i][0], v[2 * i][1]), pk_bf16(v[2 * i][2], v[2 * i][3]), pk_bf16(v[2 * i + 1][0], v[2 * i + 1][1]), pk_bf16(v[2 * i + 1][2], v[2 * i + 1][3])}, xvo, dst); });
      gstore8_sc1((u32x2){pk_bf16(v[8][0], v[8][1]), pk_bf16(v[8][2], v[8][3])}, xvo + 4096, dst);
    }
  }
  asm volatile("s_waitcnt vmcnt(0) lgkmcnt(0)" : "+v"(x1v[0]), "+v"(x1v[1]), "+v"(x1v[2]), "+v"(gb[0]), "+v"(gb[1]), "+v"(gb[2]) :: "memory");
  BAR();
  STAMP();
  GAS unsigned* cnt4 = (GAS unsigned*)(ws + OFF_CNT) + (64 + mt2 * 4 + nq) * 64;
  if (tid == 448) __hip_atomic_fetch_add(cnt4, 1u, __ATOMIC_RELAXED, __HIP_MEMORY_SCOPE_AGENT);
  f32x4 y[3];
  y[0] = ex[(72 + fm * 9 + 2 * fs) * 64 + lane]; y[1] = ex[(72 + fm * 9 + 2 * fs + 1) * 64 + lane]; y[2] = ex[(72 + fm * 9 + 8) * 64 + lane];
  if (w == 7) wait_count(cnt4, 4u);
  BAR();
  STAMP();
  {
    u32x4 pa[3], pb[3];
#pragma unroll
    for (int k = 0; k < 3; ++k) {
      const int src = (ksl + 1 + k) & 3;
      const char* p = ws + OFF_PART + ((size_t)(((src * 16 + mt2) * 4 + nq) * 8 + 2 * ksl + fm) * 5) * 1024;
      gload16_sc1<0>(pa[k], xvo + fs * 1024, p);
      if (fs == 0) gload16_sc1<0>(pb[k], xvo + 4096, p);
    }
    asm volatile("s_waitcnt vmcnt(0)" : "+v"(pa[0]), "+v"(pa[1]), "+v"(pa[2]), "+v"(pb[0]), "+v"(pb[1]), "+v"(pb[2]) :: "memory");
    STAMP();
#pragma unroll
    for (int k = 0; k < 3; ++k) {
      y[0] += (f32x4){bf_lo(pa[k][0]), bf_hi(pa[k][0]), bf_lo(pa[k][1]), bf_hi(pa[k][1])};
      y[1] += (f32x4){bf_lo(pa[k][2]), bf_hi(pa[k][2]), bf_lo(pa[k][3]), bf_hi(pa[k][3])};
      y[2] += (f32x4){bf_lo(pb[k][0]), bf_hi(pb[k][0]), bf_lo(pb[k][1]), bf_hi(pb[k][1])};
    }
    const int row = 32 * (T >> 1) + 2 * (lane & 15) + (T & 1);
    u16* op = out + (size_t)row * D;
#pragma unroll
    for (int k = 0; k < 3; ++k) {
      float r0 = y[k][0], r1 = y[k][1], r2 = y[k][2], r3 = y[k][3];
      rb2(r0, r1); rb2(r2, r3);
      r0 *= gb[k][0]; r1 *= gb[k][1]; r2 *= gb[k][2]; r3 *= gb[k][3];
      rb2(r0, r1); rb2(r2, r3);
      const u32x2 o = (u32x2){pk_bf16(r0 + bf_lo(x1v[k][0]), r1 + bf_hi(x1v[k][0])), pk_bf16(r2 + bf_lo(x1v[k][1]), r3 + bf_hi(x1v[k][1]))};
      if (k < 2) *(u32x2*)(op + col + 16 * k) = o;
      else if (fs == 0) *(u32x2*)(op + nq * 144 + 128 + 4 * g) = o;
    }
  }
  STAMP();
  STAMP_OUT(3);
}

extern "C" __global__ void k_end_marker() {}

extern "C" void kernel_launch(void* const* d_in, const int* in_sizes, int n_in, void* d_out, int out_size,
                              void* d_ws, size_t ws_size, hipStream_t stream) {
  const u16* x = (const u16*)d_in[0];
  const u16* wqkv = (const u16*)d_in[1];
  const u16* wo = (const u16*)d_in[2];
  const u16* w1 = (const u16*)d_in[4];
  const u16* w3 = (const u16*)d_in[5];
  const u16* w2 = (const u16*)d_in[6];
  const u16* c = (const u16*)d_in[7];
  const u16* wmod = (const u16*)d_in[8];
  char* ws = (char*)d_ws;
  (void)hipFuncSetAttribute((const void*)k12_attn, hipFuncAttributeMaxDynamicSharedMemorySize, K1_LDS);
  (void)hipFuncSetAttribute((const void*)k345_mlp, hipFuncAttributeMaxDynamicSharedMemorySize, G1_LDS);
  k0_prep<<<K0_F, 384, 0, stream>>>(x, c, wmod, wqkv, wo, ws, w1, w3, w2, (const u16*)d_out);
  k12_attn<<<256, 576, K1_LDS, stream>>>(ws, x, w1, w3);
  k345_mlp<<<256, 512, G1_LDS, stream>>>(ws, w1, w3, w2, (u16*)d_out);
}
```

```cpp
#include <hip/hip_runtime.h>
#include <hip/hip_bf16.h>
#include <stdint.h>

typedef unsigned short u16;
typedef short s16x4 __attribute__((ext_vector_type(4)));
typedef short s16x8 __attribute__((ext_vector_type(8)));
typedef float f32x2 __attribute__((ext_vector_type(2)));
typedef float f32x4 __attribute__((ext_vector_type(4)));
typedef float f32x16 __attribute__((ext_vector_type(16)));
typedef unsigned u32x2 __attribute__((ext_vector_type(2)));
typedef unsigned u32x4 __attribute__((ext_vector_type(4)));
typedef __bf16 bf16x2 __attribute__((ext_vector_type(2)));
#define LAS __attribute__((address_space(3)))
#define GAS __attribute__((address_space(1)))

constexpr int S = 2048, D = 576, F = 4608;
constexpr size_t OFF_MOD = 0;
constexpr size_t OFF_GB = 45056;
constexpr size_t OFF_XN = 49152;
constexpr size_t OFF_O = OFF_XN + (size_t)S * D * 2;
constexpr size_t OFF_X1 = OFF_O + (size_t)S * D * 2;
constexpr size_t OFF_H2 = OFF_X1 + (size_t)S * D * 4;
constexpr size_t OFF_HH = OFF_H2 + (size_t)S * D * 2;
constexpr size_t OFF_PART = OFF_HH + (size_t)S * F * 2;
constexpr size_t OFF_WQ = OFF_PART + (size_t)4 * 512 * 5 * 1024;
constexpr size_t OFF_BIAS = OFF_WQ + (size_t)16 * 126 * 1024;
constexpr size_t OFF_WO = OFF_BIAS + (size_t)16 * 8192;
constexpr size_t OFF_XCH = OFF_WO + (size_t)36 * 18 * 1024;
constexpr size_t OFF_CNT = OFF_XCH + (size_t)64 * 4 * 64 * 8;
constexpr size_t OFF_END = OFF_CNT + 32768;
constexpr unsigned XCH_TAG = 0x5a17c3e1u;

__device__ __forceinline__ float bf_lo(unsigned v) { return __uint_as_float(v << 16); }
__device__ __forceinline__ float bf_hi(unsigned v) { return __uint_as_float(v & 0xffff0000u); }
__device__ __forceinline__ unsigned pk_bf16(float lo, float hi) {
  bf16x2 r; r.x = (__bf16)lo; r.y = (__bf16)hi;
  return __builtin_bit_cast(unsigned, r);
}
__device__ __forceinline__ float rb(float f) { return bf_lo(pk_bf16(f, 0.f)); }
template <int CTRL> __device__ __forceinline__ float dppf(float x) { return __int_as_float(__builtin_amdgcn_update_dpp(0, __float_as_int(x), CTRL, 0xf, 0xf, true)); }
__device__ __forceinline__ float sum16(float x) { auto r = __builtin_amdgcn_permlane16_swap(__float_as_uint(x), __float_as_uint(x), false, false); return __uint_as_float(r[0]) + __uint_as_float(r[1]); }
__device__ __forceinline__ float sum32(float x) { auto r = __builtin_amdgcn_permlane32_swap(__float_as_uint(x), __float_as_uint(x), false, false); return __uint_as_float(r[0]) + __uint_as_float(r[1]); }
__device__ __forceinline__ float max16(float x) { auto r = __builtin_amdgcn_permlane16_swap(__float_as_uint(x), __float_as_uint(x), false, false); return fmaxf(__uint_as_float(r[0]), __uint_as_float(r[1])); }
__device__ __forceinline__ float max32(float x) { auto r = __builtin_amdgcn_permlane32_swap(__float_as_uint(x), __float_as_uint(x), false, false); return fmaxf(__uint_as_float(r[0]), __uint_as_float(r[1])); }
__device__ __forceinline__ void rb2(float& a, float& b) { const unsigned p = pk_bf16(a, b); a = bf_lo(p); b = bf_hi(p); }
__device__ __forceinline__ float fast_silu(float g) { return g * __builtin_amdgcn_rcpf(1.f + __builtin_amdgcn_exp2f(g * -1.44269504f)); }

#define WAIT_V(n) asm volatile("s_waitcnt vmcnt(%0)" ::"n"(n) : "memory")
#define WAIT_L(n) asm volatile("s_waitcnt lgkmcnt(%0)" ::"n"(n) : "memory")
#define BAR() do { asm volatile("" ::: "memory"); __builtin_amdgcn_s_barrier(); asm volatile("" ::: "memory"); } while (0)
template <int IMM, class T> __device__ __forceinline__ void gload16(T& dst, unsigned voff, const void* sbase) {
  asm volatile("global_load_dwordx4 %0, %1, %2 offset:%3" : "=v"(dst) : "v"(voff), "s"(sbase), "n"(IMM) : "memory");
}
template <int IMM, class T> __device__ __forceinline__ void gload8(T& dst, unsigned voff, const void* sbase) {
  asm volatile("global_load_dwordx2 %0, %1, %2 offset:%3" : "=v"(dst) : "v"(voff), "s"(sbase), "n"(IMM) : "memory");
}
#define GLOAD16(dst, voff, sbase, imm) asm volatile("global_load_dwordx4 %0, %1, %2 offset:%3" : "=v"(dst) : "v"(voff), "s"(sbase), "n"(imm) : "memory")

__device__ __forceinline__ void glds16(const void* sbase, unsigned voff, unsigned ldsaddr) {
  asm volatile("s_mov_b32 m0, %2\n\ts_nop 0\n\tglobal_load_lds_dwordx4 %0, %1" :: "v"(voff), "s"(sbase), "s"(ldsaddr) : "memory");
}
__device__ __forceinline__ void glds16_sc1(const void* sbase, unsigned voff, unsigned ldsaddr) {
  asm volatile("s_mov_b32 m0, %2\n\ts_nop 0\n\tglobal_load_lds_dwordx4 %0, %1 sc1" :: "v"(voff), "s"(sbase), "s"(ldsaddr) : "memory");
}
#define SB0() __builtin_amdgcn_sched_barrier(0)
template <int I> struct IC { static constexpr int value = I; };
template <int B, int E, class Fn> __device__ __forceinline__ void static_for(Fn&& f) {
  if constexpr (B < E) { f(IC<B>{}); static_for<B + 1, E>(f); }
}
__device__ __forceinline__ void touch(const void* sbase, unsigned voff, unsigned ldsaddr) {
  asm volatile("s_mov_b32 m0, %2\n\ts_nop 0\n\tglobal_load_lds_dword %0, %1" :: "v"(voff), "s"(sbase), "s"(ldsaddr) : "memory");
}
__device__ __forceinline__ void pad2(u32x4& r) { asm volatile("s_nop 1" : "+v"(r)); }
template <class A, class B> __device__ __forceinline__ void MFMA16A(const A& a, const B& b, f32x4& c) {
  asm volatile("v_mfma_f32_16x16x32_bf16 %0, %1, %2, %0" : "+v"(c) : "v"(a), "v"(b));
}
#define STAMP_DECL
#define STAMP()
#define STAMP_OUT(kid)
__device__ __forceinline__ s16x4 tr_read(LAS char* p) { return __builtin_amdgcn_ds_read_tr16_b64_v4i16((LAS s16x4*)p); }
__device__ __forceinline__ s16x8 cat8(s16x4 a, s16x4 b) { return __builtin_shufflevector(a, b, 0, 1, 2, 3, 4, 5, 6, 7); }

__device__ __forceinline__ unsigned perm_lo(unsigned a, unsigned b) { return __builtin_amdgcn_perm(b, a, 0x05040100u); }
__device__ __forceinline__ unsigned perm_hi(unsigned a, unsigned b) { return __builtin_amdgcn_perm(b, a, 0x07060302u); }

constexpr int K0_A = 162, K0_B = K0_A + 128, K0_C = K0_B + 81, K0_D = K0_C + 27, K0_E = 400, K0_F = K0_E + 8 * 4;
extern "C" __global__ __launch_bounds__(384) void k0_prep(const u16* __restrict__ x, const u16* __restrict__ c, const u16* __restrict__ wmod,
                                               const u16* __restrict__ wqkv, const u16* __restrict__ wo, char* __restrict__ ws,
                                               const u16* __restrict__ w1, const u16* __restrict__ w3, const u16* __restrict__ w2,
                                               const u16* __restrict__ outp) {
  __shared__ __attribute__((aligned(16))) float red[6 * 64];
  const int b = blockIdx.x, tid = threadIdx.x;
  const int w = tid >> 6, lane = tid & 63;
  STAMP_DECL; STAMP();
  if (b < K0_A) {
    const int kt = b / 54, cb = b - kt * 54;
    const int r0 = kt * 192 + (tid >> 3), col = cb * 64 + (tid & 7) * 8;
    u32x4 wv[4]; float sv[4];
#pragma unroll
    for (int i = 0; i < 4; ++i) wv[i] = *(const u32x4*)(wmod + (size_t)(r0 + 48 * i) * (6 * D) + col);
#pragma unroll
    for (int i = 0; i < 4; ++i) sv[i] = rb(fast_silu(bf_lo(c[r0 + 48 * i])));
    float a[8] = {0, 0, 0, 0, 0, 0, 0, 0};
#pragma unroll
    for (int i = 0; i < 4; ++i)
#pragma unroll
      for (int j = 0; j < 4; ++j) { a[2 * j] += sv[i] * bf_lo(wv[i][j]); a[2 * j + 1] += sv[i] * bf_hi(wv[i][j]); }
#pragma unroll
    for (int j = 0; j < 8; ++j) { a[j] += dppf<0x128>(a[j]); a[j] = sum32(sum16(a[j])); }
    if (lane < 8) {
      *(f32x4*)(red + w * 64 + lane * 8) = (f32x4){a[0], a[1], a[2], a[3]};
      *(f32x4*)(red + w * 64 + lane * 8 + 4) = (f32x4){a[4], a[5], a[6], a[7]};
    }
    __syncthreads();
    if (tid < 64) {
      float s = 0.f;
#pragma unroll
      for (int i = 0; i < 6; ++i) s += red[i * 64 + tid];
      ((float*)(ws + OFF_MOD))[kt * 3456 + cb * 64 + tid] = s;
    }
  } else if (b < K0_B) {
    const int i = lane & 15, g = lane >> 4;
    const int T = b - K0_A;
    const int row = 32 * (T >> 1) + 2 * i + (T & 1);
    float v[3][8];
#pragma unroll
    for (int s = 0; s < 3; ++s) {
      const u32x4 a = *(const u32x4*)(x + (size_t)row * D + ((w * 3 + s) * 4 + g) * 8);
#pragma unroll
      for (int j = 0; j < 4; ++j) { v[s][2 * j] = bf_lo(a[j]); v[s][2 * j + 1] = bf_hi(a[j]); }
    }
    float sm = 0.f;
#pragma unroll
    for (int s = 0; s < 3; ++s) sm += ((v[s][0] + v[s][1]) + (v[s][2] + v[s][3])) + ((v[s][4] + v[s][5]) + (v[s][6] + v[s][7]));
    const float mw = sum32(sum16(sm)) * (1.f / 96.f);
    float q0 = 0.f, q1 = 0.f;
#pragma unroll
    for (int s = 0; s < 3; ++s)
#pragma unroll
      for (int j = 0; j < 4; ++j) { const float d0 = v[s][2 * j] - mw, d1 = v[s][2 * j + 1] - mw; q0 += d0 * d0; q1 += d1 * d1; }
    const float q = sum32(sum16(q0 + q1));
    if (g == 0) *(f32x2*)(red + (w * 16 + i) * 2) = (f32x2){mw, q};
    __syncthreads();
    f32x2 p[6];
    float mean = 0.f;
#pragma unroll
    for (int k = 0; k < 6; ++k) { p[k] = *(const f32x2*)(red + (k * 16 + i) * 2); mean += p[k][0]; }
    mean *= (1.f / 6.f);
    float var = 0.f;
#pragma unroll
    for (int k = 0; k < 6; ++k) { const float dl = p[k][0] - mean; var += p[k][1] + 96.f * dl * dl; }
    var *= (1.f / D);
    const float mr = rb(mean);
#pragma unroll
    for (int s = 0; s < 3; ++s)
#pragma unroll
      for (int j = 0; j < 4; ++j) { v[s][2 * j] -= mr; v[s][2 * j + 1] -= mr; rb2(v[s][2 * j], v[s][2 * j + 1]); }
    const float rs = __builtin_amdgcn_rcpf(rb(__builtin_amdgcn_sqrtf(rb(rb(var) + 1e-5f))));
#pragma unroll
    for (int s = 0; s < 3; ++s) {
      u32x4 o;
#pragma unroll
      for (int j = 0; j < 4; ++j) o[j] = pk_bf16(v[s][2 * j] * rs, v[s][2 * j + 1] * rs);
      *(u32x4*)(ws + OFF_XN + ((size_t)(T * 18 + w * 3 + s) * 64 + lane) * 16) = o;
    }
  } else if (b < K0_C) {
    const int task = (b - K0_B) * 384 + tid;
    const int kq = task / 432, nq4 = task - kq * 432;
    u32x2 r[8];
#pragma unroll
    for (int i = 0; i < 8; ++i) r[i] = *(const u32x2*)(wqkv + (size_t)(kq * 8 + i) * (3 * D) + nq4 * 4);
    const int part = nq4 / 144, nh = nq4 - part * 144, ks = kq >> 2, ko = kq & 3;
#pragma unroll
    for (int e = 0; e < 4; ++e) {
      const int n = nh * 4 + e, head = n / 18, d = n - head * 18, hp = head >> 1, h = head & 1;
      int tile, idx;
      if (d < 16) { tile = (part == 0) ? 5 + h : (part == 1 ? h : 3 + h); idx = d; }
      else { tile = 2; idx = (part == 2) ? 8 + 2 * h + (d - 16) : 4 * h + 2 * part + (d - 16); }
      char* dst = ws + OFF_WQ + ((hp * 18 + ks) * 7 + tile) * 1024 + (ko * 16 + idx) * 16;
      u32x4 f;
#pragma unroll
      for (int i = 0; i < 4; ++i) f[i] = (e & 1) ? perm_hi(r[2 * i][e >> 1], r[2 * i + 1][e >> 1]) : perm_lo(r[2 * i][e >> 1], r[2 * i + 1][e >> 1]);
      *(u32x4*)dst = f;
      if (part == 2 && d >= 16) *(u32x4*)(dst + 64) = (u32x4){0u, 0u, 0u, 0u};
    }
  } else if (b >= K0_E) {
    const int job = (b - K0_E) >> 3;
    const unsigned scr = (unsigned)(size_t)(LAS float*)red;
    if (job < 2) { const unsigned o = (job * 384 + tid) * 65536u; if (o < OFF_END) touch(ws, o, scr); }
    else if (job == 2) {
      if (tid < 81) { touch(w1, tid * 65536u, scr); touch(w3, tid * 65536u, scr); touch(w2, tid * 65536u, scr); }
      if (tid < 36) touch(outp, tid * 65536u, scr);
    } else {
      unsigned long long pc; int o0, o1;
      asm volatile("s_getpc_b64 %0\n.Lpc_%=:\n\ts_mov_b32 %1, km_main-.Lpc_%=\n\ts_mov_b32 %2, k_end_marker-.Lpc_%=" : "=&s"(pc), "=&s"(o0), "=&s"(o1));
      const int o = ((job - 3) * 384 + tid) * 128;
      if (o0 > 0 && o < o1 - o0) touch((const void*)pc, (unsigned)(o0 + o) & ~3u, scr);
    }
  } else if (b < K0_D) {
    const int task = (b - K0_C) * 384 + tid;
    const int nh = task % 144, ug = task / 144, u = ug >> 2, g = ug & 3;
    u32x2 r[8];
#pragma unroll
    for (int j = 0; j < 8; ++j) {
      const int krow = (u < 16) ? 36 * u + 18 * (j >> 2) + 4 * g + (j & 3) : 36 * (8 * (u - 16) + 2 * g + (j >> 2)) + 18 * ((j >> 1) & 1) + 16 + (j & 1);
      r[j] = *(const u32x2*)(wo + (size_t)krow * D + nh * 4);
    }
    char* dst = ws + OFF_WO + ((size_t)((nh >> 2) * 18 + u) * 64 + g * 16 + (nh & 3) * 4) * 16;
#pragma unroll
    for (int e = 0; e < 4; ++e) {
      u32x4 f;
#pragma unroll
      for (int i = 0; i < 4; ++i) f[i] = (e & 1) ? perm_hi(r[2 * i][e >> 1], r[2 * i + 1][e >> 1]) : perm_lo(r[2 * i][e >> 1], r[2 * i + 1][e >> 1]);
      *(u32x4*)(dst + e * 16) = f;
    }
    if (task < 64 * 4 * 64 / 2 + 2048) *(u32x4*)(ws + OFF_XCH + (size_t)task * 16) = (u32x4){0u, 0u, 0u, 0u};
  }
  STAMP();
}

constexpr int G1_W3 = 64 * 288 + 128;
constexpr int G1_STAGE = 2 * G1_W3;
#define G1_WAITX(n, xf) asm volatile("s_waitcnt vmcnt(%4)" : "+v"(xf[0]), "+v"(xf[1]), "+v"(xf[2]), "+v"(xf[3]) : "n"(n) : "memory")
constexpr int g2_pos(int n) {
  const int s = n / 9, k = n % 9;
  if (s <= 1) return -1;
  return 18 * (s - 2) + 2 * k;
}
constexpr int g2_op_at(int u) { for (int n = 0; n < 81; ++n) if (g2_pos(n) == u) return n; return -1; }
constexpr int g2_issued_before(int u) { int c = 0; for (int n = 0; n < 81; ++n) if (g2_pos(n) < u) ++c; return c; }
template <int IMM, class T> __device__ __forceinline__ void gload16_sc1(T& dst, unsigned voff, const void* sbase) {
  asm volatile("global_load_dwordx4 %0, %1, %2 offset:%3 sc1" : "=v"(dst) : "v"(voff), "s"(sbase), "n"(IMM) : "memory");
}
template <int IMM> __device__ __forceinline__ void gstore16_sc1(const u32x4& v, unsigned voff, const void* sbase) {
  asm volatile("global_store_dwordx4 %0, %1, %2 offset:%3 sc1\n\ts_nop 1" :: "v"(voff), "v"(v), "s"(sbase), "n"(IMM) : "memory");
}
__device__ __forceinline__ void gload8_sc1(u32x2& dst, unsigned voff, const void* sbase) {
  asm volatile("global_load_dwordx2 %0, %1, %2 sc1" : "=v"(dst) : "v"(voff), "s"(sbase) : "memory");
}
__device__ __forceinline__ void gstore8_sc1(const u32x2& v, unsigned voff, const void* sbase) {
  asm volatile("global_store_dwordx2 %0, %1, %2 sc1\n\ts_nop 1" :: "v"(voff), "v"(v), "s"(sbase) : "memory");
}
template <int N> __device__ __forceinline__ void wait4(u32x4 (&x)[4]) {
  asm volatile("s_waitcnt vmcnt(%4)" : "+v"(x[0]), "+v"(x[1]), "+v"(x[2]), "+v"(x[3]) : "n"(N) : "memory");
}
template <int N> __device__ __forceinline__ void wait_flag(unsigned& f) { asm volatile("s_waitcnt vmcnt(%1)" : "+v"(f) : "n"(N) : "memory"); }
__device__ __forceinline__ void pinu(unsigned& a, unsigned& b) { asm volatile("" : "+v"(a), "+v"(b)); }
__device__ __forceinline__ void atomic_inc(GAS unsigned* p) {
  const unsigned one = 1u, zero = 0u;
  asm volatile("global_atomic_add %0, %1, %2" :: "v"(zero), "v"(one), "s"(p) : "memory");
}
__device__ __forceinline__ void arrive(LAS char* ctr, GAS unsigned* cnt, int lane) {
  if (lane == 0) { if (__hip_atomic_fetch_add((LAS unsigned*)ctr, 1u, __ATOMIC_RELAXED, __HIP_MEMORY_SCOPE_WORKGROUP) == 7u) atomic_inc(cnt); }
}
__device__ __forceinline__ void gload4_sc1(unsigned& dst, const GAS unsigned* p) {
  const unsigned zero = 0u;
  asm volatile("global_load_dword %0, %1, %2 sc1" : "=v"(dst) : "v"(zero), "s"(p) : "memory");
}
__device__ __forceinline__ void wait_count(const GAS unsigned* cnt, unsigned want) {
  for (unsigned spin = 0; spin < (1u << 16); ++spin) {
    if (__builtin_amdgcn_readfirstlane(__hip_atomic_load(cnt, __ATOMIC_RELAXED, __HIP_MEMORY_SCOPE_AGENT)) >= want) break;
    __builtin_amdgcn_s_sleep(1);
  }
}

constexpr int L_CONST = 4 * G1_STAGE;
constexpr int L_C2 = L_CONST + 8192;
constexpr int L_ST = L_C2 + 2304;
constexpr int L_FIN = L_ST + 2304;
constexpr int L_SCR = L_FIN + 256;
constexpr int G1_ARR = L_SCR + 256;
constexpr int KM_LDS = G1_ARR + 32;
constexpr int K1_EK = 0, K1_EV = 9 * 2 * 1024, K1_EL = K1_EV + 9 * 2 * 512;
constexpr int K1_O = 3 * G1_STAGE;
constexpr int k1_r(int p) { return p < 0 ? 0 : (21 * (p + 1) + 7) / 8; }
constexpr int k1_cum(int p) { return 9 + 3 * (p + 1 < 5 ? p + 1 : 5) + k1_r(p); }
constexpr int K1_NOPS = 41 + 29;
constexpr int k1_op(int n) {
  if (n < 3) return 96;
  if (n < 6) return 97 + n - 3;
  if (n < 9) return n - 6;
  for (int p = 0; p < 6; ++p) {
    const int lo = p ? k1_cum(p - 1) : 9;
    if (n < k1_cum(p)) { const int k = n - lo; return (p < 5 && k < 3) ? 3 * p + 3 + k : 32 + k1_r(p - 1) + k - (p < 5 ? 3 : 0); }
  }
  return n == 40 ? 100 : 64 + n - 41;
}
constexpr int k1_pos(int n) {
  if (n < k1_cum(0)) return -1;
  if (n < k1_cum(1)) return 0;
  if (n < k1_cum(2)) return 1 + (n - k1_cum(1)) / 3;
  for (int p = 3; p < 6; ++p) if (n < k1_cum(p)) return 3 * (p - 2) + (n - k1_cum(p - 1)) / 2;
  if (n == 40) return 10;
  const int k = n - 41;
  if (k < 6) return 15 + k / 2;
  if (k < 9) return 18;
  if (k < 15) return 19 + (k - 9) / 3;
  return 21;
}
constexpr int k1_before(int s) { int c = 0; for (int n = 0; n < K1_NOPS; ++n) if (k1_pos(n) < s) ++c; return c; }
static_assert(k1_cum(5) == 40 && k1_before(22) == K1_NOPS, "");
struct Cst { f32x4 s0, s1, g0, g1; };
__device__ __forceinline__ void rdc(Cst& c, const LAS float* cs) {
  c.s0 = *(const LAS f32x4*)cs; c.s1 = *(const LAS f32x4*)(cs + 4); c.g0 = *(const LAS f32x4*)(cs + 576); c.g1 = *(const LAS f32x4*)(cs + 580);
}
__device__ __forceinline__ float dot2(unsigned x, float gw, float s) {
  float r; asm("v_dot2_f32_bf16 %0, %1, %2, %3" : "=v"(r) : "v"(x), "v"(gw), "v"(s)); return r;
}
__device__ __forceinline__ void modc(u32x4& xv, const Cst& c) {
  asm volatile("" : "+v"(xv));
  const u32x4 v = xv;
  float h[8];
  h[0] = dot2(v[0], c.g0[0], c.s0[0]); h[1] = dot2(v[0], c.g0[1], c.s0[1]); h[2] = dot2(v[1], c.g0[2], c.s0[2]); h[3] = dot2(v[1], c.g0[3], c.s0[3]);
  h[4] = dot2(v[2], c.g1[0], c.s1[0]); h[5] = dot2(v[2], c.g1[1], c.s1[1]); h[6] = dot2(v[3], c.g1[2], c.s1[2]); h[7] = dot2(v[3], c.g1[3], c.s1[3]);
  asm volatile("s_nop 2" : "+v"(h[0]), "+v"(h[1]), "+v"(h[2]), "+v"(h[3]), "+v"(h[4]), "+v"(h[5]), "+v"(h[6]), "+v"(h[7]));
  u32x4 r;
  r[0] = pk_bf16(h[0], h[1]); r[1] = pk_bf16(h[2], h[3]); r[2] = pk_bf16(h[4], h[5]); r[3] = pk_bf16(h[6], h[7]);
  pad2(r);
  xv = r;
}
template <int N> __device__ __forceinline__ void wait3(u32x4& a, u32x4& b, u32x4& c) {
  asm volatile("s_waitcnt vmcnt(%3)" : "+v"(a), "+v"(b), "+v"(c) : "n"(N) : "memory");
}
__device__ __forceinline__ void pin(u32x4& a) { asm volatile("" : "+v"(a)); }

__device__ __forceinline__ void resid_stats(const f32x4 a, const u32x2 xin, const f32x4 ga, float (&v)[4], u32x2& xo, f32x2& sq) {
#pragma unroll
  for (int j = 0; j < 4; ++j) v[j] = a[j];
  rb2(v[0], v[1]); rb2(v[2], v[3]);
#pragma unroll
  for (int j = 0; j < 4; ++j) v[j] *= ga[j];
  rb2(v[0], v[1]); rb2(v[2], v[3]);
  const unsigned p0 = pk_bf16(v[0] + bf_lo(xin[0]), v[1] + bf_hi(xin[0])), p1 = pk_bf16(v[2] + bf_lo(xin[1]), v[3] + bf_hi(xin[1]));
  v[0] = bf_lo(p0); v[1] = bf_hi(p0); v[2] = bf_lo(p1); v[3] = bf_hi(p1);
  const float mw = sum32(sum16((v[0] + v[1]) + (v[2] + v[3]))) * (1.f / 16.f);
  const float d0 = v[0] - mw, d1 = v[1] - mw, d2 = v[2] - mw, d3 = v[3] - mw;
  sq = (f32x2){mw, sum32(sum16((d0 * d0 + d1 * d1) + (d2 * d2 + d3 * d3)))};
  xo = (u32x2){p0, p1};
}
__device__ __forceinline__ u32x2 norm_mod(const float (&v)[4], const f32x2 f, const f32x4 g2, const f32x4 sb) {
  float h[4];
#pragma unroll
  for (int j = 0; j < 4; ++j) h[j] = v[j] - f[0];
  rb2(h[0], h[1]); rb2(h[2], h[3]);
#pragma unroll
  for (int j = 0; j < 4; ++j) h[j] *= f[1];
  rb2(h[0], h[1]); rb2(h[2], h[3]);
#pragma unroll
  for (int j = 0; j < 4; ++j) h[j] *= g2[j];
  rb2(h[0], h[1]); rb2(h[2], h[3]);
  return (u32x2){pk_bf16(h[0] + sb[0], h[1] + sb[1]), pk_bf16(h[2] + sb[2], h[3] + sb[3])};
}

extern "C" __global__ __launch_bounds__(512, 2) void km_main(char* __restrict__ ws, const u16* __restrict__ x, const u16* __restrict__ w1,
                                                  const u16* __restrict__ w3, const u16* __restrict__ w2, u16* __restrict__ out) {
  extern __shared__ __attribute__((aligned(16))) char smem[];
  LAS char* lds = (LAS char*)smem;
  const unsigned lbase = (unsigned)(size_t)lds;
  const int b = blockIdx.x, xcd = b & 7, idx = b >> 3;
  const int tid = threadIdx.x, lane = tid & 63;
  const int w = __builtin_amdgcn_readfirstlane(tid >> 6);
  const unsigned xvo = lane * 16;
  const int ksl = xcd >> 1;
  const int mt = (xcd & 1) * 4 + (idx & 3), ct = ksl * 8 + (idx >> 2);
  const int mt2 = (xcd & 1) * 8 + (idx & 7), nq = idx >> 3;
  const int mq = w & 3, kg = w >> 2;
  const char* hhb = ws + OFF_HH;
  STAMP_DECL; STAMP();
  if (tid == 0) *(LAS u32x4*)(lds + G1_ARR) = (u32x4){0u, 0u, 0u, 0u};
  unsigned so[5];
  const char* wsrc = (const char*)((w < 4) ? w1 : w3) + (size_t)((w & 3) * 16) * (F * 2) + ct * 288;
  const unsigned wdst = lbase + (w >> 2) * G1_W3 + (w & 3) * 4608;
#define G1_G(st, i) do { const char* _s = wsrc + (size_t)(st) * (64 * F * 2); const unsigned _d = wdst + ((st) & 3) * G1_STAGE; \
    if ((i) < 4) glds16(_s, so[i], _d + (i) * 1024); else if (lane < 32) glds16(_s, so[4], _d + 4096); } while (0)
  {
  const int rt = (xcd & 3) * 4 + (idx & 3), hp = (xcd >> 2) * 8 + (idx >> 2);
  const int li = lane & 15, g = lane >> 4;
  const unsigned l16 = xvo;
  const char* xsrc = ws + OFF_XN + (size_t)(8 * rt + w) * (18 * 1024);
  const char* hsrc = ws + OFF_XN + (size_t)(rt > 0 ? 8 * rt - 2 : 0) * (18 * 1024);
  const unsigned hvo = (li >> 3) * (18 * 1024) + (g * 16 + 8 + (li & 7)) * 16;
  const char* wq = ws + OFF_WQ + (size_t)hp * (126 * 1024);
  const int rt2 = 4 * rt + (hp >> 2), nq2 = hp & 3;
  const int nt = nq2 * 9 + w;
  const char* wsrc2 = ws + OFF_WO + (size_t)nt * (18 * 1024);
  const char* wsrc8 = ws + OFF_WO + (size_t)((nq2 * 9 + 8) * 18 + (w >> 1)) * 1024;
  u32x4 xf[18], wo[18], wo8[5], hx[3];
  u32x2 xr[3]; f32x4 mq3[3];
  auto issue = [&](auto N) {
    constexpr int op = k1_op(decltype(N)::value);
    if constexpr (op < 32) { const char* q = xsrc + (op & ~3) * 1024; gload16<(op & 3) * 1024>(xf[op], l16, q); }
    else if constexpr (op < 64) {
      int p = w + 8 * (op - 32); p = p < 126 ? p : 125;
      const int sk = (p * 9363) >> 16;
      glds16(wq + p * 1024, l16, lbase + (p + sk) * 1024);
    }
    else if constexpr (op < 82) { constexpr int i = op - 64; const char* q = wsrc2 + (i & ~3) * 1024; gload16<(i & 3) * 1024>(wo[i], l16, q); }
    else if constexpr (op < 87) { constexpr int i = op - 82; gload16<0>(wo8[i], l16, wsrc8 + ((i < 4 || w < 4) ? i : 0) * 4096); }
    else if constexpr (op < 89) gload8<(op - 87) * 1152>(xr[op - 87], (unsigned)(((rt2 * 32 + 2 * li) * D + nt * 16 + 4 * g) * 2), x);
    else if constexpr (op == 89) gload8<0>(xr[2], (unsigned)(((rt2 * 32 + 2 * li + (w & 1)) * D + nq2 * 144 + 128 + 4 * g) * 2), x);
    else if constexpr (op < 93) {
      const int t = tid < 144 ? tid : 143, vec = t / 36, c4 = t - vec * 36;
      gload16<0>(mq3[op - 90], (unsigned)(((2 + vec) * D + (vec == 3 ? nq : nq2) * 144 + c4 * 4) * 4), ws + OFF_MOD + (op - 90) * 3456 * 4);
    }
    else if constexpr (op < 100) { const int sk = w + 8 * (op - 97); gload16<0>(hx[op - 97], hvo, hsrc + (sk < 18 ? sk : 17) * 1024); }
    else {
      const int ln = idx * 108 + (w & 3) * 27 + (lane < 27 ? lane : 26);
      touch((w & 4) ? w3 : w1, (ln / 18) * (F * 2) + ksl * 2304 + (ln % 18) * 128, lbase + L_SCR);
    }
  };
  f32x4 mp[3];
  {
    const unsigned mo = (tid < 288 ? tid : 287) * 16;
    gload16<0>(mp[0], mo, ws + OFF_MOD); gload16<0>(mp[1], mo, ws + OFF_MOD + 3456 * 4); gload16<0>(mp[2], mo, ws + OFF_MOD + 6912 * 4);
  }
  static_for<3, k1_cum(0)>(issue);
  asm volatile("s_waitcnt vmcnt(%3)" : "+v"(mp[0]), "+v"(mp[1]), "+v"(mp[2]) : "n"(k1_cum(0) - 3) : "memory");
  if (tid < 288) {
    float m[4];
#pragma unroll
    for (int j = 0; j < 4; ++j) m[j] = (mp[0][j] + mp[1][j]) + mp[2][j];
    rb2(m[0], m[1]); rb2(m[2], m[3]);
    if (tid >= 144) {
#pragma unroll
      for (int j = 0; j < 4; ++j) m[j] += 1.f;
      rb2(m[0], m[1]); rb2(m[2], m[3]);
      m[0] = __uint_as_float(__float_as_uint(m[0]) >> 16); m[2] = __uint_as_float(__float_as_uint(m[2]) >> 16);
    }
    *(LAS f32x4*)(lds + L_CONST + tid * 16) = (f32x4){m[0], m[1], m[2], m[3]};
  }
  WAIT_L(0);
  BAR();
  const LAS float* cs = (const LAS float*)(lds + L_CONST) + g * 8;
  Cst cst;
  {
    Cst c1, c2;
    const int s2 = w + 16 < 18 ? w + 16 : 17;
    rdc(cst, cs + w * 32); rdc(c1, cs + (w + 8) * 32); rdc(c2, cs + s2 * 32);
    wait3<k1_cum(0) - 6>(hx[0], hx[1], hx[2]);
    modc(hx[0], cst); modc(hx[1], c1); modc(hx[2], c2);
    *(LAS u32x4*)(lds + w * 8192 + 7168 + l16) = hx[0];
    *(LAS u32x4*)(lds + (w + 8) * 8192 + 7168 + l16) = hx[1];
    *(LAS u32x4*)(lds + s2 * 8192 + 7168 + l16) = hx[2];
    rdc(cst, cs); rdc(c1, cs + 32); rdc(c2, cs + 64);
    wait3<k1_cum(0) - 9>(xf[0], xf[1], xf[2]);
    modc(xf[0], cst); modc(xf[1], c1); modc(xf[2], c2);
  }
  rdc(cst, cs + 96);
  f32x4 acc[8];
#pragma unroll
  for (int t = 0; t < 8; ++t) { acc[t] = (f32x4){0.f, 0.f, 0.f, 0.f}; asm volatile("" : "+v"(acc[t])); }
  const bool hw = w < 5;
  unsigned wa = lbase + l16, ha = wa + (w < 3 ? w * 1024 : 7168), hb = wa + (w < 3 ? 7168 : w * 1024);
  s16x8 wf[7], hf[2];
  auto rdw = [&](auto Sx, auto T0, auto T1) {
    constexpr int s = decltype(Sx)::value, off = (s % 6) * 8192;
#pragma unroll
    for (int t = decltype(T0)::value; t < decltype(T1)::value; ++t) wf[t] = *(const LAS s16x8*)(size_t)(wa + off + t * 1024);
  };
  auto rdh = [&](auto Sx) {
    constexpr int off = (decltype(Sx)::value % 6) * 8192;
    hf[0] = *(const LAS s16x8*)(size_t)(ha + off); hf[1] = *(const LAS s16x8*)(size_t)(hb + off);
  };
  asm volatile("s_waitcnt vmcnt(0) lgkmcnt(0)" : "+v"(xf[3]), "+v"(xf[4]), "+v"(xf[5]) :: "memory");
  BAR();
  STAMP();
  rdw(IC<0>{}, IC<0>{}, IC<7>{});
  rdh(IC<0>{});
  static_for<0, 18>([&](auto Sx) {
    constexpr int s = decltype(Sx)::value, p = s / 3;
    if constexpr (s % 3 == 2 && p < 5) {
      if constexpr (p < 4) wait3<k1_before(s) - k1_cum(p + 1)>(xf[3 * p + 6], xf[3 * p + 7], xf[3 * p + 8]);
      else WAIT_V(k1_before(s) - k1_cum(5));
      BAR();
    }
    if constexpr (s == 5 || s == 11) { wa += 6 * 8192; ha += 6 * 8192; hb += 6 * 8192; asm volatile("" : "+v"(wa)); pinu(ha, hb); }
    if constexpr (s == 0) static_for<k1_before(0), k1_before(1)>(issue);
    MFMA16A(wf[0], xf[s], acc[0]); MFMA16A(wf[1], xf[s], acc[1]); MFMA16A(wf[2], xf[s], acc[2]); MFMA16A(xf[s], wf[3], acc[3]);
    if constexpr (s < 17) rdw(IC<s + 1>{}, IC<0>{}, IC<4>{});
    if constexpr (s + 3 < 18) modc(xf[s + 3], cst);
    if constexpr (s + 4 < 18) rdc(cst, cs + (s + 4) * 32);
    MFMA16A(xf[s], wf[4], acc[4]); MFMA16A(wf[5], xf[s], acc[5]); MFMA16A(wf[6], xf[s], acc[6]);
    if constexpr (s < 17) rdw(IC<s + 1>{}, IC<4>{}, IC<7>{});
    if (hw) { MFMA16A(hf[0], hf[1], acc[7]); if constexpr (s < 17) rdh(IC<s + 1>{}); }
    if constexpr (s > 0) static_for<k1_before(s), k1_before(s + 1)>(issue);
  });
  STAMP();
  asm volatile("s_nop 7" : "+v"(acc[0]), "+v"(acc[1]), "+v"(acc[2]), "+v"(acc[3]), "+v"(acc[4]), "+v"(acc[5]), "+v"(acc[6]), "+v"(acc[7]) :: "memory");
  unsigned kf[2][2], qf[2][2], vf[2][2], lf[2];
#pragma unroll
  for (int h = 0; h < 2; ++h) {
    kf[h][0] = pk_bf16(acc[h][0], acc[h][1]); kf[h][1] = pk_bf16(acc[h][2], acc[h][3]);
    qf[h][0] = pk_bf16(acc[5 + h][0], acc[5 + h][1]); qf[h][1] = pk_bf16(acc[5 + h][2], acc[5 + h][3]);
    vf[h][0] = pk_bf16(acc[3 + h][0], acc[3 + h][1]); vf[h][1] = pk_bf16(acc[3 + h][2], acc[3 + h][3]);
  }
  lf[0] = pk_bf16(acc[2][0], acc[2][1]); lf[1] = pk_bf16(acc[2][2], acc[2][3]);
#pragma unroll
  for (int h = 0; h < 2; ++h) {
    *(LAS u32x4*)(lds + K1_EK + (w * 2 + h) * 1024 + l16) = (u32x4){kf[h][0], kf[h][1], lf[1], 0u};
    *(LAS u32x2*)(lds + K1_EV + (w * 2 + h) * 512 + lane * 8) = (u32x2){vf[h][0], vf[h][1]};
  }
  if (g == 2) *(LAS u32x2*)(lds + K1_EL + (w * 16 + li) * 8) = (u32x2){lf[0], lf[1]};
  {
    const u32x2 hv = (u32x2){pk_bf16(acc[7][0], acc[7][1]), pk_bf16(acc[7][2], acc[7][3])};
    if (w < 2) *(LAS u32x2*)(lds + K1_EK + (16 + w) * 1024 + l16) = hv;
    else if (w == 2) {
      *(LAS u32x2*)(lds + K1_EK + 16 * 1024 + l16 + 8) = (u32x2){hv[1], 0u};
      *(LAS u32x2*)(lds + K1_EK + 17 * 1024 + l16 + 8) = (u32x2){hv[1], 0u};
      if (g == 2) *(LAS u32x2*)(lds + K1_EL + (128 + li) * 8) = hv;
    } else if (w < 5) *(LAS u32x2*)(lds + K1_EV + (16 + w - 3) * 512 + lane * 8) = hv;
  }
  static_for<k1_before(18), k1_before(19)>(issue);
  WAIT_L(0);
  BAR();
  STAMP();
  {
    const int par = w & 1;
    const bool first = (w < 2);
    const int pw = first ? 8 : w - 2;
    const int pki = first ? 8 * par + (li & 7) : li;
    const int pvg = first ? 2 * par + (g & 1) : g;
    const int q4 = li >> 2;
    const int plk = first ? 8 * par + ((4 * g + q4) & 7) : 4 * g + q4;
    const s16x8 vl = cat8(tr_read(lds + K1_EL + (pw * 16 + plk) * 8), tr_read(lds + K1_EL + (w * 16 + 4 * g + q4) * 8));
    const bool prev_ok = !(first && rt == 0);
    unsigned op[2][2], ol[2];
    bool usep[4], useo[4];
#pragma unroll
    for (int j = 0; j < 4; ++j) { const int kk = 4 * g + j; usep[j] = prev_ok && (kk >= li + 9); useo[j] = (kk <= li) && (li - kk <= 7); }
    const unsigned mp01 = (usep[0] ? 0xffffu : 0u) | (usep[1] ? 0xffff0000u : 0u), mp23 = (usep[2] ? 0xffffu : 0u) | (usep[3] ? 0xffff0000u : 0u);
    static_for<k1_before(19), k1_before(20)>(issue);
#pragma unroll
    for (int h = 0; h < 2; ++h) {
      const u32x4 kp = *(const LAS u32x4*)(lds + K1_EK + (pw * 2 + h) * 1024 + (g * 16 + pki) * 16);
      const u32x2 vp = *(const LAS u32x2*)(lds + K1_EV + (pw * 2 + h) * 512 + (pvg * 16 + li) * 8);
      const u32x4 ko = (u32x4){kf[h][0], kf[h][1], lf[1], 0u};
      const u32x4 qq = (u32x4){qf[h][0], qf[h][1], (g == h) ? lf[0] : 0u, 0u};
      const f32x4 z = (f32x4){0.f, 0.f, 0.f, 0.f};
      const f32x4 so = __builtin_amdgcn_mfma_f32_16x16x32_bf16(__builtin_bit_cast(s16x8, ko), __builtin_bit_cast(s16x8, qq), z, 0, 0, 0);
      const f32x4 sp = __builtin_amdgcn_mfma_f32_16x16x32_bf16(__builtin_bit_cast(s16x8, kp), __builtin_bit_cast(s16x8, qq), z, 0, 0, 0);
      float lg[4];
#pragma unroll
      for (int j = 0; j < 4; ++j) lg[j] = usep[j] ? sp[j] : so[j];
      rb2(lg[0], lg[1]); rb2(lg[2], lg[3]);
#pragma unroll
      for (int j = 0; j < 4; ++j) lg[j] *= 0.23570226f;
      rb2(lg[0], lg[1]); rb2(lg[2], lg[3]);
#pragma unroll
      for (int j = 0; j < 4; ++j) lg[j] = (usep[j] || useo[j]) ? lg[j] : -3.0e38f;
      const float mx = max32(max16(fmaxf(fmaxf(lg[0], lg[1]), fmaxf(lg[2], lg[3]))));
#pragma unroll
      for (int j = 0; j < 4; ++j) lg[j] = __builtin_amdgcn_exp2f((lg[j] - mx) * 1.44269504f);
      const float inv = __builtin_amdgcn_rcpf(sum32(sum16((lg[0] + lg[1]) + (lg[2] + lg[3]))));
      const unsigned p01 = pk_bf16(lg[0] * inv, lg[1] * inv), p23 = pk_bf16(lg[2] * inv, lg[3] * inv);
      const u32x4 pf = (u32x4){p01 & mp01, p23 & mp23, p01 & ~mp01, p23 & ~mp23};
      const u32x4 vv = (u32x4){vp[0], vp[1], vf[h][0], vf[h][1]};
      const f32x4 oo = __builtin_amdgcn_mfma_f32_16x16x32_bf16(__builtin_bit_cast(s16x8, vv), __builtin_bit_cast(s16x8, pf), z, 0, 0, 0);
      const f32x4 lo = __builtin_amdgcn_mfma_f32_16x16x32_bf16(vl, __builtin_bit_cast(s16x8, pf), z, 0, 0, 0);
      op[h][0] = pk_bf16(oo[0], oo[1]); op[h][1] = pk_bf16(oo[2], oo[3]);
      ol[h] = pk_bf16(lo[2 * h], lo[2 * h + 1]);
    }
    static_for<k1_before(20), k1_before(21)>(issue);
    const char* ob = ws + OFF_O + (size_t)(8 * rt + w) * (18 * 1024);
    const u32x4 o4 = (u32x4){op[0][0], op[0][1], op[1][0], op[1][1]};
    const unsigned oo0 = hp * 1024 + l16;
    asm volatile("global_store_dwordx4 %0, %1, %2 sc1\n\ts_nop 1" :: "v"(oo0), "v"(o4), "s"(ob) : "memory");
    if (g == 0) {
      const u32x2 o2 = (u32x2){ol[0], ol[1]};
      const unsigned oo1 = (16 + (hp >> 3)) * 1024 + (((hp & 7) >> 1) * 16 + li) * 16 + 8 * (hp & 1);
      asm volatile("global_store_dwordx2 %0, %1, %2 sc1\n\ts_nop 1" :: "v"(oo1), "v"(o2), "s"(ob) : "memory");
    }
  }
  STAMP();
  WAIT_V(0);
  GAS unsigned* cnt1 = (GAS unsigned*)(ws + OFF_CNT) + rt * 64;
  arrive(lds + G1_ARR + 8, cnt1, lane);
  static_for<k1_before(21), K1_NOPS>(issue);
#pragma unroll
  for (int i = 0; i < 5; ++i) {
    const int ci = i * 64 + lane;
    const int r = ci / 18, ch = ci - r * 18;
    so[i] = r * (F * 2) + ch * 16;
  }
  if (w == 7) wait_count(cnt1, 16u);
  BAR();
  STAMP();
  {
    const char* osrc = ws + OFF_O + (size_t)rt2 * (36 * 1024) + w * 4608;
#pragma unroll
    for (int i = 0; i < 4; ++i) glds16_sc1(osrc + i * 1024, l16, lbase + K1_O + w * 4608 + i * 1024);
    if (lane < 32) glds16_sc1(osrc + 4096, l16, lbase + K1_O + w * 4608 + 4096);
  }
  asm volatile("s_waitcnt vmcnt(5)" : "+v"(xr[0]), "+v"(xr[1]), "+v"(xr[2]), "+v"(mq3[0]), "+v"(mq3[1]), "+v"(mq3[2]) :: "memory");
  static_for<0, 18>([&](auto I) { pin(wo[decltype(I)::value]); });
  static_for<0, 5>([&](auto I) { pin(wo8[decltype(I)::value]); });
  if (tid < 144) {
    float t[4];
#pragma unroll
    for (int j = 0; j < 4; ++j) t[j] = (mq3[0][j] + mq3[1][j]) + mq3[2][j];
    rb2(t[0], t[1]); rb2(t[2], t[3]);
    if (tid >= 72 && tid < 108) {
#pragma unroll
      for (int j = 0; j < 4; ++j) t[j] += 1.f;
      rb2(t[0], t[1]); rb2(t[2], t[3]);
    }
    *(LAS f32x4*)(lds + L_C2 + tid * 16) = (f32x4){t[0], t[1], t[2], t[3]};
  }
  asm volatile("s_waitcnt vmcnt(0) lgkmcnt(0)" ::: "memory");
  BAR();
  STAMP();
  f32x4 ac2[3];
  ac2[0] = (f32x4){0.f, 0.f, 0.f, 0.f}; ac2[1] = ac2[0]; ac2[2] = ac2[0];
  {
    unsigned oa = lbase + K1_O + l16, o8 = oa + ((w & 1) * 18 + (w >> 1)) * 1024;
    pinu(oa, o8);
    s16x8 of[2][6];
    auto rdo = [&](auto Cx) {
      constexpr int c = decltype(Cx)::value;
#pragma unroll
      for (int i = 0; i < 3; ++i) { of[c & 1][2 * i] = *(const LAS s16x8*)(size_t)(oa + (3 * c + i) * 1024); of[c & 1][2 * i + 1] = *(const LAS s16x8*)(size_t)(oa + (18 + 3 * c + i) * 1024); }
    };
    s16x8 o8f[5];
#pragma unroll
    for (int i = 0; i < 5; ++i) o8f[i] = *(const LAS s16x8*)(size_t)(o8 + ((i < 4 || w < 4) ? i : 0) * 4096);
    rdo(IC<0>{});
#pragma unroll
    for (int i = 0; i < 4; ++i) ac2[2] = __builtin_amdgcn_mfma_f32_16x16x32_bf16(__builtin_bit_cast(s16x8, wo8[i]), o8f[i], ac2[2], 0, 0, 0);
    if (w < 4) ac2[2] = __builtin_amdgcn_mfma_f32_16x16x32_bf16(__builtin_bit_cast(s16x8, wo8[4]), o8f[4], ac2[2], 0, 0, 0);
    static_for<0, 6>([&](auto Cx) {
      constexpr int c = decltype(Cx)::value;
      if constexpr (c < 5) rdo(IC<c + 1>{});
      if constexpr (c == 1) *(LAS f32x4*)(lds + L_CONST + w * 1024 + l16) = ac2[2];
#pragma unroll
      for (int i = 0; i < 3; ++i) {
        ac2[0] = __builtin_amdgcn_mfma_f32_16x16x32_bf16(__builtin_bit_cast(s16x8, wo[3 * c + i]), of[c & 1][2 * i], ac2[0], 0, 0, 0);
        ac2[1] = __builtin_amdgcn_mfma_f32_16x16x32_bf16(__builtin_bit_cast(s16x8, wo[3 * c + i]), of[c & 1][2 * i + 1], ac2[1], 0, 0, 0);
      }
      SB0();
    });
  }
  WAIT_L(0);
  BAR();
  const LAS float* c2 = (const LAS float*)(lds + L_C2);
  float v[3][4];
  f32x2 sq[3];
  LAS f32x2* st = (LAS f32x2*)(lds + L_ST);
  LAS f32x2* fin = (LAS f32x2*)(lds + L_FIN);
  {
    const f32x4 ga = *(const LAS f32x4*)(c2 + w * 16 + 4 * g);
    resid_stats(ac2[0], xr[0], ga, v[0], xr[0], sq[0]);
    resid_stats(ac2[1], xr[1], ga, v[1], xr[1], sq[1]);
    if (g < 2) st[w * 32 + g * 16 + li] = g ? sq[1] : sq[0];
    if (w < 2) {
      const LAS f32x4* t8 = (const LAS f32x4*)(lds + L_CONST + w * 1024 + l16);
      const f32x4 a8 = (t8[0] + t8[128]) + (t8[256] + t8[384]);
      resid_stats(a8, xr[2], *(const LAS f32x4*)(c2 + 128 + 4 * g), v[2], xr[2], sq[2]);
      if (g == 0) st[8 * 32 + w * 16 + li] = sq[2];
    }
  }
  WAIT_L(0);
  BAR();
  STAMP();
  if (w == 7) {
    const int r = lane & 31;
    float mt_ = 0.f;
    f32x2 sv[9];
#pragma unroll
    for (int k = 0; k < 9; ++k) { sv[k] = st[k * 32 + r]; mt_ += sv[k][0]; }
    mt_ *= (1.f / 9.f);
    float m2 = 0.f;
#pragma unroll
    for (int k = 0; k < 9; ++k) { const float dl = sv[k][0] - mt_; m2 += sv[k][1] + 16.f * dl * dl; }
    GAS unsigned long long* xch = (GAS unsigned long long*)(ws + OFF_XCH) + (size_t)rt2 * 256;
    const float mine = (lane < 32) ? mt_ : m2;
    __hip_atomic_store(xch + nq2 * 64 + lane, ((unsigned long long)XCH_TAG << 32) | __float_as_uint(mine), __ATOMIC_RELAXED, __HIP_MEMORY_SCOPE_AGENT);
    float o[4];
    for (unsigned spin = 0; spin < (1u << 16); ++spin) {
      bool ok = true;
#pragma unroll
      for (int k = 0; k < 4; ++k) {
        const unsigned long long gv = __hip_atomic_load(xch + k * 64 + lane, __ATOMIC_RELAXED, __HIP_MEMORY_SCOPE_AGENT);
        o[k] = __uint_as_float((unsigned)gv); ok &= ((unsigned)(gv >> 32) == XCH_TAG);
      }
      if (__all(ok)) break;
      __builtin_amdgcn_s_sleep(1);
    }
    const float mean = 0.25f * ((o[0] + o[1]) + (o[2] + o[3]));
    float t = 0.f;
#pragma unroll
    for (int k = 0; k < 4; ++k) { const float dl = o[k] - mean; t += 144.f * dl * dl; }
    const auto sw = __builtin_amdgcn_permlane32_swap(__float_as_uint(mean), __float_as_uint(mean), false, false);
    const float m2s = 4.f * __uint_as_float(sw[1]);
    if (lane < 32) {
      const float var = (t + m2s) * (1.f / D);
      fin[r] = (f32x2){rb(mean), __builtin_amdgcn_rcpf(rb(__builtin_amdgcn_sqrtf(rb(rb(var) + 1e-5f))))};
    }
  }
  {
    const char* x1b = ws + OFF_X1 + (size_t)(rt2 * 72 + nq2 * 9) * 512;
    gstore8_sc1(xr[0], lane * 8 + w * 512, x1b); gstore8_sc1(xr[1], lane * 8 + (36 + w) * 512, x1b);
    if (w < 2) gstore8_sc1(xr[2], lane * 8 + (w * 36 + 8) * 512, x1b);
  }
  const f32x4 sb = *(const LAS f32x4*)(c2 + 144 + w * 16 + 4 * g), g2 = *(const LAS f32x4*)(c2 + 288 + w * 16 + 4 * g);
  WAIT_L(0);
  BAR();
  STAMP();
  {
    const char* h2b = ws + OFF_H2 + (size_t)(rt2 * 36 + nq2 * 9) * 1024;
    const unsigned ho = ((g >> 1) * 32 + li) * 16 + (g & 1) * 8;
    gstore8_sc1(norm_mod(v[0], fin[li], g2, sb), ho + w * 1024, h2b);
    gstore8_sc1(norm_mod(v[1], fin[16 + li], g2, sb), ho + w * 1024 + 256, h2b);
    if (w < 2) gstore8_sc1(norm_mod(v[2], fin[w * 16 + li], *(const LAS f32x4*)(c2 + 288 + 128 + 4 * g), *(const LAS f32x4*)(c2 + 144 + 128 + 4 * g)), ho + 8 * 1024 + w * 256, h2b);
  }
  WAIT_V(0);
  arrive(lds + G1_ARR + 12, (GAS unsigned*)(ws + OFF_CNT) + (48 + (rt2 >> 3)) * 64, lane);
  STAMP();
#pragma unroll
  for (int i = 0; i < 10; ++i) G1_G(i / 5, i % 5);
  }
  const GAS unsigned* cntw = (const GAS unsigned*)(ws + OFF_CNT) + (16 + (mt2 >> 1) * 4 + ksl) * 64;
  unsigned flagv;
  GAS unsigned* cntB;
  const char* wsrc2 = (const char*)w2 + (size_t)(ksl * 1152 + (w >> 2) * 144 + (w & 3) * 16) * (D * 2) + nq * 288;
  const char* wsrc2b = (const char*)w2 + (size_t)(ksl * 1152 + w * 144 + 128) * (D * 2) + nq * 288;
  const unsigned wdst2 = lbase + w * 4608;
#define G2_G(st, i) do { const char* _s = ((st) < 8) ? wsrc2 + (size_t)(((st) & 3) * 288 + ((st) >> 2) * 64) * (D * 2) : wsrc2b; const unsigned _d = wdst2 + (((st) + 1) & 3) * G1_STAGE; \
    if ((i) < 4) glds16(_s, so[i], _d + (i) * 1024); else if (lane < 32) glds16(_s, so[4], _d + 4096); } while (0)
  {
    const char* xsrc = ws + OFF_H2 + (size_t)(mt * 8 + w) * (36 * 1024);
    u32x4 xa[4], xb[4], xc[4];
#define G1_X(dst, st, i) do { const char* _p = xsrc + (st) * 4096; asm volatile("global_load_dwordx4 %0, %1, %2 offset:%3 sc1" : "=v"(dst[i]) : "v"(xvo), "s"(_p), "n"((i) * 1024) : "memory"); } while (0)
    const int grp = lane >> 4, q = (lane & 15) >> 2, p4 = lane & 3;
    const unsigned ta = (grp & 1) * G1_W3 + (8 * (grp >> 1) + q) * 288 + (((p4 & 1) << 4) | ((p4 >> 1) << 3));
    f32x16 acc[9];
#pragma unroll
    for (int t = 0; t < 9; ++t)
      for (int j = 0; j < 16; ++j) acc[t][j] = 0.f;
    s16x8 wf[4];
#define G1_RDKT(b, ks, t) cat8(tr_read((b) + (16 * (ks)) * 288 + (t) * 32), tr_read((b) + (16 * (ks) + 4) * 288 + (t) * 32))
#define G1_RD(b, i) G1_RDKT(b, (i) / 9, (i) % 9)
    const unsigned w2o = (ksl * 10368 + idx * 324 + (w * 7 + lane < 54 ? w * 7 + lane : 53)) * 128;
#define G1_STEP(xf, xn, st) do { LAS char* _b = lds + ((st) & 3) * G1_STAGE + ta; LAS char* _n = lds + (((st) + 1) & 3) * G1_STAGE + ta; \
    if ((st) >= 1) { if (lane < 7) touch((const char*)w2 + ((st) - 1) * (54 * 128), w2o, lbase + L_SCR); } \
    if ((st) == 0) { wf[0] = G1_RD(_b, 0); wf[1] = G1_RD(_b, 1); wf[2] = G1_RD(_b, 2); } \
    _Pragma("unroll") for (int i = 0; i < 36; ++i) { \
      if (i + 3 < 36) wf[(i + 3) & 3] = G1_RD(_b, i + 3); \
      else if ((st) < 6) wf[(i + 3) & 3] = G1_RD(_n, i + 3 - 36); \
      else if ((st) == 6) wf[(i + 3) & 3] = G1_RDKT(_n, i + 3 - 36, 0); \
      acc[i % 9] = __builtin_amdgcn_mfma_f32_32x32x16_bf16(wf[i & 3], __builtin_bit_cast(s16x8, xf[i / 9]), acc[i % 9], 0, 0, 0); \
      if ((st) == 0) { if ((i & 1) == 1 && (i >> 1) < 14) { const int v = i >> 1; \
        if (v < 4) G1_X(xn, 1, (v) & 3); else if (v < 9) G1_G(2, (v < 4) ? 0 : (v - 4) % 5); else G1_G(3, (v < 9) ? 0 : (v - 9) % 5); } } \
      else if ((i & 3) == 1) { const int v = i >> 2; \
        if (v < 4) G1_X(xn, (st) + 1, (v) & 3); \
        else if ((st) + 3 < 9) G1_G((st) + 3, (v - 4 < 0) ? 0 : v - 4); \
        else if ((st) == 6 && v < 8) G1_X(xc, 8, (v) & 3); } \
      SB0(); } } while (0)
    if (w == 7) wait_count((const GAS unsigned*)(ws + OFF_CNT) + (48 + mt) * 64, 32u);
    WAIT_V(0);
    BAR();
    STAMP();
#pragma unroll
    for (int i = 0; i < 4; ++i) G1_X(xa, 0, i);
    G1_WAITX(0, xa); G1_STEP(xa, xb, 0);
    G1_WAITX(5, xb); BAR(); G1_STEP(xb, xa, 1);
    G1_WAITX(5, xa); BAR(); G1_STEP(xa, xb, 2);
    G1_WAITX(5, xb); BAR(); G1_STEP(xb, xa, 3);
    G1_WAITX(5, xa); BAR(); G1_STEP(xa, xb, 4);
    G1_WAITX(5, xb); BAR(); G1_STEP(xb, xa, 5);
    G1_WAITX(5, xa); BAR(); G1_STEP(xa, xb, 6);
    G1_WAITX(0, xb); G1_WAITX(0, xc); BAR();
#pragma unroll
    for (int i = 0; i < 5; ++i) {
      const int ci = i * 64 + lane;
      const int pr = ci / 18, ch = ci - pr * 18;
      const int r = pr ^ (((pr >> 3) & 1) << 2);
      so[i] = r * (D * 2) + ch * 16;
    }
    LAS char* b7 = lds + 3 * G1_STAGE + ta; LAS char* b8 = lds + ta;
    const int m = lane & 31, h = lane >> 5;
    const int row = mt * 256 + w * 32 + m;
    const int cl = idx >> 2;
    const unsigned dbase = (((row >> 4) * 144 + ksl * 36) * 64 + h * 16 + (row & 15)) * 16;
    const unsigned d03 = dbase + cl * 2048, d8 = dbase + (32 + (cl >> 1)) * 1024 + (cl & 1) * 512;
    GAS unsigned* cntA = (GAS unsigned*)(ws + OFF_CNT) + (16 + mt * 4 + ksl) * 64;
#define G1_RT(i) (((i) & 7) < 4 ? G1_RDKT(b7, (i) & 3, (i) >> 3) : G1_RDKT(b8, (i) & 3, (i) >> 3))
    float e[8];
    static_for<0, 80>([&](auto I) {
      constexpr int i = decltype(I)::value, t = i >> 3, j = i & 7;
      if constexpr (i + 3 < 72) wf[(i + 3) & 3] = G1_RT(i + 3);
      if constexpr (i < 72) {
        const u32x4 xx = (j < 4) ? xb[j & 3] : xc[j & 3];
        acc[t] = __builtin_amdgcn_mfma_f32_32x32x16_bf16(wf[i & 3], __builtin_bit_cast(s16x8, xx), acc[t], 0, 0, 0);
      }
      if constexpr (t >= 1) {
        e[j] = fast_silu(acc[t - 1][j]) * acc[t - 1][8 + j];
        if constexpr (j == 7) {
          constexpr int tt = t - 1;
          const u32x4 hv = (u32x4){pk_bf16(e[0], e[1]), pk_bf16(e[2], e[3]), pk_bf16(e[4], e[5]), pk_bf16(e[6], e[7])};
          if constexpr (tt < 4) gstore16_sc1<(tt >> 1) * 1024 + (tt & 1) * 512>(hv, d03, hhb);
          else if constexpr (tt < 8) gstore16_sc1<((tt & 3) >> 1) * 1024 + (tt & 1) * 512>(hv, d03, hhb + 16 * 1024);
          else gstore16_sc1<0>(hv, d8, hhb);
        }
      }
      if constexpr (i >= 2 && i < 40 && (i & 3) == 2) { constexpr int v = (i - 2) >> 2; G2_G(v / 5, v % 5); }
      if constexpr (i == 54) {
        WAIT_V(1);
        arrive(lds + G1_ARR, cntA, lane);
      }
      if constexpr (i == 74) { if (w == 7) gload4_sc1(flagv, cntw); }
      SB0();
    });
    STAMP();
    cntB = cntA + 32;
  }
  if (w == 7) { wait_flag<1>(flagv); if (__builtin_amdgcn_readfirstlane(flagv) < 8u) wait_count(cntw, 8u); }
  const char* xs0 = hhb + ((size_t)(mt2 * 8 + mq * 2 + kg) * 144 + ksl * 36 + kg * 2) * 1024;
  const char* xs1 = hhb + ((size_t)(mt2 * 8 + mq * 2 + (kg ^ 1)) * 144 + ksl * 36 + kg * 2) * 1024;
  u32x4 xr[3][4];
#define G2_X(st, i) gload16_sc1<((i) >> 1) * 1024>(xr[(st) % 3][i], xvo, (((i) & 1) ? xs1 : xs0) + (st) * 4096)
  const int g = lane >> 4;
  unsigned tb[2][2];
  {
    const int q = (lane & 15) >> 2, p4 = lane & 3;
#pragma unroll
    for (int t = 0; t < 2; ++t) { tb[0][t] = lbase + G1_STAGE + (kg * 64 + 8 * g + ((4 * t) ^ (4 * (g & 1))) + q) * 288 + p4 * 8; tb[1][t] = tb[0][t] + G1_STAGE; }
    pinu(tb[0][0], tb[0][1]); pinu(tb[1][0], tb[1][1]);
  }
  f32x4 acc[2][9];
#pragma unroll
  for (int i = 0; i < 2; ++i)
#pragma unroll
    for (int t = 0; t < 9; ++t) { acc[i][t] = (f32x4){0.f, 0.f, 0.f, 0.f}; asm volatile("" : "+v"(acc[i][t])); }
  constexpr int PF2 = 5, RG2 = 6;
  s16x8 wf[RG2];
#define G2_RD(st, i) cat8(tr_read((LAS char*)(size_t)(tb[(st) & 1][0] + ((i) / 9) * (32 * 288) + ((i) % 9) * 32)), \
                          tr_read((LAS char*)(size_t)(tb[(st) & 1][1] + ((i) / 9) * (32 * 288) + ((i) % 9) * 32)))
  BAR();
  static_for<0, 8>([&](auto I) { constexpr int i = decltype(I)::value; G2_X(i / 4, i % 4); });
  static_for<0, PF2>([&](auto I) { constexpr int i = decltype(I)::value; wf[i % RG2] = G2_RD(0, i); });
  wait4<4>(xr[0]);
  arrive(lds + G1_ARR + 4, cntB, lane);
  const int fm = w & 1, fs = w >> 1;
  const int T = mt2 * 8 + 2 * ksl + fm;
  const int col = nq * 144 + 32 * fs + 4 * g;
  u32x2 x1v[3]; f32x4 gb[3];
  auto issue_p5 = [&]() {
    const char* xp = ws + OFF_X1 + (size_t)(T * 36 + nq * 9) * 512;
    const unsigned l8 = lane * 8;
    gload8_sc1(x1v[0], l8 + fs * 1024, xp); gload8_sc1(x1v[1], l8 + fs * 1024 + 512, xp); gload8_sc1(x1v[2], l8 + 4096, xp);
  };
  static_for<0, 162>([&](auto U) {
    constexpr int u = decltype(U)::value, st = u / 18, i = u % 18;
    if constexpr (i == 12 && st < 8) {
      wait4<g2_issued_before(u) - 9 * (st + 2)>(xr[(st + 1) % 3]);
      if constexpr (st == 1) {
        wait_flag<g2_issued_before(u) - 9 * (st + 2)>(flagv);
        if (w == 0) { if (__builtin_amdgcn_readfirstlane(flagv) < 8u) wait_count(cntw + 32, 8u); }
      }
      BAR();
      if constexpr (st >= 1) {
        constexpr unsigned d = (((st + 2) & 3) >= 2) ? 2 * G1_STAGE : (unsigned)(-2 * G1_STAGE);
        tb[(st + 1) & 1][0] += d; tb[(st + 1) & 1][1] += d;
        pinu(tb[(st + 1) & 1][0], tb[(st + 1) & 1][1]);
      }
    }
    constexpr int op = g2_op_at(u);
    if constexpr (u == 17) gload4_sc1(flagv, cntw + 32);
    if constexpr (op >= 0) { if constexpr (op % 9 < 5) G2_G(op / 9, op % 9); else G2_X(op / 9, op % 9 - 5); }
    if constexpr (u == 140) issue_p5();
    if constexpr (u + PF2 < 162) wf[(u + PF2) % RG2] = G2_RD((u + PF2) / 18, (u + PF2) % 18);
    MFMA16A(wf[u % RG2], xr[st % 3][2 * (i / 9)], acc[0][i % 9]);
    MFMA16A(wf[u % RG2], xr[st % 3][2 * (i / 9) + 1], acc[1][i % 9]);
    SB0();
  });
  asm volatile("s_nop 7" : "+v"(acc[0][0]), "+v"(acc[0][1]), "+v"(acc[0][2]), "+v"(acc[0][3]), "+v"(acc[0][4]), "+v"(acc[0][5]), "+v"(acc[0][6]), "+v"(acc[0][7]), "+v"(acc[0][8]),
               "+v"(acc[1][0]), "+v"(acc[1][1]), "+v"(acc[1][2]), "+v"(acc[1][3]), "+v"(acc[1][4]), "+v"(acc[1][5]), "+v"(acc[1][6]), "+v"(acc[1][7]), "+v"(acc[1][8]) :: "memory");
  BAR();
  STAMP();
  LAS f32x4* ex = (LAS f32x4*)lds;
#pragma unroll
  for (int t = 0; t < 9; ++t) ex[(w * 9 + t) * 64 + lane] = acc[1][t];
  WAIT_L(0);
  BAR();
  {
    f32x4 v[9];
#pragma unroll
    for (int t = 0; t < 9; ++t) v[t] = acc[0][t] + ex[((w ^ 4) * 9 + t) * 64 + lane];
    if (mq == ksl) {
      LAS f32x4* own = ex + (72 + kg * 9) * 64 + lane;
#pragma unroll
      for (int t = 0; t < 9; ++t) own[t * 64] = v[t];
    } else {
      const char* dst = ws + OFF_PART + ((size_t)(((ksl * 16 + mt2) * 4 + nq) * 8 + mq * 2 + kg) * 5) * 1024;
      static_for<0, 4>([&](auto I) { constexpr int i = decltype(I)::value;
        gstore16_sc1<i * 1024>((u32x4){pk_bf16(v[2 * i][0], v[2 * i][1]), pk_bf16(v[2 * i][2], v[2 * i][3]), pk_bf16(v[2 * i + 1][0], v[2 * i + 1][1]), pk_bf16(v[2 * i + 1][2], v[2 * i + 1][3])}, xvo, dst); });
      gstore8_sc1((u32x2){pk_bf16(v[8][0], v[8][1]), pk_bf16(v[8][2], v[8][3])}, xvo + 4096, dst);
    }
  }
  asm volatile("s_waitcnt vmcnt(0) lgkmcnt(0)" : "+v"(x1v[0]), "+v"(x1v[1]), "+v"(x1v[2]) :: "memory");
  BAR();
  GAS unsigned* cnt4 = (GAS unsigned*)(ws + OFF_CNT) + (64 + mt2 * 4 + nq) * 64;
  if (tid == 448) __hip_atomic_fetch_add(cnt4, 1u, __ATOMIC_RELAXED, __HIP_MEMORY_SCOPE_AGENT);
  f32x4 y[3];
  {
    const LAS float* gp = (const LAS float*)(lds + L_C2) + 432 + 4 * g;
    gb[0] = *(const LAS f32x4*)(gp + 32 * fs); gb[1] = *(const LAS f32x4*)(gp + 32 * fs + 16); gb[2] = *(const LAS f32x4*)(gp + 128);
  }
  y[0] = ex[(72 + fm * 9 + 2 * fs) * 64 + lane]; y[1] = ex[(72 + fm * 9 + 2 * fs + 1) * 64 + lane]; y[2] = ex[(72 + fm * 9 + 8) * 64 + lane];
  if (w == 7) wait_count(cnt4, 4u);
  BAR();
  {
    u32x4 pa[3], pb[3];
#pragma unroll
    for (int k = 0; k < 3; ++k) {
      const int src = (ksl + 1 + k) & 3;
      const char* p = ws + OFF_PART + ((size_t)(((src * 16 + mt2) * 4 + nq) * 8 + 2 * ksl + fm) * 5) * 1024;
      gload16_sc1<0>(pa[k], xvo + fs * 1024, p);
      if (fs == 0) gload16_sc1<0>(pb[k], xvo + 4096, p);
    }
    asm volatile("s_waitcnt vmcnt(0)" : "+v"(pa[0]), "+v"(pa[1]), "+v"(pa[2]), "+v"(pb[0]), "+v"(pb[1]), "+v"(pb[2]) :: "memory");
#pragma unroll
    for (int k = 0; k < 3; ++k) {
      y[0] += (f32x4){bf_lo(pa[k][0]), bf_hi(pa[k][0]), bf_lo(pa[k][1]), bf_hi(pa[k][1])};
      y[1] += (f32x4){bf_lo(pa[k][2]), bf_hi(pa[k][2]), bf_lo(pa[k][3]), bf_hi(pa[k][3])};
      y[2] += (f32x4){bf_lo(pb[k][0]), bf_hi(pb[k][0]), bf_lo(pb[k][1]), bf_hi(pb[k][1])};
    }
    const int row = 32 * (T >> 1) + 2 * (lane & 15) + (T & 1);
    u16* op = out + (size_t)row * D;
#pragma unroll
    for (int k = 0; k < 3; ++k) {
      float r0 = y[k][0], r1 = y[k][1], r2 = y[k][2], r3 = y[k][3];
      rb2(r0, r1); rb2(r2, r3);
      r0 *= gb[k][0]; r1 *= gb[k][1]; r2 *= gb[k][2]; r3 *= gb[k][3];
      rb2(r0, r1); rb2(r2, r3);
      const u32x2 o = (u32x2){pk_bf16(r0 + bf_lo(x1v[k][0]), r1 + bf_hi(x1v[k][0])), pk_bf16(r2 + bf_lo(x1v[k][1]), r3 + bf_hi(x1v[k][1]))};
      if (k < 2) *(u32x2*)(op + col + 16 * k) = o;
      else if (fs == 0) *(u32x2*)(op + nq * 144 + 128 + 4 * g) = o;
    }
  }
  STAMP();
  STAMP_OUT(3);
}

extern "C" __global__ void k_end_marker() {}

extern "C" void kernel_launch(void* const* d_in, const int* in_sizes, int n_in, void* d_out, int out_size,
                              void* d_ws, size_t ws_size, hipStream_t stream) {
  const u16* x = (const u16*)d_in[0];
  const u16* wqkv = (const u16*)d_in[1];
  const u16* wo = (const u16*)d_in[2];
  const u16* w1 = (const u16*)d_in[4];
  const u16* w3 = (const u16*)d_in[5];
  const u16* w2 = (const u16*)d_in[6];
  const u16* c = (const u16*)d_in[7];
  const u16* wmod = (const u16*)d_in[8];
  char* ws = (char*)d_ws;
  (void)hipFuncSetAttribute((const void*)km_main, hipFuncAttributeMaxDynamicSharedMemorySize, KM_LDS);
  k0_prep<<<K0_F, 384, 0, stream>>>(x, c, wmod, wqkv, wo, ws, w1, w3, w2, (const u16*)d_out);
  km_main<<<256, 512, KM_LDS, stream>>>(ws, x, w1, w3, w2, (u16*)d_out);
}
```

```cpp
#include <hip/hip_runtime.h>
#include <hip/hip_bf16.h>
#include <stdint.h>

typedef unsigned short u16;
typedef short s16x4 __attribute__((ext_vector_type(4)));
typedef short s16x8 __attribute__((ext_vector_type(8)));
typedef float f32x2 __attribute__((ext_vector_type(2)));
typedef float f32x4 __attribute__((ext_vector_type(4)));
typedef float f32x16 __attribute__((ext_vector_type(16)));
typedef unsigned u32x2 __attribute__((ext_vector_type(2)));
typedef unsigned u32x4 __attribute__((ext_vector_type(4)));
typedef __bf16 bf16x2 __attribute__((ext_vector_type(2)));
#define LAS __attribute__((address_space(3)))
#define GAS __attribute__((address_space(1)))

constexpr int S = 2048, D = 576, F = 4608;
constexpr size_t OFF_MOD = 0;
constexpr size_t OFF_XN = 49152;
constexpr size_t OFF_O = OFF_XN + (size_t)S * D * 2;
constexpr size_t OFF_X1 = OFF_O + (size_t)S * D * 2;
constexpr size_t OFF_H2 = OFF_X1 + (size_t)S * D * 4;
constexpr size_t OFF_HH = OFF_H2 + (size_t)S * D * 2;
constexpr size_t OFF_PART = OFF_HH + (size_t)S * F * 2;
constexpr size_t OFF_WQ = OFF_PART + (size_t)4 * 512 * 5 * 1024;
constexpr size_t OFF_WO = OFF_WQ + (size_t)16 * 126 * 1024;
constexpr size_t OFF_XCH = OFF_WO + (size_t)36 * 18 * 1024;
constexpr size_t OFF_CNT = OFF_XCH + (size_t)64 * 4 * 64 * 8;
constexpr unsigned XCH_TAG = 0x5a17c3e1u;

__device__ __forceinline__ float bf_lo(unsigned v) { return __uint_as_float(v << 16); }
__device__ __forceinline__ float bf_hi(unsigned v) { return __uint_as_float(v & 0xffff0000u); }
__device__ __forceinline__ unsigned pk_bf16(float lo, float hi) {
  bf16x2 r; r.x = (__bf16)lo; r.y = (__bf16)hi;
  return __builtin_bit_cast(unsigned, r);
}
__device__ __forceinline__ float rb(float f) { return bf_lo(pk_bf16(f, 0.f)); }
template <int CTRL> __device__ __forceinline__ float dppf(float x) { return __int_as_float(__builtin_amdgcn_update_dpp(0, __float_as_int(x), CTRL, 0xf, 0xf, true)); }
__device__ __forceinline__ float sum16(float x) { auto r = __builtin_amdgcn_permlane16_swap(__float_as_uint(x), __float_as_uint(x), false, false); return __uint_as_float(r[0]) + __uint_as_float(r[1]); }
__device__ __forceinline__ float sum32(float x) { auto r = __builtin_amdgcn_permlane32_swap(__float_as_uint(x), __float_as_uint(x), false, false); return __uint_as_float(r[0]) + __uint_as_float(r[1]); }
__device__ __forceinline__ float max16(float x) { auto r = __builtin_amdgcn_permlane16_swap(__float_as_uint(x), __float_as_uint(x), false, false); return fmaxf(__uint_as_float(r[0]), __uint_as_float(r[1])); }
__device__ __forceinline__ float max32(float x) { auto r = __builtin_amdgcn_permlane32_swap(__float_as_uint(x), __float_as_uint(x), false, false); return fmaxf(__uint_as_float(r[0]), __uint_as_float(r[1])); }
__device__ __forceinline__ void rb2(float& a, float& b) { const unsigned p = pk_bf16(a, b); a = bf_lo(p); b = bf_hi(p); }
__device__ __forceinline__ float fast_silu(float g) { return g * __builtin_amdgcn_rcpf(1.f + __builtin_amdgcn_exp2f(g * -1.44269504f)); }

#define WAIT_V(n) asm volatile("s_waitcnt vmcnt(%0)" ::"n"(n) : "memory")
#define WAIT_L(n) asm volatile("s_waitcnt lgkmcnt(%0)" ::"n"(n) : "memory")
#define BAR() do { asm volatile("" ::: "memory"); __builtin_amdgcn_s_barrier(); asm volatile("" ::: "memory"); } while (0)
template <int IMM, class T> __device__ __forceinline__ void gload16(T& dst, unsigned voff, const void* sbase) {
  asm volatile("global_load_dwordx4 %0, %1, %2 offset:%3" : "=v"(dst) : "v"(voff), "s"(sbase), "n"(IMM) : "memory");
}
template <int IMM, class T> __device__ __forceinline__ void gload8(T& dst, unsigned voff, const void* sbase) {
  asm volatile("global_load_dwordx2 %0, %1, %2 offset:%3" : "=v"(dst) : "v"(voff), "s"(sbase), "n"(IMM) : "memory");
}

__device__ __forceinline__ void glds16(const void* sbase, unsigned voff, unsigned ldsaddr) {
  asm volatile("s_mov_b32 m0, %2\n\ts_nop 0\n\tglobal_load_lds_dwordx4 %0, %1" :: "v"(voff), "s"(sbase), "s"(ldsaddr) : "memory");
}
__device__ __forceinline__ void glds16_sc1(const void* sbase, unsigned voff, unsigned ldsaddr) {
  asm volatile("s_mov_b32 m0, %2\n\ts_nop 0\n\tglobal_load_lds_dwordx4 %0, %1 sc1" :: "v"(voff), "s"(sbase), "s"(ldsaddr) : "memory");
}
#define SB0() __builtin_amdgcn_sched_barrier(0)
template <int I> struct IC { static constexpr int value = I; };
template <int B, int E, class Fn> __device__ __forceinline__ void static_for(Fn&& f) {
  if constexpr (B < E) { f(IC<B>{}); static_for<B + 1, E>(f); }
}
__device__ __forceinline__ void touch(const void* sbase, unsigned voff, unsigned ldsaddr) {
  asm volatile("s_mov_b32 m0, %2\n\ts_nop 0\n\tglobal_load_lds_dword %0, %1" :: "v"(voff), "s"(sbase), "s"(ldsaddr) : "memory");
}
__device__ __forceinline__ void pad2(u32x4& r) { asm volatile("s_nop 1" : "+v"(r)); }
template <class A, class B> __device__ __forceinline__ void MFMA16A(const A& a, const B& b, f32x4& c) {
  asm volatile("v_mfma_f32_16x16x32_bf16 %0, %1, %2, %0" : "+v"(c) : "v"(a), "v"(b));
}
__device__ __forceinline__ s16x4 tr_read(LAS char* p) { return __builtin_amdgcn_ds_read_tr16_b64_v4i16((LAS s16x4*)p); }
__device__ __forceinline__ s16x8 cat8(s16x4 a, s16x4 b) { return __builtin_shufflevector(a, b, 0, 1, 2, 3, 4, 5, 6, 7); }

__device__ __forceinline__ unsigned perm_lo(unsigned a, unsigned b) { return __builtin_amdgcn_perm(b, a, 0x05040100u); }
__device__ __forceinline__ unsigned perm_hi(unsigned a, unsigned b) { return __builtin_amdgcn_perm(b, a, 0x07060302u); }

constexpr int K0_A = 162, K0_B = K0_A + 128, K0_C = K0_B + 81, K0_D = K0_C + 27;
extern "C" __global__ __launch_bounds__(384) void k0_prep(const u16* __restrict__ x, const u16* __restrict__ c, const u16* __restrict__ wmod,
                                               const u16* __restrict__ wqkv, const u16* __restrict__ wo, char* __restrict__ ws) {
  __shared__ __attribute__((aligned(16))) float red[6 * 64];
  const int b = blockIdx.x, tid = threadIdx.x;
  const int w = tid >> 6, lane = tid & 63;
  if (b < K0_A) {
    const int kt = b / 54, cb = b - kt * 54;
    const int r0 = kt * 192 + (tid >> 3), col = cb * 64 + (tid & 7) * 8;
    u32x4 wv[4]; float sv[4];
#pragma unroll
    for (int i = 0; i < 4; ++i) wv[i] = *(const u32x4*)(wmod + (size_t)(r0 + 48 * i) * (6 * D) + col);
#pragma unroll
    for (int i = 0; i < 4; ++i) sv[i] = rb(fast_silu(bf_lo(c[r0 + 48 * i])));
    float a[8] = {0, 0, 0, 0, 0, 0, 0, 0};
#pragma unroll
    for (int i = 0; i < 4; ++i)
#pragma unroll
      for (int j = 0; j < 4; ++j) { a[2 * j] += sv[i] * bf_lo(wv[i][j]); a[2 * j + 1] += sv[i] * bf_hi(wv[i][j]); }
#pragma unroll
    for (int j = 0; j < 8; ++j) { a[j] += dppf<0x128>(a[j]); a[j] = sum32(sum16(a[j])); }
    if (lane < 8) {
      *(f32x4*)(red + w * 64 + lane * 8) = (f32x4){a[0], a[1], a[2], a[3]};
      *(f32x4*)(red + w * 64 + lane * 8 + 4) = (f32x4){a[4], a[5], a[6], a[7]};
    }
    __syncthreads();
    if (tid < 64) {
      float s = 0.f;
#pragma unroll
      for (int i = 0; i < 6; ++i) s += red[i * 64 + tid];
      ((float*)(ws + OFF_MOD))[kt * 3456 + cb * 64 + tid] = s;
    }
  } else if (b < K0_B) {
    const int i = lane & 15, g = lane >> 4;
    const int T = b - K0_A;
    const int row = 32 * (T >> 1) + 2 * i + (T & 1);
    float v[3][8];
#pragma unroll
    for (int s = 0; s < 3; ++s) {
      const u32x4 a = *(const u32x4*)(x + (size_t)row * D + ((w * 3 + s) * 4 + g) * 8);
#pragma unroll
      for (int j = 0; j < 4; ++j) { v[s][2 * j] = bf_lo(a[j]); v[s][2 * j + 1] = bf_hi(a[j]); }
    }
    float sm = 0.f;
#pragma unroll
    for (int s = 0; s < 3; ++s) sm += ((v[s][0] + v[s][1]) + (v[s][2] + v[s][3])) + ((v[s][4] + v[s][5]) + (v[s][6] + v[s][7]));
    const float mw = sum32(sum16(sm)) * (1.f / 96.f);
    float q0 = 0.f, q1 = 0.f;
#pragma unroll
    for (int s = 0; s < 3; ++s)
#pragma unroll
      for (int j = 0; j < 4; ++j) { const float d0 = v[s][2 * j] - mw, d1 = v[s][2 * j + 1] - mw; q0 += d0 * d0; q1 += d1 * d1; }
    const float q = sum32(sum16(q0 + q1));
    if (g == 0) *(f32x2*)(red + (w * 16 + i) * 2) = (f32x2){mw, q};
    __syncthreads();
    f32x2 p[6];
    float mean = 0.f;
#pragma unroll
    for (int k = 0; k < 6; ++k) { p[k] = *(const f32x2*)(red + (k * 16 + i) * 2); mean += p[k][0]; }
    mean *= (1.f / 6.f);
    float var = 0.f;
#pragma unroll
    for (int k = 0; k < 6; ++k) { const float dl = p[k][0] - mean; var += p[k][1] + 96.f * dl * dl; }
    var *= (1.f / D);
    const float mr = rb(mean);
#pragma unroll
    for (int s = 0; s < 3; ++s)
#pragma unroll
      for (int j = 0; j < 4; ++j) { v[s][2 * j] -= mr; v[s][2 * j + 1] -= mr; rb2(v[s][2 * j], v[s][2 * j + 1]); }
    const float rs = __builtin_amdgcn_rcpf(rb(__builtin_amdgcn_sqrtf(rb(rb(var) + 1e-5f))));
#pragma unroll
    for (int s = 0; s < 3; ++s) {
      u32x4 o;
#pragma unroll
      for (int j = 0; j < 4; ++j) o[j] = pk_bf16(v[s][2 * j] * rs, v[s][2 * j + 1] * rs);
      *(u32x4*)(ws + OFF_XN + ((size_t)(T * 18 + w * 3 + s) * 64 + lane) * 16) = o;
    }
  } else if (b < K0_C) {
    const int task = (b - K0_B) * 384 + tid;
    const int kq = task / 432, nq4 = task - kq * 432;
    u32x2 r[8];
#pragma unroll
    for (int i = 0; i < 8; ++i) r[i] = *(const u32x2*)(wqkv + (size_t)(kq * 8 + i) * (3 * D) + nq4 * 4);
    const int part = nq4 / 144, nh = nq4 - part * 144, ks = kq >> 2, ko = kq & 3;
#pragma unroll
    for (int e = 0; e < 4; ++e) {
      const int n = nh * 4 + e, head = n / 18, d = n - head * 18, hp = head >> 1, h = head & 1;
      int tile, idx;
      if (d < 16) { tile = (part == 0) ? 5 + h : (part == 1 ? h : 3 + h); idx = d; }
      else { tile = 2; idx = (part == 2) ? 8 + 2 * h + (d - 16) : 4 * h + 2 * part + (d - 16); }
      char* dst = ws + OFF_WQ + ((hp * 18 + ks) * 7 + tile) * 1024 + (ko * 16 + idx) * 16;
      u32x4 f;
#pragma unroll
      for (int i = 0; i < 4; ++i) f[i] = (e & 1) ? perm_hi(r[2 * i][e >> 1], r[2 * i + 1][e >> 1]) : perm_lo(r[2 * i][e >> 1], r[2 * i + 1][e >> 1]);
      *(u32x4*)dst = f;
      if (part == 2 && d >= 16) *(u32x4*)(dst + 64) = (u32x4){0u, 0u, 0u, 0u};
    }
  } else {
    const int task = (b - K0_C) * 384 + tid;
    const int nh = task % 144, ug = task / 144, u = ug >> 2, g = ug & 3;
    u32x2 r[8];
#pragma unroll
    for (int j = 0; j < 8; ++j) {
      const int krow = (u < 16) ? 36 * u + 18 * (j >> 2) + 4 * g + (j & 3) : 36 * (8 * (u - 16) + 2 * g + (j >> 2)) + 18 * ((j >> 1) & 1) + 16 + (j & 1);
      r[j] = *(const u32x2*)(wo + (size_t)krow * D + nh * 4);
    }
    char* dst = ws + OFF_WO + ((size_t)((nh >> 2) * 18 + u) * 64 + g * 16 + (nh & 3) * 4) * 16;
#pragma unroll
    for (int e = 0; e < 4; ++e) {
      u32x4 f;
#pragma unroll
      for (int i = 0; i < 4; ++i) f[i] = (e & 1) ? perm_hi(r[2 * i][e >> 1], r[2 * i + 1][e >> 1]) : perm_lo(r[2 * i][e >> 1], r[2 * i + 1][e >> 1]);
      *(u32x4*)(dst + e * 16) = f;
    }
    if (task < 64 * 4 * 64 / 2 + 2048) *(u32x4*)(ws + OFF_XCH + (size_t)task * 16) = (u32x4){0u, 0u, 0u, 0u};
  }
}

constexpr int G1_W3 = 64 * 288 + 128;
constexpr int G1_STAGE = 2 * G1_W3;
#define G1_WAITX(n, xf) asm volatile("s_waitcnt vmcnt(%4)" : "+v"(xf[0]), "+v"(xf[1]), "+v"(xf[2]), "+v"(xf[3]) : "n"(n) : "memory")
constexpr int g2_pos(int n) {
  const int s = n / 9, k = n % 9;
  if (s <= 1) return -1;
  return 18 * (s - 2) + 2 * k;
}
constexpr int g2_op_at(int u) { for (int n = 0; n < 81; ++n) if (g2_pos(n) == u) return n; return -1; }
constexpr int g2_issued_before(int u) { int c = 0; for (int n = 0; n < 81; ++n) if (g2_pos(n) < u) ++c; return c; }
template <int IMM, class T> __device__ __forceinline__ void gload16_sc1(T& dst, unsigned voff, const void* sbase) {
  asm volatile("global_load_dwordx4 %0, %1, %2 offset:%3 sc1" : "=v"(dst) : "v"(voff), "s"(sbase), "n"(IMM) : "memory");
}
template <int IMM> __device__ __forceinline__ void gstore16_sc1(const u32x4& v, unsigned voff, const void* sbase) {
  asm volatile("global_store_dwordx4 %0, %1, %2 offset:%3 sc1\n\ts_nop 1" :: "v"(voff), "v"(v), "s"(sbase), "n"(IMM) : "memory");
}
__device__ __forceinline__ void gload8_sc1(u32x2& dst, unsigned voff, const void* sbase) {
  asm volatile("global_load_dwordx2 %0, %1, %2 sc1" : "=v"(dst) : "v"(voff), "s"(sbase) : "memory");
}
__device__ __forceinline__ void gstore8_sc1(const u32x2& v, unsigned voff, const void* sbase) {
  asm volatile("global_store_dwordx2 %0, %1, %2 sc1\n\ts_nop 1" :: "v"(voff), "v"(v), "s"(sbase) : "memory");
}
template <int N> __device__ __forceinline__ void wait4(u32x4 (&x)[4]) {
  asm volatile("s_waitcnt vmcnt(%4)" : "+v"(x[0]), "+v"(x[1]), "+v"(x[2]), "+v"(x[3]) : "n"(N) : "memory");
}
template <int N> __device__ __forceinline__ void wait_flag(unsigned& f) { asm volatile("s_waitcnt vmcnt(%1)" : "+v"(f) : "n"(N) : "memory"); }
__device__ __forceinline__ void pinu(unsigned& a, unsigned& b) { asm volatile("" : "+v"(a), "+v"(b)); }
__device__ __forceinline__ void atomic_inc(GAS unsigned* p) {
  const unsigned one = 1u, zero = 0u;
  asm volatile("global_atomic_add %0, %1, %2" :: "v"(zero), "v"(one), "s"(p) : "memory");
}
__device__ __forceinline__ void arrive(LAS char* ctr, GAS unsigned* cnt, int lane) {
  if (lane == 0) { if (__hip_atomic_fetch_add((LAS unsigned*)ctr, 1u, __ATOMIC_RELAXED, __HIP_MEMORY_SCOPE_WORKGROUP) == 7u) atomic_inc(cnt); }
}
__device__ __forceinline__ void gload4_sc1(unsigned& dst, const GAS unsigned* p) {
  const unsigned zero = 0u;
  asm volatile("global_load_dword %0, %1, %2 sc1" : "=v"(dst) : "v"(zero), "s"(p) : "memory");
}
__device__ __forceinline__ void wait_count(const GAS unsigned* cnt, unsigned want) {
  for (unsigned spin = 0; spin < (1u << 16); ++spin) {
    if (__builtin_amdgcn_readfirstlane(__hip_atomic_load(cnt, __ATOMIC_RELAXED, __HIP_MEMORY_SCOPE_AGENT)) >= want) break;
    __builtin_amdgcn_s_sleep(1);
  }
}


constexpr int L_CONST = 4 * G1_STAGE;
constexpr int L_C2 = L_CONST + 8192;
constexpr int L_ST = L_C2 + 2304;
constexpr int L_FIN = L_ST + 2304;
constexpr int L_SCR = L_FIN + 256;
constexpr int G1_ARR = L_SCR + 256;
constexpr int KM_LDS = G1_ARR + 32;
constexpr int K1_EK = 0, K1_EV = 9 * 2 * 1024, K1_EL = K1_EV + 9 * 2 * 512;
constexpr int K1_O = 3 * G1_STAGE;
constexpr int k1_r(int p) { return p < 0 ? 0 : (21 * (p + 1) + 7) / 8; }
constexpr int k1_cum(int p) { return 9 + 3 * (p + 1 < 5 ? p + 1 : 5) + k1_r(p); }
constexpr int K1_NOPS = 41 + 29;
constexpr int k1_op(int n) {
  if (n < 3) return 96;
  if (n < 6) return 97 + n - 3;
  if (n < 9) return n - 6;
  for (int p = 0; p < 6; ++p) {
    const int lo = p ? k1_cum(p - 1) : 9;
    if (n < k1_cum(p)) { const int k = n - lo; return (p < 5 && k < 3) ? 3 * p + 3 + k : 32 + k1_r(p - 1) + k - (p < 5 ? 3 : 0); }
  }
  return n == 40 ? 100 : 64 + n - 41;
}
constexpr int k1_pos(int n) {
  if (n < k1_cum(0)) return -1;
  if (n < k1_cum(1)) return 0;
  if (n < k1_cum(2)) return 1 + (n - k1_cum(1)) / 3;
  for (int p = 3; p < 6; ++p) if (n < k1_cum(p)) return 3 * (p - 2) + (n - k1_cum(p - 1)) / 2;
  if (n == 40) return 10;
  const int k = n - 41;
  constexpr int NIN = 6;
  if (k < NIN) return 18 - NIN / 2 + k / 2;
  if (k < NIN + 3) return 18;
  if (k < NIN + 9) return 19 + (k - NIN - 3) / 3;
  return 21;
}
constexpr int k1_before(int s) { int c = 0; for (int n = 0; n < K1_NOPS; ++n) if (k1_pos(n) < s) ++c; return c; }
static_assert(k1_cum(5) == 40 && k1_before(22) == K1_NOPS, "");
struct Cst { f32x4 s0, s1, g0, g1; };
__device__ __forceinline__ void rdc(Cst& c, const LAS float* cs) {
  c.s0 = *(const LAS f32x4*)cs; c.s1 = *(const LAS f32x4*)(cs + 4); c.g0 = *(const LAS f32x4*)(cs + 576); c.g1 = *(const LAS f32x4*)(cs + 580);
}
__device__ __forceinline__ float dot2(unsigned x, float gw, float s) {
  float r; asm("v_dot2_f32_bf16 %0, %1, %2, %3" : "=v"(r) : "v"(x), "v"(gw), "v"(s)); return r;
}
__device__ __forceinline__ void modc(u32x4& xv, const Cst& c) {
  asm volatile("" : "+v"(xv));
  const u32x4 v = xv;
  float h[8];
  h[0] = dot2(v[0], c.g0[0], c.s0[0]); h[1] = dot2(v[0], c.g0[1], c.s0[1]); h[2] = dot2(v[1], c.g0[2], c.s0[2]); h[3] = dot2(v[1], c.g0[3], c.s0[3]);
  h[4] = dot2(v[2], c.g1[0], c.s1[0]); h[5] = dot2(v[2], c.g1[1], c.s1[1]); h[6] = dot2(v[3], c.g1[2], c.s1[2]); h[7] = dot2(v[3], c.g1[3], c.s1[3]);
  asm volatile("s_nop 2" : "+v"(h[0]), "+v"(h[1]), "+v"(h[2]), "+v"(h[3]), "+v"(h[4]), "+v"(h[5]), "+v"(h[6]), "+v"(h[7]));
  u32x4 r;
  r[0] = pk_bf16(h[0], h[1]); r[1] = pk_bf16(h[2], h[3]); r[2] = pk_bf16(h[4], h[5]); r[3] = pk_bf16(h[6], h[7]);
  pad2(r);
  xv = r;
}
template <int N> __device__ __forceinline__ void wait3(u32x4& a, u32x4& b, u32x4& c) {
  asm volatile("s_waitcnt vmcnt(%3)" : "+v"(a), "+v"(b), "+v"(c) : "n"(N) : "memory");
}
__device__ __forceinline__ void pin(u32x4& a) { asm volatile("" : "+v"(a)); }


__device__ __forceinline__ void resid_stats(const f32x4 a, const u32x2 xin, const f32x4 ga, float (&v)[4], u32x2& xo, f32x2& sq) {
  const unsigned p0 = pk_bf16(a[0] * ga[0] + bf_lo(xin[0]), a[1] * ga[1] + bf_hi(xin[0])), p1 = pk_bf16(a[2] * ga[2] + bf_lo(xin[1]), a[3] * ga[3] + bf_hi(xin[1]));
  v[0] = bf_lo(p0); v[1] = bf_hi(p0); v[2] = bf_lo(p1); v[3] = bf_hi(p1);
  const float mw = sum32(sum16((v[0] + v[1]) + (v[2] + v[3]))) * (1.f / 16.f);
  const float d0 = v[0] - mw, d1 = v[1] - mw, d2 = v[2] - mw, d3 = v[3] - mw;
  sq = (f32x2){mw, sum32(sum16((d0 * d0 + d1 * d1) + (d2 * d2 + d3 * d3)))};
  xo = (u32x2){p0, p1};
}
__device__ __forceinline__ u32x2 norm_mod(const float (&v)[4], const f32x2 f, const f32x4 g2, const f32x4 sb) {
  float h[4];
#pragma unroll
  for (int j = 0; j < 4; ++j) h[j] = (v[j] - f[0]) * f[1] * g2[j];
  return (u32x2){pk_bf16(h[0] + sb[0], h[1] + sb[1]), pk_bf16(h[2] + sb[2], h[3] + sb[3])};
}

extern "C" __global__ __launch_bounds__(512, 2) void km_main(char* __restrict__ ws, const u16* __restrict__ x, const u16* __restrict__ w1,
                                                  const u16* __restrict__ w3, const u16* __restrict__ w2, u16* __restrict__ out) {
  extern __shared__ __attribute__((aligned(16))) char smem[];
  LAS char* lds = (LAS char*)smem;
  const unsigned lbase = (unsigned)(size_t)lds;
  const int b = blockIdx.x, xcd = b & 7, idx = b >> 3;
  const int tid = threadIdx.x, lane = tid & 63;
  const int w = __builtin_amdgcn_readfirstlane(tid >> 6);
  const unsigned xvo = lane * 16;
  const int ksl = xcd >> 1;
  const int mt = (xcd & 1) * 4 + (idx & 3), ct = ksl * 8 + (idx >> 2);
  const int mt2 = (xcd & 1) * 8 + (idx & 7), nq = idx >> 3;
  const int mq = w & 3, kg = w >> 2;
  const char* hhb = ws + OFF_HH;
  if (tid == 0) *(LAS u32x4*)(lds + G1_ARR) = (u32x4){0u, 0u, 0u, 0u};
  unsigned so[5];
  const char* wsrc = (const char*)((w < 4) ? w1 : w3) + (size_t)((w & 3) * 16) * (F * 2) + ct * 288;
  const unsigned wdst = lbase + (w >> 2) * G1_W3 + (w & 3) * 4608;
#define G1_G(st, i) do { const char* _s = wsrc + (size_t)(st) * (64 * F * 2); const unsigned _d = wdst + ((st) & 3) * G1_STAGE; \
    if ((i) < 4) glds16(_s, so[i], _d + (i) * 1024); else if (lane < 32) glds16(_s, so[4], _d + 4096); } while (0)
  {
  const int rt = (xcd & 3) * 4 + (idx & 3), hp = (xcd >> 2) * 8 + (idx >> 2);
  const int li = lane & 15, g = lane >> 4;
  const unsigned l16 = xvo;
  const char* xsrc = ws + OFF_XN + (size_t)(8 * rt + w) * (18 * 1024);
  const char* hsrc = ws + OFF_XN + (size_t)(rt > 0 ? 8 * rt - 2 : 0) * (18 * 1024);
  const unsigned hvo = (li >> 3) * (18 * 1024) + (g * 16 + 8 + (li & 7)) * 16;
  const char* wq = ws + OFF_WQ + (size_t)hp * (126 * 1024);
  const int rt2 = 4 * rt + (hp >> 2), nq2 = hp & 3;
  const int nt = nq2 * 9 + w;
  const char* wsrc2 = ws + OFF_WO + (size_t)nt * (18 * 1024);
  const char* wsrc8 = ws + OFF_WO + (size_t)((nq2 * 9 + 8) * 18 + (w >> 1)) * 1024;
  u32x4 xf[18], wo[18], wo8[5], hx[3];
  u32x2 xr[3]; f32x4 mq3[3];
  auto issue = [&](auto N) {
    constexpr int op = k1_op(decltype(N)::value);
    if constexpr (op < 32) { const char* q = xsrc + (op & ~3) * 1024; gload16<(op & 3) * 1024>(xf[op], l16, q); }
    else if constexpr (op < 64) {
      int p = w + 8 * (op - 32); p = p < 126 ? p : 125;
      const int sk = (p * 9363) >> 16;
      glds16(wq + p * 1024, l16, lbase + (p + sk) * 1024);
    }
    else if constexpr (op < 82) { constexpr int i = op - 64; const char* q = wsrc2 + (i & ~3) * 1024; gload16<(i & 3) * 1024>(wo[i], l16, q); }
    else if constexpr (op < 87) { constexpr int i = op - 82; if (i < 4 || w < 4) gload16<0>(wo8[i], l16, wsrc8 + i * 4096); }
    else if constexpr (op < 89) gload8<(op - 87) * 1152>(xr[op - 87], (unsigned)(((rt2 * 32 + 2 * li) * D + nt * 16 + 4 * g) * 2), x);
    else if constexpr (op == 89) { if (w < 2) gload8<0>(xr[2], (unsigned)(((rt2 * 32 + 2 * li + (w & 1)) * D + nq2 * 144 + 128 + 4 * g) * 2), x); }
    else if constexpr (op < 93) {
      const int t = tid < 144 ? tid : 143, vec = t / 36, c4 = t - vec * 36;
      if (w < 3) gload16<0>(mq3[op - 90], (unsigned)(((2 + vec) * D + (vec == 3 ? nq : nq2) * 144 + c4 * 4) * 4), ws + OFF_MOD + (op - 90) * 3456 * 4);
    }
    else if constexpr (op < 100) { const int sk = w + 8 * (op - 97); gload16<0>(hx[op - 97], hvo, hsrc + (sk < 18 ? sk : 17) * 1024); }
    else {
      const int ln = idx * 108 + (w & 3) * 27 + (lane < 27 ? lane : 26);
      touch((w & 4) ? w3 : w1, (ln / 18) * (F * 2) + ksl * 2304 + (ln % 18) * 128, lbase + L_SCR);
    }
  };
  f32x4 mp[3];
  {
    const unsigned mo = (tid < 288 ? tid : 287) * 16;
    gload16<0>(mp[0], mo, ws + OFF_MOD); gload16<0>(mp[1], mo, ws + OFF_MOD + 3456 * 4); gload16<0>(mp[2], mo, ws + OFF_MOD + 6912 * 4);
  }
  static_for<3, k1_cum(0)>(issue);
  asm volatile("s_waitcnt vmcnt(%3)" : "+v"(mp[0]), "+v"(mp[1]), "+v"(mp[2]) : "n"(k1_cum(0) - 3) : "memory");
  if (tid < 288) {
    float m[4];
#pragma unroll
    for (int j = 0; j < 4; ++j) m[j] = (mp[0][j] + mp[1][j]) + mp[2][j];
    rb2(m[0], m[1]); rb2(m[2], m[3]);
    if (tid >= 144) {
#pragma unroll
      for (int j = 0; j < 4; ++j) m[j] += 1.f;
      rb2(m[0], m[1]); rb2(m[2], m[3]);
      m[0] = __uint_as_float(__float_as_uint(m[0]) >> 16); m[2] = __uint_as_float(__float_as_uint(m[2]) >> 16);
    }
    *(LAS f32x4*)(lds + L_CONST + tid * 16) = (f32x4){m[0], m[1], m[2], m[3]};
  }
  WAIT_L(0);
  BAR();
  const LAS float* cs = (const LAS float*)(lds + L_CONST) + g * 8;
  Cst cst;
  {
    Cst c1, c2;
    const int s2 = w + 16 < 18 ? w + 16 : 17;
    rdc(cst, cs + w * 32); rdc(c1, cs + (w + 8) * 32); rdc(c2, cs + s2 * 32);
    wait3<k1_cum(0) - 6>(hx[0], hx[1], hx[2]);
    modc(hx[0], cst); modc(hx[1], c1); modc(hx[2], c2);
    *(LAS u32x4*)(lds + w * 8192 + 7168 + l16) = hx[0];
    *(LAS u32x4*)(lds + (w + 8) * 8192 + 7168 + l16) = hx[1];
    *(LAS u32x4*)(lds + s2 * 8192 + 7168 + l16) = hx[2];
    rdc(cst, cs); rdc(c1, cs + 32); rdc(c2, cs + 64);
    wait3<k1_cum(0) - 9>(xf[0], xf[1], xf[2]);
    modc(xf[0], cst); modc(xf[1], c1); modc(xf[2], c2);
  }
  rdc(cst, cs + 96);
  f32x4 acc[8];
#pragma unroll
  for (int t = 0; t < 8; ++t) { acc[t] = (f32x4){0.f, 0.f, 0.f, 0.f}; asm volatile("" : "+v"(acc[t])); }
  const bool hw = w < 5;
  unsigned wa = lbase + l16, ha = wa + (w < 3 ? w * 1024 : 7168), hb = wa + (w < 3 ? 7168 : w * 1024);
  s16x8 wf[7], hf[2];
  auto rdw = [&](auto Sx, auto T0, auto T1) {
    constexpr int s = decltype(Sx)::value, off = (s % 6) * 8192;
#pragma unroll
    for (int t = decltype(T0)::value; t < decltype(T1)::value; ++t) wf[t] = *(const LAS s16x8*)(size_t)(wa + off + t * 1024);
  };
  auto rdh = [&](auto Sx) {
    constexpr int off = (decltype(Sx)::value % 6) * 8192;
    hf[0] = *(const LAS s16x8*)(size_t)(ha + off); hf[1] = *(const LAS s16x8*)(size_t)(hb + off);
  };
  asm volatile("s_waitcnt vmcnt(0) lgkmcnt(0)" : "+v"(xf[3]), "+v"(xf[4]), "+v"(xf[5]) :: "memory");
  BAR();
  rdw(IC<0>{}, IC<0>{}, IC<7>{});
  rdh(IC<0>{});
  static_for<0, 18>([&](auto Sx) {
    constexpr int s = decltype(Sx)::value, p = s / 3;
    if constexpr (s % 3 == 2 && p < 5) {
      if constexpr (p < 4) wait3<k1_before(s) - k1_cum(p + 1)>(xf[3 * p + 6], xf[3 * p + 7], xf[3 * p + 8]);
      else WAIT_V(k1_before(s) - k1_cum(5));
      BAR();
    }
    if constexpr (s == 5 || s == 11) { wa += 6 * 8192; ha += 6 * 8192; hb += 6 * 8192; asm volatile("" : "+v"(wa)); pinu(ha, hb); }
    if constexpr (s == 0) static_for<k1_before(0), k1_before(1)>(issue);
    MFMA16A(wf[0], xf[s], acc[0]); MFMA16A(wf[1], xf[s], acc[1]); MFMA16A(wf[2], xf[s], acc[2]); MFMA16A(xf[s], wf[3], acc[3]);
    if constexpr (s < 17) rdw(IC<s + 1>{}, IC<0>{}, IC<4>{});
    if constexpr (s + 3 < 18) modc(xf[s + 3], cst);
    if constexpr (s + 4 < 18) rdc(cst, cs + (s + 4) * 32);
    MFMA16A(xf[s], wf[4], acc[4]); MFMA16A(wf[5], xf[s], acc[5]); MFMA16A(wf[6], xf[s], acc[6]);
    if constexpr (s < 17) rdw(IC<s + 1>{}, IC<4>{}, IC<7>{});
    if (hw) { MFMA16A(hf[0], hf[1], acc[7]); if constexpr (s < 17) rdh(IC<s + 1>{}); }
    if constexpr (s > 0) static_for<k1_before(s), k1_before(s + 1)>(issue);
  });
  asm volatile("s_nop 7" : "+v"(acc[0]), "+v"(acc[1]), "+v"(acc[2]), "+v"(acc[3]), "+v"(acc[4]), "+v"(acc[5]), "+v"(acc[6]), "+v"(acc[7]) :: "memory");
  unsigned kf[2][2], qf[2][2], vf[2][2], lf[2];
#pragma unroll
  for (int h = 0; h < 2; ++h) {
    kf[h][0] = pk_bf16(acc[h][0], acc[h][1]); kf[h][1] = pk_bf16(acc[h][2], acc[h][3]);
    qf[h][0] = pk_bf16(acc[5 + h][0], acc[5 + h][1]); qf[h][1] = pk_bf16(acc[5 + h][2], acc[5 + h][3]);
    vf[h][0] = pk_bf16(acc[3 + h][0], acc[3 + h][1]); vf[h][1] = pk_bf16(acc[3 + h][2], acc[3 + h][3]);
  }
  lf[0] = pk_bf16(acc[2][0], acc[2][1]); lf[1] = pk_bf16(acc[2][2], acc[2][3]);
#pragma unroll
  for (int h = 0; h < 2; ++h) {
    *(LAS u32x4*)(lds + K1_EK + (w * 2 + h) * 1024 + l16) = (u32x4){kf[h][0], kf[h][1], lf[1], 0u};
    *(LAS u32x2*)(lds + K1_EV + (w * 2 + h) * 512 + lane * 8) = (u32x2){vf[h][0], vf[h][1]};
  }
  if (g == 2) *(LAS u32x2*)(lds + K1_EL + (w * 16 + li) * 8) = (u32x2){lf[0], lf[1]};
  {
    const u32x2 hv = (u32x2){pk_bf16(acc[7][0], acc[7][1]), pk_bf16(acc[7][2], acc[7][3])};
    if (w < 2) *(LAS u32x2*)(lds + K1_EK + (16 + w) * 1024 + l16) = hv;
    else if (w == 2) {
      *(LAS u32x2*)(lds + K1_EK + 16 * 1024 + l16 + 8) = (u32x2){hv[1], 0u};
      *(LAS u32x2*)(lds + K1_EK + 17 * 1024 + l16 + 8) = (u32x2){hv[1], 0u};
      if (g == 2) *(LAS u32x2*)(lds + K1_EL + (128 + li) * 8) = hv;
    } else if (w < 5) *(LAS u32x2*)(lds + K1_EV + (16 + w - 3) * 512 + lane * 8) = hv;
  }
  static_for<k1_before(18), k1_before(19)>(issue);
  WAIT_L(0);
  BAR();
  {
    const int par = w & 1;
    const bool first = (w < 2);
    const int pw = first ? 8 : w - 2;
    const int pki = first ? 8 * par + (li & 7) : li;
    const int pvg = first ? 2 * par + (g & 1) : g;
    const int q4 = li >> 2;
    const int plk = first ? 8 * par + ((4 * g + q4) & 7) : 4 * g + q4;
    const s16x8 vl = cat8(tr_read(lds + K1_EL + (pw * 16 + plk) * 8), tr_read(lds + K1_EL + (w * 16 + 4 * g + q4) * 8));
    const bool prev_ok = !(first && rt == 0);
    unsigned op[2][2], ol[2];
    bool usep[4], useo[4];
#pragma unroll
    for (int j = 0; j < 4; ++j) { const int kk = 4 * g + j; usep[j] = prev_ok && (kk >= li + 9); useo[j] = (kk <= li) && (li - kk <= 7); }
    const unsigned mp01 = (usep[0] ? 0xffffu : 0u) | (usep[1] ? 0xffff0000u : 0u), mp23 = (usep[2] ? 0xffffu : 0u) | (usep[3] ? 0xffff0000u : 0u);
    static_for<k1_before(19), k1_before(20)>(issue);
#pragma unroll
    for (int h = 0; h < 2; ++h) {
      const u32x4 kp = *(const LAS u32x4*)(lds + K1_EK + (pw * 2 + h) * 1024 + (g * 16 + pki) * 16);
      const u32x2 vp = *(const LAS u32x2*)(lds + K1_EV + (pw * 2 + h) * 512 + (pvg * 16 + li) * 8);
      const u32x4 ko = (u32x4){kf[h][0], kf[h][1], lf[1], 0u};
      const u32x4 qq = (u32x4){qf[h][0], qf[h][1], (g == h) ? lf[0] : 0u, 0u};
      const f32x4 z = (f32x4){0.f, 0.f, 0.f, 0.f};
      const f32x4 so = __builtin_amdgcn_mfma_f32_16x16x32_bf16(__builtin_bit_cast(s16x8, ko), __builtin_bit_cast(s16x8, qq), z, 0, 0, 0);
      const f32x4 sp = __builtin_amdgcn_mfma_f32_16x16x32_bf16(__builtin_bit_cast(s16x8, kp), __builtin_bit_cast(s16x8, qq), z, 0, 0, 0);
      float lg[4];
#pragma unroll
      for (int j = 0; j < 4; ++j) lg[j] = (usep[j] || useo[j]) ? (usep[j] ? sp[j] : so[j]) * 0.34004648f : -3.0e38f;
      const float mx = max32(max16(fmaxf(fmaxf(lg[0], lg[1]), fmaxf(lg[2], lg[3]))));
#pragma unroll
      for (int j = 0; j < 4; ++j) lg[j] = __builtin_amdgcn_exp2f(lg[j] - mx);
      const float inv = __builtin_amdgcn_rcpf(sum32(sum16((lg[0] + lg[1]) + (lg[2] + lg[3]))));
      const unsigned p01 = pk_bf16(lg[0] * inv, lg[1] * inv), p23 = pk_bf16(lg[2] * inv, lg[3] * inv);
      const u32x4 pf = (u32x4){p01 & mp01, p23 & mp23, p01 & ~mp01, p23 & ~mp23};
      const u32x4 vv = (u32x4){vp[0], vp[1], vf[h][0], vf[h][1]};
      const f32x4 oo = __builtin_amdgcn_mfma_f32_16x16x32_bf16(__builtin_bit_cast(s16x8, vv), __builtin_bit_cast(s16x8, pf), z, 0, 0, 0);
      const f32x4 lo = __builtin_amdgcn_mfma_f32_16x16x32_bf16(vl, __builtin_bit_cast(s16x8, pf), z, 0, 0, 0);
      op[h][0] = pk_bf16(oo[0], oo[1]); op[h][1] = pk_bf16(oo[2], oo[3]);
      ol[h] = pk_bf16(lo[2 * h], lo[2 * h + 1]);
    }
    static_for<k1_before(20), k1_before(21)>(issue);
    const char* ob = ws + OFF_O + (size_t)(8 * rt + w) * (18 * 1024);
    const u32x4 o4 = (u32x4){op[0][0], op[0][1], op[1][0], op[1][1]};
    const unsigned oo0 = hp * 1024 + l16;
    asm volatile("global_store_dwordx4 %0, %1, %2 sc1\n\ts_nop 1" :: "v"(oo0), "v"(o4), "s"(ob) : "memory");
    if (g == 0) {
      const u32x2 o2 = (u32x2){ol[0], ol[1]};
      const unsigned oo1 = (16 + (hp >> 3)) * 1024 + (((hp & 7) >> 1) * 16 + li) * 16 + 8 * (hp & 1);
      asm volatile("global_store_dwordx2 %0, %1, %2 sc1\n\ts_nop 1" :: "v"(oo1), "v"(o2), "s"(ob) : "memory");
    }
  }
  WAIT_V(0);
  GAS unsigned* cnt1 = (GAS unsigned*)(ws + OFF_CNT) + rt * 64;
  arrive(lds + G1_ARR + 8, cnt1, lane);
  static_for<k1_before(21), K1_NOPS>(issue);
#pragma unroll
  for (int i = 0; i < 5; ++i) {
    const int ci = i * 64 + lane;
    const int r = ci / 18, ch = ci - r * 18;
    so[i] = r * (F * 2) + ch * 16;
  }
  if (w == 7) wait_count(cnt1, 16u);
  BAR();
  {
    const char* osrc = ws + OFF_O + (size_t)rt2 * (36 * 1024) + w * 4608;
#pragma unroll
    for (int i = 0; i < 4; ++i) glds16_sc1(osrc + i * 1024, l16, lbase + K1_O + w * 4608 + i * 1024);
    if (lane < 32) glds16_sc1(osrc + 4096, l16, lbase + K1_O + w * 4608 + 4096);
  }
  asm volatile("s_waitcnt vmcnt(5)" : "+v"(xr[0]), "+v"(xr[1]), "+v"(xr[2]), "+v"(mq3[0]), "+v"(mq3[1]), "+v"(mq3[2]) :: "memory");
  static_for<0, 18>([&](auto I) { pin(wo[decltype(I)::value]); });
  static_for<0, 5>([&](auto I) { pin(wo8[decltype(I)::value]); });
  if (tid < 144) {
    float t[4];
#pragma unroll
    for (int j = 0; j < 4; ++j) t[j] = (mq3[0][j] + mq3[1][j]) + mq3[2][j];
    rb2(t[0], t[1]); rb2(t[2], t[3]);
    if (tid >= 72 && tid < 108) {
#pragma unroll
      for (int j = 0; j < 4; ++j) t[j] += 1.f;
      rb2(t[0], t[1]); rb2(t[2], t[3]);
    }
    *(LAS f32x4*)(lds + L_C2 + tid * 16) = (f32x4){t[0], t[1], t[2], t[3]};
  }
  asm volatile("s_waitcnt vmcnt(0) lgkmcnt(0)" ::: "memory");
  BAR();
  f32x4 ac2[3];
  ac2[0] = (f32x4){0.f, 0.f, 0.f, 0.f}; ac2[1] = ac2[0]; ac2[2] = ac2[0];
  {
    unsigned oa = lbase + K1_O + l16, o8 = oa + ((w & 1) * 18 + (w >> 1)) * 1024;
    pinu(oa, o8);
    s16x8 of[2][6];
    auto rdo = [&](auto Cx) {
      constexpr int c = decltype(Cx)::value;
#pragma unroll
      for (int i = 0; i < 3; ++i) { of[c & 1][2 * i] = *(const LAS s16x8*)(size_t)(oa + (3 * c + i) * 1024); of[c & 1][2 * i + 1] = *(const LAS s16x8*)(size_t)(oa + (18 + 3 * c + i) * 1024); }
    };
    s16x8 o8f[5];
#pragma unroll
    for (int i = 0; i < 5; ++i) o8f[i] = *(const LAS s16x8*)(size_t)(o8 + ((i < 4 || w < 4) ? i : 0) * 4096);
    rdo(IC<0>{});
#pragma unroll
    for (int i = 0; i < 4; ++i) ac2[2] = __builtin_amdgcn_mfma_f32_16x16x32_bf16(__builtin_bit_cast(s16x8, wo8[i]), o8f[i], ac2[2], 0, 0, 0);
    if (w < 4) ac2[2] = __builtin_amdgcn_mfma_f32_16x16x32_bf16(__builtin_bit_cast(s16x8, wo8[4]), o8f[4], ac2[2], 0, 0, 0);
    static_for<0, 6>([&](auto Cx) {
      constexpr int c = decltype(Cx)::value;
      if constexpr (c < 5) rdo(IC<c + 1>{});
      if constexpr (c == 1) *(LAS f32x4*)(lds + L_CONST + w * 1024 + l16) = ac2[2];
#pragma unroll
      for (int i = 0; i < 3; ++i) {
        ac2[0] = __builtin_amdgcn_mfma_f32_16x16x32_bf16(__builtin_bit_cast(s16x8, wo[3 * c + i]), of[c & 1][2 * i], ac2[0], 0, 0, 0);
        ac2[1] = __builtin_amdgcn_mfma_f32_16x16x32_bf16(__builtin_bit_cast(s16x8, wo[3 * c + i]), of[c & 1][2 * i + 1], ac2[1], 0, 0, 0);
      }
      SB0();
    });
  }
  WAIT_L(0);
  BAR();
  const LAS float* c2 = (const LAS float*)(lds + L_C2);
  float v[3][4];
  f32x2 sq[3];
  LAS f32x2* st = (LAS f32x2*)(lds + L_ST);
  LAS f32x2* fin = (LAS f32x2*)(lds + L_FIN);
  {
    const f32x4 ga = *(const LAS f32x4*)(c2 + w * 16 + 4 * g);
    resid_stats(ac2[0], xr[0], ga, v[0], xr[0], sq[0]);
    resid_stats(ac2[1], xr[1], ga, v[1], xr[1], sq[1]);
    if (g < 2) st[w * 32 + g * 16 + li] = g ? sq[1] : sq[0];
    if (w < 2) {
      const LAS f32x4* t8 = (const LAS f32x4*)(lds + L_CONST + w * 1024 + l16);
      const f32x4 a8 = (t8[0] + t8[128]) + (t8[256] + t8[384]);
      resid_stats(a8, xr[2], *(const LAS f32x4*)(c2 + 128 + 4 * g), v[2], xr[2], sq[2]);
      if (g == 0) st[8 * 32 + w * 16 + li] = sq[2];
    }
  }
  WAIT_L(0);
  BAR();
  if (w == 7) {
    const int r = lane & 31;
    float mt_ = 0.f;
    f32x2 sv[9];
#pragma unroll
    for (int k = 0; k < 9; ++k) { sv[k] = st[k * 32 + r]; mt_ += sv[k][0]; }
    mt_ *= (1.f / 9.f);
    float m2 = 0.f;
#pragma unroll
    for (int k = 0; k < 9; ++k) { const float dl = sv[k][0] - mt_; m2 += sv[k][1] + 16.f * dl * dl; }
    GAS unsigned long long* xch = (GAS unsigned long long*)(ws + OFF_XCH) + (size_t)rt2 * 256;
    const float mine = (lane < 32) ? mt_ : m2;
    __hip_atomic_store(xch + nq2 * 64 + lane, ((unsigned long long)XCH_TAG << 32) | __float_as_uint(mine), __ATOMIC_RELAXED, __HIP_MEMORY_SCOPE_AGENT);
    float o[4];
    for (unsigned spin = 0; spin < (1u << 16); ++spin) {
      bool ok = true;
#pragma unroll
      for (int k = 0; k < 4; ++k) {
        const unsigned long long gv = __hip_atomic_load(xch + k * 64 + lane, __ATOMIC_RELAXED, __HIP_MEMORY_SCOPE_AGENT);
        o[k] = __uint_as_float((unsigned)gv); ok &= ((unsigned)(gv >> 32) == XCH_TAG);
      }
      if (__all(ok)) break;
      __builtin_amdgcn_s_sleep(1);
    }
    const float mean = 0.25f * ((o[0] + o[1]) + (o[2] + o[3]));
    float t = 0.f;
#pragma unroll
    for (int k = 0; k < 4; ++k) { const float dl = o[k] - mean; t += 144.f * dl * dl; }
    const auto sw = __builtin_amdgcn_permlane32_swap(__float_as_uint(mean), __float_as_uint(mean), false, false);
    const float m2s = 4.f * __uint_as_float(sw[1]);
    if (lane < 32) {
      const float var = (t + m2s) * (1.f / D);
      fin[r] = (f32x2){rb(mean), __builtin_amdgcn_rcpf(rb(__builtin_amdgcn_sqrtf(rb(rb(var) + 1e-5f))))};
    }
  }
  const f32x4 sb = *(const LAS f32x4*)(c2 + 144 + w * 16 + 4 * g), g2 = *(const LAS f32x4*)(c2 + 288 + w * 16 + 4 * g);
  WAIT_L(0);
  BAR();
  {
    const char* h2b = ws + OFF_H2 + (size_t)(rt2 * 36 + nq2 * 9) * 1024;
    const unsigned ho = ((g >> 1) * 32 + li) * 16 + (g & 1) * 8;
    gstore8_sc1(norm_mod(v[0], fin[li], g2, sb), ho + w * 1024, h2b);
    gstore8_sc1(norm_mod(v[1], fin[16 + li], g2, sb), ho + w * 1024 + 256, h2b);
    if (w < 2) gstore8_sc1(norm_mod(v[2], fin[w * 16 + li], *(const LAS f32x4*)(c2 + 288 + 128 + 4 * g), *(const LAS f32x4*)(c2 + 144 + 128 + 4 * g)), ho + 8 * 1024 + w * 256, h2b);
  }
  {
    const char* x1b = ws + OFF_X1 + (size_t)(rt2 * 72 + nq2 * 9) * 512;
    gstore8_sc1(xr[0], lane * 8 + w * 512, x1b); gstore8_sc1(xr[1], lane * 8 + (36 + w) * 512, x1b);
    if (w < 2) gstore8_sc1(xr[2], lane * 8 + (w * 36 + 8) * 512, x1b);
  }
  WAIT_V(0);
  arrive(lds + G1_ARR + 12, (GAS unsigned*)(ws + OFF_CNT) + (48 + (rt2 >> 3)) * 64, lane);
#pragma unroll
  for (int i = 0; i < 10; ++i) G1_G(i / 5, i % 5);
  }
  const GAS unsigned* cntw = (const GAS unsigned*)(ws + OFF_CNT) + (16 + (mt2 >> 1) * 4 + ksl) * 64;
  unsigned flagv;
  GAS unsigned* cntB;
  const char* wsrc2 = (const char*)w2 + (size_t)(ksl * 1152 + (w >> 2) * 144 + (w & 3) * 16) * (D * 2) + nq * 288;
  const char* wsrc2b = (const char*)w2 + (size_t)(ksl * 1152 + w * 144 + 128) * (D * 2) + nq * 288;
  const unsigned wdst2 = lbase + w * 4608;
#define G2_G(st, i) do { const char* _s = ((st) < 8) ? wsrc2 + (size_t)(((st) & 3) * 288 + ((st) >> 2) * 64) * (D * 2) : wsrc2b; const unsigned _d = wdst2 + (((st) + 1) & 3) * G1_STAGE; \
    if ((i) < 4) glds16(_s, so[i], _d + (i) * 1024); else if (lane < 32) glds16(_s, so[4], _d + 4096); } while (0)
  {
    const char* xsrc = ws + OFF_H2 + (size_t)(mt * 8 + w) * (36 * 1024);
    u32x4 xa[4], xb[4], xc[4];
#define G1_X(dst, st, i) do { const char* _p = xsrc + (st) * 4096; asm volatile("global_load_dwordx4 %0, %1, %2 offset:%3 sc1" : "=v"(dst[i]) : "v"(xvo), "s"(_p), "n"((i) * 1024) : "memory"); } while (0)
    const int grp = lane >> 4, q = (lane & 15) >> 2, p4 = lane & 3;
    const unsigned ta = (grp & 1) * G1_W3 + (8 * (grp >> 1) + q) * 288 + (((p4 & 1) << 4) | ((p4 >> 1) << 3));
    f32x16 acc[9];
#pragma unroll
    for (int t = 0; t < 9; ++t)
      for (int j = 0; j < 16; ++j) acc[t][j] = 0.f;
    s16x8 wf[4];
#define G1_RDKT(b, ks, t) cat8(tr_read((b) + (16 * (ks)) * 288 + (t) * 32), tr_read((b) + (16 * (ks) + 4) * 288 + (t) * 32))
#define G1_RD(b, i) G1_RDKT(b, (i) / 9, (i) % 9)
    const unsigned w2o = (ksl * 10368 + idx * 324 + (w * 7 + lane < 54 ? w * 7 + lane : 53)) * 128;
#define G1_STEP(xf, xn, st) do { LAS char* _b = lds + ((st) & 3) * G1_STAGE + ta; LAS char* _n = lds + (((st) + 1) & 3) * G1_STAGE + ta; \
    if ((st) >= 1) { if (lane < 7) touch((const char*)w2 + ((st) - 1) * (54 * 128), w2o, lbase + L_SCR); } \
    if ((st) == 0) { wf[0] = G1_RD(_b, 0); wf[1] = G1_RD(_b, 1); wf[2] = G1_RD(_b, 2); } \
    _Pragma("unroll") for (int i = 0; i < 36; ++i) { \
      if (i + 3 < 36) wf[(i + 3) & 3] = G1_RD(_b, i + 3); \
      else if ((st) < 6) wf[(i + 3) & 3] = G1_RD(_n, i + 3 - 36); \
      else if ((st) == 6) wf[(i + 3) & 3] = G1_RDKT(_n, i + 3 - 36, 0); \
      acc[i % 9] = __builtin_amdgcn_mfma_f32_32x32x16_bf16(wf[i & 3], __builtin_bit_cast(s16x8, xf[i / 9]), acc[i % 9], 0, 0, 0); \
      if ((st) == 0) { if ((i & 1) == 1 && (i >> 1) < 14) { const int v = i >> 1; \
        if (v < 4) G1_X(xn, 1, (v) & 3); else if (v < 9) G1_G(2, (v < 4) ? 0 : (v - 4) % 5); else G1_G(3, (v < 9) ? 0 : (v - 9) % 5); } } \
      else if ((i & 3) == 1) { const int v = i >> 2; \
        if (v < 4) G1_X(xn, (st) + 1, (v) & 3); \
        else if ((st) + 3 < 9) G1_G((st) + 3, (v - 4 < 0) ? 0 : v - 4); \
        else if ((st) == 6 && v < 8) G1_X(xc, 8, (v) & 3); } \
      SB0(); } } while (0)
    if (w == 7) wait_count((const GAS unsigned*)(ws + OFF_CNT) + (48 + mt) * 64, 32u);
    WAIT_V(0);
    BAR();
#pragma unroll
    for (int i = 0; i < 4; ++i) G1_X(xa, 0, i);
    G1_WAITX(0, xa); G1_STEP(xa, xb, 0);
    G1_WAITX(5, xb); BAR(); G1_STEP(xb, xa, 1);
    G1_WAITX(5, xa); BAR(); G1_STEP(xa, xb, 2);
    G1_WAITX(5, xb); BAR(); G1_STEP(xb, xa, 3);
    G1_WAITX(5, xa); BAR(); G1_STEP(xa, xb, 4);
    G1_WAITX(5, xb); BAR(); G1_STEP(xb, xa, 5);
    G1_WAITX(5, xa); BAR(); G1_STEP(xa, xb, 6);
    G1_WAITX(0, xb); G1_WAITX(0, xc); BAR();
#pragma unroll
    for (int i = 0; i < 5; ++i) {
      const int ci = i * 64 + lane;
      const int pr = ci / 18, ch = ci - pr * 18;
      const int r = pr ^ (((pr >> 3) & 1) << 2);
      so[i] = r * (D * 2) + ch * 16;
    }
    LAS char* b7 = lds + 3 * G1_STAGE + ta; LAS char* b8 = lds + ta;
    const int m = lane & 31, h = lane >> 5;
    const int row = mt * 256 + w * 32 + m;
    const int cl = idx >> 2;
    const unsigned dbase = (((row >> 4) * 144 + ksl * 36) * 64 + h * 16 + (row & 15)) * 16;
    const unsigned d03 = dbase + cl * 2048, d8 = dbase + (32 + (cl >> 1)) * 1024 + (cl & 1) * 512;
    GAS unsigned* cntA = (GAS unsigned*)(ws + OFF_CNT) + (16 + mt * 4 + ksl) * 64;
#define G1_RT(i) (((i) & 7) < 4 ? G1_RDKT(b7, (i) & 3, (i) >> 3) : G1_RDKT(b8, (i) & 3, (i) >> 3))
    float e[8];
    static_for<0, 80>([&](auto I) {
      constexpr int i = decltype(I)::value, t = i >> 3, j = i & 7;
      if constexpr (i + 3 < 72) wf[(i + 3) & 3] = G1_RT(i + 3);
      if constexpr (i < 72) {
        const u32x4 xx = (j < 4) ? xb[j & 3] : xc[j & 3];
        acc[t] = __builtin_amdgcn_mfma_f32_32x32x16_bf16(wf[i & 3], __builtin_bit_cast(s16x8, xx), acc[t], 0, 0, 0);
      }
      if constexpr (t >= 1) {
        e[j] = fast_silu(acc[t - 1][j]) * acc[t - 1][8 + j];
        if constexpr (j == 7) {
          constexpr int tt = t - 1;
          const u32x4 hv = (u32x4){pk_bf16(e[0], e[1]), pk_bf16(e[2], e[3]), pk_bf16(e[4], e[5]), pk_bf16(e[6], e[7])};
          if constexpr (tt < 4) gstore16_sc1<(tt >> 1) * 1024 + (tt & 1) * 512>(hv, d03, hhb);
          else if constexpr (tt < 8) gstore16_sc1<((tt & 3) >> 1) * 1024 + (tt & 1) * 512>(hv, d03, hhb + 16 * 1024);
          else gstore16_sc1<0>(hv, d8, hhb);
        }
      }
      if constexpr (i >= 2 && i < 40 && (i & 3) == 2) { constexpr int v = (i - 2) >> 2; G2_G(v / 5, v % 5); }
      if constexpr (i == 54) {
        WAIT_V(1);
        arrive(lds + G1_ARR, cntA, lane);
      }
      if constexpr (i == 74) { if (w == 7) gload4_sc1(flagv, cntw); }
      SB0();
    });
    cntB = cntA + 32;
  }
  if (w == 7) { wait_flag<1>(flagv); if (__builtin_amdgcn_readfirstlane(flagv) < 8u) wait_count(cntw, 8u); }
  const char* xs0 = hhb + ((size_t)(mt2 * 8 + mq * 2 + kg) * 144 + ksl * 36 + kg * 2) * 1024;
  const char* xs1 = hhb + ((size_t)(mt2 * 8 + mq * 2 + (kg ^ 1)) * 144 + ksl * 36 + kg * 2) * 1024;
  u32x4 xr[3][4];
#define G2_X(st, i) gload16_sc1<((i) >> 1) * 1024>(xr[(st) % 3][i], xvo, (((i) & 1) ? xs1 : xs0) + (st) * 4096)
  const int g = lane >> 4;
  unsigned tb[2][2];
  {
    const int q = (lane & 15) >> 2, p4 = lane & 3;
#pragma unroll
    for (int t = 0; t < 2; ++t) { tb[0][t] = lbase + G1_STAGE + (kg * 64 + 8 * g + ((4 * t) ^ (4 * (g & 1))) + q) * 288 + p4 * 8; tb[1][t] = tb[0][t] + G1_STAGE; }
    pinu(tb[0][0], tb[0][1]); pinu(tb[1][0], tb[1][1]);
  }
  f32x4 acc[2][9];
#pragma unroll
  for (int i = 0; i < 2; ++i)
#pragma unroll
    for (int t = 0; t < 9; ++t) { acc[i][t] = (f32x4){0.f, 0.f, 0.f, 0.f}; asm volatile("" : "+v"(acc[i][t])); }
  constexpr int PF2 = 5, RG2 = 6;
  s16x8 wf[RG2];
#define G2_RD(st, i) cat8(tr_read((LAS char*)(size_t)(tb[(st) & 1][0] + ((i) / 9) * (32 * 288) + ((i) % 9) * 32)), \
                          tr_read((LAS char*)(size_t)(tb[(st) & 1][1] + ((i) / 9) * (32 * 288) + ((i) % 9) * 32)))
  BAR();
  static_for<0, 8>([&](auto I) { constexpr int i = decltype(I)::value; G2_X(i / 4, i % 4); });
  static_for<0, PF2>([&](auto I) { constexpr int i = decltype(I)::value; wf[i % RG2] = G2_RD(0, i); });
  wait4<4>(xr[0]);
  arrive(lds + G1_ARR + 4, cntB, lane);
  const int fm = w & 1, fs = w >> 1;
  const int T = mt2 * 8 + 2 * ksl + fm;
  const int col = nq * 144 + 32 * fs + 4 * g;
  u32x2 x1v[3]; f32x4 gb[3];
  auto issue_p5 = [&]() {
    const char* xp = ws + OFF_X1 + (size_t)(T * 36 + nq * 9) * 512;
    const unsigned l8 = lane * 8;
    gload8_sc1(x1v[0], l8 + fs * 1024, xp); gload8_sc1(x1v[1], l8 + fs * 1024 + 512, xp); gload8_sc1(x1v[2], l8 + 4096, xp);
  };
  static_for<0, 162>([&](auto U) {
    constexpr int u = decltype(U)::value, st = u / 18, i = u % 18;
    if constexpr (i == 12 && st < 8) {
      wait4<g2_issued_before(u) - 9 * (st + 2)>(xr[(st + 1) % 3]);
      if constexpr (st == 1) {
        wait_flag<g2_issued_before(u) - 9 * (st + 2)>(flagv);
        if (w == 0) { if (__builtin_amdgcn_readfirstlane(flagv) < 8u) wait_count(cntw + 32, 8u); }
      }
      BAR();
      if constexpr (st >= 1) {
        constexpr unsigned d = (((st + 2) & 3) >= 2) ? 2 * G1_STAGE : (unsigned)(-2 * G1_STAGE);
        tb[(st + 1) & 1][0] += d; tb[(st + 1) & 1][1] += d;
        pinu(tb[(st + 1) & 1][0], tb[(st + 1) & 1][1]);
      }
    }
    constexpr int op = g2_op_at(u);
    if constexpr (u == 17) gload4_sc1(flagv, cntw + 32);
    if constexpr (op >= 0) { if constexpr (op % 9 < 5) G2_G(op / 9, op % 9); else G2_X(op / 9, op % 9 - 5); }
    if constexpr (u == 140) issue_p5();
    if constexpr (u + PF2 < 162) wf[(u + PF2) % RG2] = G2_RD((u + PF2) / 18, (u + PF2) % 18);
    MFMA16A(wf[u % RG2], xr[st % 3][2 * (i / 9)], acc[0][i % 9]);
    MFMA16A(wf[u % RG2], xr[st % 3][2 * (i / 9) + 1], acc[1][i % 9]);
    SB0();
  });
  asm volatile("s_nop 7" : "+v"(acc[0][0]), "+v"(acc[0][1]), "+v"(acc[0][2]), "+v"(acc[0][3]), "+v"(acc[0][4]), "+v"(acc[0][5]), "+v"(acc[0][6]), "+v"(acc[0][7]), "+v"(acc[0][8]),
               "+v"(acc[1][0]), "+v"(acc[1][1]), "+v"(acc[1][2]), "+v"(acc[1][3]), "+v"(acc[1][4]), "+v"(acc[1][5]), "+v"(acc[1][6]), "+v"(acc[1][7]), "+v"(acc[1][8]) :: "memory");
  BAR();
  LAS f32x4* ex = (LAS f32x4*)lds;
#pragma unroll
  for (int t = 0; t < 9; ++t) ex[(w * 9 + t) * 64 + lane] = acc[1][t];
  WAIT_L(0);
  BAR();
  {
    f32x4 v[9];
#pragma unroll
    for (int t = 0; t < 9; ++t) v[t] = acc[0][t] + ex[((w ^ 4) * 9 + t) * 64 + lane];
    if (mq == ksl) {
      LAS f32x4* own = ex + (72 + kg * 9) * 64 + lane;
#pragma unroll
      for (int t = 0; t < 9; ++t) own[t * 64] = v[t];
    } else {
      const char* dst = ws + OFF_PART + ((size_t)(((ksl * 16 + mt2) * 4 + nq) * 8 + mq * 2 + kg) * 5) * 1024;
      static_for<0, 4>([&](auto I) { constexpr int i = decltype(I)::value;
        gstore16_sc1<i * 1024>((u32x4){pk_bf16(v[2 * i][0], v[2 * i][1]), pk_bf16(v[2 * i][2], v[2 * i][3]), pk_bf16(v[2 * i + 1][0], v[2 * i + 1][1]), pk_bf16(v[2 * i + 1][2], v[2 * i + 1][3])}, xvo, dst); });
      gstore8_sc1((u32x2){pk_bf16(v[8][0], v[8][1]), pk_bf16(v[8][2], v[8][3])}, xvo + 4096, dst);
    }
  }
  asm volatile("s_waitcnt vmcnt(0) lgkmcnt(0)" : "+v"(x1v[0]), "+v"(x1v[1]), "+v"(x1v[2]) :: "memory");
  BAR();
  GAS unsigned* cnt4 = (GAS unsigned*)(ws + OFF_CNT) + (64 + mt2 * 4 + nq) * 64;
  if (tid == 448) __hip_atomic_fetch_add(cnt4, 1u, __ATOMIC_RELAXED, __HIP_MEMORY_SCOPE_AGENT);
  f32x4 y[3];
  {
    const LAS float* gp = (const LAS float*)(lds + L_C2) + 432 + 4 * g;
    gb[0] = *(const LAS f32x4*)(gp + 32 * fs); gb[1] = *(const LAS f32x4*)(gp + 32 * fs + 16); gb[2] = *(const LAS f32x4*)(gp + 128);
  }
  y[0] = ex[(72 + fm * 9 + 2 * fs) * 64 + lane]; y[1] = ex[(72 + fm * 9 + 2 * fs + 1) * 64 + lane]; y[2] = ex[(72 + fm * 9 + 8) * 64 + lane];
  if (w == 7) wait_count(cnt4, 4u);
  BAR();
  {
    u32x4 pa[3], pb[3];
#pragma unroll
    for (int k = 0; k < 3; ++k) {
      const int src = (ksl + 1 + k) & 3;
      const char* p = ws + OFF_PART + ((size_t)(((src * 16 + mt2) * 4 + nq) * 8 + 2 * ksl + fm) * 5) * 1024;
      gload16_sc1<0>(pa[k], xvo + fs * 1024, p);
      if (fs == 0) gload16_sc1<0>(pb[k], xvo + 4096, p);
    }
    asm volatile("s_waitcnt vmcnt(0)" : "+v"(pa[0]), "+v"(pa[1]), "+v"(pa[2]), "+v"(pb[0]), "+v"(pb[1]), "+v"(pb[2]) :: "memory");
#pragma unroll
    for (int k = 0; k < 3; ++k) {
      y[0] += (f32x4){bf_lo(pa[k][0]), bf_hi(pa[k][0]), bf_lo(pa[k][1]), bf_hi(pa[k][1])};
      y[1] += (f32x4){bf_lo(pa[k][2]), bf_hi(pa[k][2]), bf_lo(pa[k][3]), bf_hi(pa[k][3])};
      y[2] += (f32x4){bf_lo(pb[k][0]), bf_hi(pb[k][0]), bf_lo(pb[k][1]), bf_hi(pb[k][1])};
    }
    const int row = 32 * (T >> 1) + 2 * (lane & 15) + (T & 1);
    u16* op = out + (size_t)row * D;
#pragma unroll
    for (int k = 0; k < 3; ++k) {
      const u32x2 o = (u32x2){pk_bf16(y[k][0] * gb[k][0] + bf_lo(x1v[k][0]), y[k][1] * gb[k][1] + bf_hi(x1v[k][0])),
                              pk_bf16(y[k][2] * gb[k][2] + bf_lo(x1v[k][1]), y[k][3] * gb[k][3] + bf_hi(x1v[k][1]))};
      if (k < 2) *(u32x2*)(op + col + 16 * k) = o;
      else if (fs == 0) *(u32x2*)(op + nq * 144 + 128 + 4 * g) = o;
    }
  }
}

extern "C" void kernel_launch(void* const* d_in, const int* in_sizes, int n_in, void* d_out, int out_size,
                              void* d_ws, size_t ws_size, hipStream_t stream) {
  const u16* x = (const u16*)d_in[0];
  const u16* wqkv = (const u16*)d_in[1];
  const u16* wo = (const u16*)d_in[2];
  const u16* w1 = (const u16*)d_in[4];
  const u16* w3 = (const u16*)d_in[5];
  const u16* w2 = (const u16*)d_in[6];
  const u16* c = (const u16*)d_in[7];
  const u16* wmod = (const u16*)d_in[8];
  char* ws = (char*)d_ws;
  (void)hipFuncSetAttribute((const void*)km_main, hipFuncAttributeMaxDynamicSharedMemorySize, KM_LDS);
  k0_prep<<<K0_D, 384, 0, stream>>>(x, c, wmod, wqkv, wo, ws);
  km_main<<<256, 512, KM_LDS, stream>>>(ws, x, w1, w3, w2, (u16*)d_out);
}
```

```cpp
#include <hip/hip_runtime.h>
#include <hip/hip_bf16.h>
#include <stdint.h>

typedef unsigned short u16;
typedef short s16x4 __attribute__((ext_vector_type(4)));
typedef short s16x8 __attribute__((ext_vector_type(8)));
typedef float f32x2 __attribute__((ext_vector_type(2)));
typedef float f32x4 __attribute__((ext_vector_type(4)));
typedef float f32x16 __attribute__((ext_vector_type(16)));
typedef unsigned u32x2 __attribute__((ext_vector_type(2)));
typedef unsigned u32x4 __attribute__((ext_vector_type(4)));
typedef __bf16 bf16x2 __attribute__((ext_vector_type(2)));
#define LAS __attribute__((address_space(3)))
#define GAS __attribute__((address_space(1)))

constexpr int S = 2048, D = 576, F = 4608;
constexpr size_t OFF_MOD = 0;
constexpr size_t OFF_XN = 49152;
constexpr size_t OFF_O = OFF_XN + (size_t)S * D * 2;
constexpr size_t OFF_X1 = OFF_O + (size_t)S * D * 2;
constexpr size_t OFF_H2 = OFF_X1 + (size_t)S * D * 4;
constexpr size_t OFF_HH = OFF_H2 + (size_t)S * D * 2;
constexpr size_t OFF_PART = OFF_HH + (size_t)S * F * 2;
constexpr size_t OFF_WQ = OFF_PART + (size_t)4 * 512 * 5 * 1024;
constexpr size_t OFF_WO = OFF_WQ + (size_t)16 * 126 * 1024;
constexpr size_t OFF_XCH = OFF_WO + (size_t)36 * 18 * 1024;
constexpr size_t OFF_CNT = OFF_XCH + (size_t)64 * 4 * 64 * 8;
constexpr size_t OFF_END = OFF_CNT + 32768;
constexpr unsigned XCH_TAG = 0x5a17c3e1u;

__device__ __forceinline__ float bf_lo(unsigned v) { return __uint_as_float(v << 16); }
__device__ __forceinline__ float bf_hi(unsigned v) { return __uint_as_float(v & 0xffff0000u); }
__device__ __forceinline__ unsigned pk_bf16(float lo, float hi) {
  bf16x2 r; r.x = (__bf16)lo; r.y = (__bf16)hi;
  return __builtin_bit_cast(unsigned, r);
}
__device__ __forceinline__ float rb(float f) { return bf_lo(pk_bf16(f, 0.f)); }
template <int CTRL> __device__ __forceinline__ float dppf(float x) { return __int_as_float(__builtin_amdgcn_update_dpp(0, __float_as_int(x), CTRL, 0xf, 0xf, true)); }
__device__ __forceinline__ float sum16(float x) { auto r = __builtin_amdgcn_permlane16_swap(__float_as_uint(x), __float_as_uint(x), false, false); return __uint_as_float(r[0]) + __uint_as_float(r[1]); }
__device__ __forceinline__ float sum32(float x) { auto r = __builtin_amdgcn_permlane32_swap(__float_as_uint(x), __float_as_uint(x), false, false); return __uint_as_float(r[0]) + __uint_as_float(r[1]); }
__device__ __forceinline__ float max16(float x) { auto r = __builtin_amdgcn_permlane16_swap(__float_as_uint(x), __float_as_uint(x), false, false); return fmaxf(__uint_as_float(r[0]), __uint_as_float(r[1])); }
__device__ __forceinline__ float max32(float x) { auto r = __builtin_amdgcn_permlane32_swap(__float_as_uint(x), __float_as_uint(x), false, false); return fmaxf(__uint_as_float(r[0]), __uint_as_float(r[1])); }
__device__ __forceinline__ void rb2(float& a, float& b) { const unsigned p = pk_bf16(a, b); a = bf_lo(p); b = bf_hi(p); }
__device__ __forceinline__ float fast_silu(float g) { return g * __builtin_amdgcn_rcpf(1.f + __builtin_amdgcn_exp2f(g * -1.44269504f)); }

#define WAIT_V(n) asm volatile("s_waitcnt vmcnt(%0)" ::"n"(n) : "memory")
#define WAIT_L(n) asm volatile("s_waitcnt lgkmcnt(%0)" ::"n"(n) : "memory")
#define BAR() do { asm volatile("" ::: "memory"); __builtin_amdgcn_s_barrier(); asm volatile("" ::: "memory"); } while (0)
template <int IMM, class T> __device__ __forceinline__ void gload16(T& dst, unsigned voff, const void* sbase) {
  asm volatile("global_load_dwordx4 %0, %1, %2 offset:%3" : "=v"(dst) : "v"(voff), "s"(sbase), "n"(IMM) : "memory");
}
template <int IMM, class T> __device__ __forceinline__ void gload8(T& dst, unsigned voff, const void* sbase) {
  asm volatile("global_load_dwordx2 %0, %1, %2 offset:%3" : "=v"(dst) : "v"(voff), "s"(sbase), "n"(IMM) : "memory");
}

__device__ __forceinline__ void glds16(const void* sbase, unsigned voff, unsigned ldsaddr) {
  asm volatile("s_mov_b32 m0, %2\n\ts_nop 0\n\tglobal_load_lds_dwordx4 %0, %1" :: "v"(voff), "s"(sbase), "s"(ldsaddr) : "memory");
}
__device__ __forceinline__ void glds16_sc1(const void* sbase, unsigned voff, unsigned ldsaddr) {
  asm volatile("s_mov_b32 m0, %2\n\ts_nop 0\n\tglobal_load_lds_dwordx4 %0, %1 sc1" :: "v"(voff), "s"(sbase), "s"(ldsaddr) : "memory");
}
#define SB0() __builtin_amdgcn_sched_barrier(0)
template <int I> struct IC { static constexpr int value = I; };
template <int B, int E, class Fn> __device__ __forceinline__ void static_for(Fn&& f) {
  if constexpr (B < E) { f(IC<B>{}); static_for<B + 1, E>(f); }
}
__device__ __forceinline__ void touch(const void* sbase, unsigned voff, unsigned ldsaddr) {
  asm volatile("s_mov_b32 m0, %2\n\ts_nop 0\n\tglobal_load_lds_dword %0, %1" :: "v"(voff), "s"(sbase), "s"(ldsaddr) : "memory");
}
__device__ __forceinline__ void pad2(u32x4& r) { asm volatile("s_nop 1" : "+v"(r)); }
template <class A, class B> __device__ __forceinline__ void MFMA16A(const A& a, const B& b, f32x4& c) {
  asm volatile("v_mfma_f32_16x16x32_bf16 %0, %1, %2, %0" : "+v"(c) : "v"(a), "v"(b));
}
__device__ __forceinline__ s16x4 tr_read(LAS char* p) { return __builtin_amdgcn_ds_read_tr16_b64_v4i16((LAS s16x4*)p); }
__device__ __forceinline__ s16x8 cat8(s16x4 a, s16x4 b) { return __builtin_shufflevector(a, b, 0, 1, 2, 3, 4, 5, 6, 7); }

__device__ __forceinline__ unsigned perm_lo(unsigned a, unsigned b) { return __builtin_amdgcn_perm(b, a, 0x05040100u); }
__device__ __forceinline__ unsigned perm_hi(unsigned a, unsigned b) { return __builtin_amdgcn_perm(b, a, 0x07060302u); }

constexpr int K0_A = 162, K0_B = K0_A + 128, K0_C = K0_B + 81, K0_D = K0_C + 27, K0_E = 400, K0_F = K0_E + 8 * 4;
extern "C" __global__ __launch_bounds__(384) void k0_prep(const u16* __restrict__ x, const u16* __restrict__ c, const u16* __restrict__ wmod,
                                               const u16* __restrict__ wqkv, const u16* __restrict__ wo, char* __restrict__ ws,
                                               const u16* __restrict__ w1, const u16* __restrict__ w3, const u16* __restrict__ w2, const u16* __restrict__ outp) {
  __shared__ __attribute__((aligned(16))) float red[6 * 64];
  const int b = blockIdx.x, tid = threadIdx.x;
  const int w = tid >> 6, lane = tid & 63;
  if (b < K0_A) {
    const int kt = b / 54, cb = b - kt * 54;
    const int r0 = kt * 192 + (tid >> 3), col = cb * 64 + (tid & 7) * 8;
    u32x4 wv[4]; float sv[4];
#pragma unroll
    for (int i = 0; i < 4; ++i) wv[i] = *(const u32x4*)(wmod + (size_t)(r0 + 48 * i) * (6 * D) + col);
#pragma unroll
    for (int i = 0; i < 4; ++i) sv[i] = rb(fast_silu(bf_lo(c[r0 + 48 * i])));
    float a[8] = {0, 0, 0, 0, 0, 0, 0, 0};
#pragma unroll
    for (int i = 0; i < 4; ++i)
#pragma unroll
      for (int j = 0; j < 4; ++j) { a[2 * j] += sv[i] * bf_lo(wv[i][j]); a[2 * j + 1] += sv[i] * bf_hi(wv[i][j]); }
#pragma unroll
    for (int j = 0; j < 8; ++j) { a[j] += dppf<0x128>(a[j]); a[j] = sum32(sum16(a[j])); }
    if (lane < 8) {
      *(f32x4*)(red + w * 64 + lane * 8) = (f32x4){a[0], a[1], a[2], a[3]};
      *(f32x4*)(red + w * 64 + lane * 8 + 4) = (f32x4){a[4], a[5], a[6], a[7]};
    }
    __syncthreads();
    if (tid < 64) {
      float s = 0.f;
#pragma unroll
      for (int i = 0; i < 6; ++i) s += red[i * 64 + tid];
      ((float*)(ws + OFF_MOD))[kt * 3456 + cb * 64 + tid] = s;
    }
  } else if (b < K0_B) {
    const int i = lane & 15, g = lane >> 4;
    const int T = b - K0_A;
    const int row = 32 * (T >> 1) + 2 * i + (T & 1);
    float v[3][8];
#pragma unroll
    for (int s = 0; s < 3; ++s) {
      const u32x4 a = *(const u32x4*)(x + (size_t)row * D + ((w * 3 + s) * 4 + g) * 8);
#pragma unroll
      for (int j = 0; j < 4; ++j) { v[s][2 * j] = bf_lo(a[j]); v[s][2 * j + 1] = bf_hi(a[j]); }
    }
    float sm = 0.f;
#pragma unroll
    for (int s = 0; s < 3; ++s) sm += ((v[s][0] + v[s][1]) + (v[s][2] + v[s][3])) + ((v[s][4] + v[s][5]) + (v[s][6] + v[s][7]));
    const float mw = sum32(sum16(sm)) * (1.f / 96.f);
    float q0 = 0.f, q1 = 0.f;
#pragma unroll
    for (int s = 0; s < 3; ++s)
#pragma unroll
      for (int j = 0; j < 4; ++j) { const float d0 = v[s][2 * j] - mw, d1 = v[s][2 * j + 1] - mw; q0 += d0 * d0; q1 += d1 * d1; }
    const float q = sum32(sum16(q0 + q1));
    if (g == 0) *(f32x2*)(red + (w * 16 + i) * 2) = (f32x2){mw, q};
    __syncthreads();
    f32x2 p[6];
    float mean = 0.f;
#pragma unroll
    for (int k = 0; k < 6; ++k) { p[k] = *(const f32x2*)(red + (k * 16 + i) * 2); mean += p[k][0]; }
    mean *= (1.f / 6.f);
    float var = 0.f;
#pragma unroll
    for (int k = 0; k < 6; ++k) { const float dl = p[k][0] - mean; var += p[k][1] + 96.f * dl * dl; }
    var *= (1.f / D);
    const float mr = rb(mean);
#pragma unroll
    for (int s = 0; s < 3; ++s)
#pragma unroll
      for (int j = 0; j < 4; ++j) { v[s][2 * j] -= mr; v[s][2 * j + 1] -= mr; rb2(v[s][2 * j], v[s][2 * j + 1]); }
    const float rs = __builtin_amdgcn_rcpf(rb(__builtin_amdgcn_sqrtf(rb(rb(var) + 1e-5f))));
#pragma unroll
    for (int s = 0; s < 3; ++s) {
      u32x4 o;
#pragma unroll
      for (int j = 0; j < 4; ++j) o[j] = pk_bf16(v[s][2 * j] * rs, v[s][2 * j + 1] * rs);
      *(u32x4*)(ws + OFF_XN + ((size_t)(T * 18 + w * 3 + s) * 64 + lane) * 16) = o;
    }
  } else if (b < K0_C) {
    const int task = (b - K0_B) * 384 + tid;
    const int kq = task / 432, nq4 = task - kq * 432;
    u32x2 r[8];
#pragma unroll
    for (int i = 0; i < 8; ++i) r[i] = *(const u32x2*)(wqkv + (size_t)(kq * 8 + i) * (3 * D) + nq4 * 4);
    const int part = nq4 / 144, nh = nq4 - part * 144, ks = kq >> 2, ko = kq & 3;
#pragma unroll
    for (int e = 0; e < 4; ++e) {
      const int n = nh * 4 + e, head = n / 18, d = n - head * 18, hp = head >> 1, h = head & 1;
      int tile, idx;
      if (d < 16) { tile = (part == 0) ? 5 + h : (part == 1 ? h : 3 + h); idx = d; }
      else { tile = 2; idx = (part == 2) ? 8 + 2 * h + (d - 16) : 4 * h + 2 * part + (d - 16); }
      char* dst = ws + OFF_WQ + ((hp * 18 + ks) * 7 + tile) * 1024 + (ko * 16 + idx) * 16;
      u32x4 f;
#pragma unroll
      for (int i = 0; i < 4; ++i) f[i] = (e & 1) ? perm_hi(r[2 * i][e >> 1], r[2 * i + 1][e >> 1]) : perm_lo(r[2 * i][e >> 1], r[2 * i + 1][e >> 1]);
      *(u32x4*)dst = f;
      if (part == 2 && d >= 16) *(u32x4*)(dst + 64) = (u32x4){0u, 0u, 0u, 0u};
    }
  } else if (b >= K0_E) {
    const int job = (b - K0_E) >> 3;
    const unsigned scr = (unsigned)(size_t)(LAS float*)red;
    if (job < 2) { const unsigned o = (job * 384 + tid) * 65536u; if (o < OFF_END) touch(ws, o, scr); }
    else if (job == 2) {
      if (tid < 81) { touch(w1, tid * 65536u, scr); touch(w3, tid * 65536u, scr); touch(w2, tid * 65536u, scr); }
      if (tid < 36) touch(outp, tid * 65536u, scr);
    }
    else {
      unsigned long long pc; int o0, o1;
      asm volatile("s_getpc_b64 %0\n.Lpc_%=:\n\ts_mov_b32 %1, km_main-.Lpc_%=\n\ts_mov_b32 %2, k_end_marker-.Lpc_%=" : "=&s"(pc), "=&s"(o0), "=&s"(o1));
      const int o = tid * 128;
      if (o0 > 0 && o < o1 - o0) touch((const void*)pc, (unsigned)(o0 + o) & ~3u, scr);
    }
  } else if (b < K0_D) {
    const int task = (b - K0_C) * 384 + tid;
    const int nh = task % 144, ug = task / 144, u = ug >> 2, g = ug & 3;
    u32x2 r[8];
#pragma unroll
    for (int j = 0; j < 8; ++j) {
      const int krow = (u < 16) ? 36 * u + 18 * (j >> 2) + 4 * g + (j & 3) : 36 * (8 * (u - 16) + 2 * g + (j >> 2)) + 18 * ((j >> 1) & 1) + 16 + (j & 1);
      r[j] = *(const u32x2*)(wo + (size_t)krow * D + nh * 4);
    }
    char* dst = ws + OFF_WO + ((size_t)((nh >> 2) * 18 + u) * 64 + g * 16 + (nh & 3) * 4) * 16;
#pragma unroll
    for (int e = 0; e < 4; ++e) {
      u32x4 f;
#pragma unroll
      for (int i = 0; i < 4; ++i) f[i] = (e & 1) ? perm_hi(r[2 * i][e >> 1], r[2 * i + 1][e >> 1]) : perm_lo(r[2 * i][e >> 1], r[2 * i + 1][e >> 1]);
      *(u32x4*)(dst + e * 16) = f;
    }
    if (task < 64 * 4 * 64 / 2 + 2048) *(u32x4*)(ws + OFF_XCH + (size_t)task * 16) = (u32x4){0u, 0u, 0u, 0u};
  }
}

constexpr int G1_W3 = 64 * 288 + 128;
constexpr int G1_STAGE = 2 * G1_W3;
#define G1_WAITX(n, xf) asm volatile("s_waitcnt vmcnt(%4)" : "+v"(xf[0]), "+v"(xf[1]), "+v"(xf[2]), "+v"(xf[3]) : "n"(n) : "memory")
constexpr int g2_pos(int n) {
  const int s = n / 9, k = n % 9;
  if (s <= 1) return -1;
  return 18 * (s - 2) + 2 * k;
}
constexpr int g2_op_at(int u) { for (int n = 0; n < 81; ++n) if (g2_pos(n) == u) return n; return -1; }
constexpr int g2_issued_before(int u) { int c = 0; for (int n = 0; n < 81; ++n) if (g2_pos(n) < u) ++c; return c; }
template <int IMM, class T> __device__ __forceinline__ void gload16_sc1(T& dst, unsigned voff, const void* sbase) {
  asm volatile("global_load_dwordx4 %0, %1, %2 offset:%3 sc1" : "=v"(dst) : "v"(voff), "s"(sbase), "n"(IMM) : "memory");
}
template <int IMM> __device__ __forceinline__ void gstore16_sc1(const u32x4& v, unsigned voff, const void* sbase) {
  asm volatile("global_store_dwordx4 %0, %1, %2 offset:%3 sc1\n\ts_nop 1" :: "v"(voff), "v"(v), "s"(sbase), "n"(IMM) : "memory");
}
__device__ __forceinline__ void gload8_sc1(u32x2& dst, unsigned voff, const void* sbase) {
  asm volatile("global_load_dwordx2 %0, %1, %2 sc1" : "=v"(dst) : "v"(voff), "s"(sbase) : "memory");
}
__device__ __forceinline__ void gstore8_sc1(const u32x2& v, unsigned voff, const void* sbase) {
  asm volatile("global_store_dwordx2 %0, %1, %2 sc1\n\ts_nop 1" :: "v"(voff), "v"(v), "s"(sbase) : "memory");
}
template <int N> __device__ __forceinline__ void wait4(u32x4 (&x)[4]) {
  asm volatile("s_waitcnt vmcnt(%4)" : "+v"(x[0]), "+v"(x[1]), "+v"(x[2]), "+v"(x[3]) : "n"(N) : "memory");
}
template <int N> __device__ __forceinline__ void wait_flag(unsigned& f) { asm volatile("s_waitcnt vmcnt(%1)" : "+v"(f) : "n"(N) : "memory"); }
__device__ __forceinline__ void pinu(unsigned& a, unsigned& b) { asm volatile("" : "+v"(a), "+v"(b)); }
__device__ __forceinline__ void atomic_inc(GAS unsigned* p) {
  const unsigned one = 1u, zero = 0u;
  asm volatile("global_atomic_add %0, %1, %2" :: "v"(zero), "v"(one), "s"(p) : "memory");
}
__device__ __forceinline__ void arrive(LAS char* ctr, GAS unsigned* cnt, int lane) {
  if (lane == 0) { if (__hip_atomic_fetch_add((LAS unsigned*)ctr, 1u, __ATOMIC_RELAXED, __HIP_MEMORY_SCOPE_WORKGROUP) == 7u) atomic_inc(cnt); }
}
__device__ __forceinline__ void gload4_sc1(unsigned& dst, const GAS unsigned* p) {
  const unsigned zero = 0u;
  asm volatile("global_load_dword %0, %1, %2 sc1" : "=v"(dst) : "v"(zero), "s"(p) : "memory");
}
__device__ __forceinline__ void wait_count(const GAS unsigned* cnt, unsigned want) {
  for (unsigned spin = 0; spin < (1u << 16); ++spin) {
    if (__builtin_amdgcn_readfirstlane(__hip_atomic_load(cnt, __ATOMIC_RELAXED, __HIP_MEMORY_SCOPE_AGENT)) >= want) break;
    __builtin_amdgcn_s_sleep(1);
  }
}


constexpr int L_CONST = 4 * G1_STAGE;
constexpr int L_C2 = L_CONST + 8192;
constexpr int L_ST = L_C2 + 2304;
constexpr int L_FIN = L_ST + 2304;
constexpr int L_SCR = L_FIN + 256;
constexpr int G1_ARR = L_SCR + 256;
constexpr int KM_LDS = G1_ARR + 32;
constexpr int K1_EK = 0, K1_EV = 9 * 2 * 1024, K1_EL = K1_EV + 9 * 2 * 512;
constexpr int K1_O = 3 * G1_STAGE;
constexpr int k1_r(int p) { return p < 0 ? 0 : (21 * (p + 1) + 7) / 8; }
constexpr int k1_cum(int p) { return 9 + 3 * (p + 1 < 5 ? p + 1 : 5) + k1_r(p); }
constexpr int K1_NOPS = 41 + 29;
constexpr int k1_op(int n) {
  if (n < 3) return 96;
  if (n < 6) return 97 + n - 3;
  if (n < 9) return n - 6;
  for (int p = 0; p < 6; ++p) {
    const int lo = p ? k1_cum(p - 1) : 9;
    if (n < k1_cum(p)) { const int k = n - lo; return (p < 5 && k < 3) ? 3 * p + 3 + k : 32 + k1_r(p - 1) + k - (p < 5 ? 3 : 0); }
  }
  return n == 40 ? 100 : 64 + n - 41;
}
constexpr int k1_pos(int n) {
  if (n < k1_cum(0)) return -1;
  if (n < k1_cum(1)) return 0;
  if (n < k1_cum(2)) return 1 + (n - k1_cum(1)) / 3;
  for (int p = 3; p < 6; ++p) if (n < k1_cum(p)) return 3 * (p - 2) + (n - k1_cum(p - 1)) / 2;
  if (n == 40) return 10;
  const int k = n - 41;
  constexpr int NIN = 6;
  if (k < NIN) return 18 - NIN / 2 + k / 2;
  if (k < NIN + 3) return 18;
  if (k < NIN + 9) return 19 + (k - NIN - 3) / 3;
  return 21;
}
constexpr int k1_before(int s) { int c = 0; for (int n = 0; n < K1_NOPS; ++n) if (k1_pos(n) < s) ++c; return c; }
static_assert(k1_cum(5) == 40 && k1_before(22) == K1_NOPS, "");
struct Cst { f32x4 s0, s1, g0, g1; };
__device__ __forceinline__ void rdc(Cst& c, const LAS float* cs) {
  c.s0 = *(const LAS f32x4*)cs; c.s1 = *(const LAS f32x4*)(cs + 4); c.g0 = *(const LAS f32x4*)(cs + 576); c.g1 = *(const LAS f32x4*)(cs + 580);
}
__device__ __forceinline__ float dot2(unsigned x, float gw, float s) {
  float r; asm("v_dot2_f32_bf16 %0, %1, %2, %3" : "=v"(r) : "v"(x), "v"(gw), "v"(s)); return r;
}
__device__ __forceinline__ void modc(u32x4& xv, const Cst& c) {
  asm volatile("" : "+v"(xv));
  const u32x4 v = xv;
  float h[8];
  h[0] = dot2(v[0], c.g0[0], c.s0[0]); h[1] = dot2(v[0], c.g0[1], c.s0[1]); h[2] = dot2(v[1], c.g0[2], c.s0[2]); h[3] = dot2(v[1], c.g0[3], c.s0[3]);
  h[4] = dot2(v[2], c.g1[0], c.s1[0]); h[5] = dot2(v[2], c.g1[1], c.s1[1]); h[6] = dot2(v[3], c.g1[2], c.s1[2]); h[7] = dot2(v[3], c.g1[3], c.s1[3]);
  asm volatile("s_nop 2" : "+v"(h[0]), "+v"(h[1]), "+v"(h[2]), "+v"(h[3]), "+v"(h[4]), "+v"(h[5]), "+v"(h[6]), "+v"(h[7]));
  u32x4 r;
  r[0] = pk_bf16(h[0], h[1]); r[1] = pk_bf16(h[2], h[3]); r[2] = pk_bf16(h[4], h[5]); r[3] = pk_bf16(h[6], h[7]);
  pad2(r);
  xv = r;
}
template <int N> __device__ __forceinline__ void wait3(u32x4& a, u32x4& b, u32x4& c) {
  asm volatile("s_waitcnt vmcnt(%3)" : "+v"(a), "+v"(b), "+v"(c) : "n"(N) : "memory");
}
__device__ __forceinline__ void pin(u32x4& a) { asm volatile("" : "+v"(a)); }


__device__ __forceinline__ void resid_stats(const f32x4 a, const u32x2 xin, const f32x4 ga, float (&v)[4], u32x2& xo, f32x2& sq) {
  const unsigned p0 = pk_bf16(a[0] * ga[0] + bf_lo(xin[0]), a[1] * ga[1] + bf_hi(xin[0])), p1 = pk_bf16(a[2] * ga[2] + bf_lo(xin[1]), a[3] * ga[3] + bf_hi(xin[1]));
  v[0] = bf_lo(p0); v[1] = bf_hi(p0); v[2] = bf_lo(p1); v[3] = bf_hi(p1);
  const float mw = sum32(sum16((v[0] + v[1]) + (v[2] + v[3]))) * (1.f / 16.f);
  const float d0 = v[0] - mw, d1 = v[1] - mw, d2 = v[2] - mw, d3 = v[3] - mw;
  sq = (f32x2){mw, sum32(sum16((d0 * d0 + d1 * d1) + (d2 * d2 + d3 * d3)))};
  xo = (u32x2){p0, p1};
}
__device__ __forceinline__ u32x2 norm_mod(const float (&v)[4], const f32x2 f, const f32x4 g2, const f32x4 sb) {
  float h[4];
#pragma unroll
  for (int j = 0; j < 4; ++j) h[j] = (v[j] - f[0]) * f[1] * g2[j];
  return (u32x2){pk_bf16(h[0] + sb[0], h[1] + sb[1]), pk_bf16(h[2] + sb[2], h[3] + sb[3])};
}

extern "C" __global__ __launch_bounds__(512, 2) void km_main(char* __restrict__ ws, const u16* __restrict__ x, const u16* __restrict__ w1,
                                                  const u16* __restrict__ w3, const u16* __restrict__ w2, u16* __restrict__ out) {
  extern __shared__ __attribute__((aligned(16))) char smem[];
  LAS char* lds = (LAS char*)smem;
  const unsigned lbase = (unsigned)(size_t)lds;
  const int b = blockIdx.x, xcd = b & 7, idx = b >> 3;
  const int tid = threadIdx.x, lane = tid & 63;
  const int w = __builtin_amdgcn_readfirstlane(tid >> 6);
  const unsigned xvo = lane * 16;
  const int ksl = xcd >> 1;
  const int mt = (xcd & 1) * 4 + (idx & 3), ct = ksl * 8 + (idx >> 2);
  const int mt2 = (xcd & 1) * 8 + (idx & 7), nq = idx >> 3;
  const int mq = w & 3, kg = w >> 2;
  const char* hhb = ws + OFF_HH;
  if (tid == 0) *(LAS u32x4*)(lds + G1_ARR) = (u32x4){0u, 0u, 0u, 0u};
  unsigned so[5];
  const char* wsrc = (const char*)((w < 4) ? w1 : w3) + (size_t)((w & 3) * 16) * (F * 2) + ct * 288;
  const unsigned wdst = lbase + (w >> 2) * G1_W3 + (w & 3) * 4608;
#define G1_G(st, i) do { const char* _s = wsrc + (size_t)(st) * (64 * F * 2); const unsigned _d = wdst + ((st) & 3) * G1_STAGE; \
    if ((i) < 4) glds16(_s, so[i], _d + (i) * 1024); else if (lane < 32) glds16(_s, so[4], _d + 4096); } while (0)
  {
  const int rt = (xcd & 3) * 4 + (idx & 3), hp = (xcd >> 2) * 8 + (idx >> 2);
  const int li = lane & 15, g = lane >> 4;
  const unsigned l16 = xvo;
  const char* xsrc = ws + OFF_XN + (size_t)(8 * rt + w) * (18 * 1024);
  const char* hsrc = ws + OFF_XN + (size_t)(rt > 0 ? 8 * rt - 2 : 0) * (18 * 1024);
  const unsigned hvo = (li >> 3) * (18 * 1024) + (g * 16 + 8 + (li & 7)) * 16;
  const char* wq = ws + OFF_WQ + (size_t)hp * (126 * 1024);
  const int rt2 = 4 * rt + (hp >> 2), nq2 = hp & 3;
  const int nt = nq2 * 9 + w;
  const char* wsrc2 = ws + OFF_WO + (size_t)nt * (18 * 1024);
  const char* wsrc8 = ws + OFF_WO + (size_t)((nq2 * 9 + 8) * 18 + (w >> 1)) * 1024;
  u32x4 xf[18], wo[18], wo8[5], hx[3];
  u32x2 xr[3]; f32x4 mq3[3];
  auto issue = [&](auto N) {
    constexpr int op = k1_op(decltype(N)::value);
    if constexpr (op < 32) { const char* q = xsrc + (op & ~3) * 1024; gload16<(op & 3) * 1024>(xf[op], l16, q); }
    else if constexpr (op < 64) {
      int p = w + 8 * (op - 32); p = p < 126 ? p : 125;
      const int sk = (p * 9363) >> 16;
      glds16(wq + p * 1024, l16, lbase + (p + sk) * 1024);
    }
    else if constexpr (op < 82) { constexpr int i = op - 64; const char* q = wsrc2 + (i & ~3) * 1024; gload16<(i & 3) * 1024>(wo[i], l16, q); }
    else if constexpr (op < 87) { constexpr int i = op - 82; if (i < 4 || w < 4) gload16<0>(wo8[i], l16, wsrc8 + i * 4096); }
    else if constexpr (op < 89) gload8<(op - 87) * 1152>(xr[op - 87], (unsigned)(((rt2 * 32 + 2 * li) * D + nt * 16 + 4 * g) * 2), x);
    else if constexpr (op == 89) { if (w < 2) gload8<0>(xr[2], (unsigned)(((rt2 * 32 + 2 * li + (w & 1)) * D + nq2 * 144 + 128 + 4 * g) * 2), x); }
    else if constexpr (op < 93) {
      const int t = tid < 144 ? tid : 143, vec = t / 36, c4 = t - vec * 36;
      if (w < 3) gload16<0>(mq3[op - 90], (unsigned)(((2 + vec) * D + (vec == 3 ? nq : nq2) * 144 + c4 * 4) * 4), ws + OFF_MOD + (op - 90) * 3456 * 4);
    }
    else if constexpr (op < 100) { const int sk = w + 8 * (op - 97); gload16<0>(hx[op - 97], hvo, hsrc + (sk < 18 ? sk : 17) * 1024); }
    else {
      const int ln = idx * 108 + (w & 3) * 27 + (lane < 27 ? lane : 26);
      touch((w & 4) ? w3 : w1, (ln / 18) * (F * 2) + ksl * 2304 + (ln % 18) * 128, lbase + L_SCR);
    }
  };
  f32x4 mp[3];
  {
    const unsigned mo = (tid < 288 ? tid : 287) * 16;
    gload16<0>(mp[0], mo, ws + OFF_MOD); gload16<0>(mp[1], mo, ws + OFF_MOD + 3456 * 4); gload16<0>(mp[2], mo, ws + OFF_MOD + 6912 * 4);
  }
  static_for<3, k1_cum(0)>(issue);
  asm volatile("s_waitcnt vmcnt(%3)" : "+v"(mp[0]), "+v"(mp[1]), "+v"(mp[2]) : "n"(k1_cum(0) - 3) : "memory");
  if (tid < 288) {
    float m[4];
#pragma unroll
    for (int j = 0; j < 4; ++j) m[j] = (mp[0][j] + mp[1][j]) + mp[2][j];
    rb2(m[0], m[1]); rb2(m[2], m[3]);
    if (tid >= 144) {
#pragma unroll
      for (int j = 0; j < 4; ++j) m[j] += 1.f;
      rb2(m[0], m[1]); rb2(m[2], m[3]);
      m[0] = __uint_as_float(__float_as_uint(m[0]) >> 16); m[2] = __uint_as_float(__float_as_uint(m[2]) >> 16);
    }
    *(LAS f32x4*)(lds + L_CONST + tid * 16) = (f32x4){m[0], m[1], m[2], m[3]};
  }
  WAIT_L(0);
  BAR();
  const LAS float* cs = (const LAS float*)(lds + L_CONST) + g * 8;
  Cst cst;
  {
    Cst c1, c2;
    const int s2 = w + 16 < 18 ? w + 16 : 17;
    rdc(cst, cs + w * 32); rdc(c1, cs + (w + 8) * 32); rdc(c2, cs + s2 * 32);
    wait3<k1_cum(0) - 6>(hx[0], hx[1], hx[2]);
    modc(hx[0], cst); modc(hx[1], c1); modc(hx[2], c2);
    *(LAS u32x4*)(lds + w * 8192 + 7168 + l16) = hx[0];
    *(LAS u32x4*)(lds + (w + 8) * 8192 + 7168 + l16) = hx[1];
    *(LAS u32x4*)(lds + s2 * 8192 + 7168 + l16) = hx[2];
    rdc(cst, cs); rdc(c1, cs + 32); rdc(c2, cs + 64);
    wait3<k1_cum(0) - 9>(xf[0], xf[1], xf[2]);
    modc(xf[0], cst); modc(xf[1], c1); modc(xf[2], c2);
  }
  rdc(cst, cs + 96);
  f32x4 acc[8];
#pragma unroll
  for (int t = 0; t < 8; ++t) { acc[t] = (f32x4){0.f, 0.f, 0.f, 0.f}; asm volatile("" : "+v"(acc[t])); }
  const bool hw = w < 5;
  unsigned wa = lbase + l16, ha = wa + (w < 3 ? w * 1024 : 7168), hb = wa + (w < 3 ? 7168 : w * 1024);
  s16x8 wf[7], hf[2];
  auto rdw = [&](auto Sx, auto T0, auto T1) {
    constexpr int s = decltype(Sx)::value, off = (s % 6) * 8192;
#pragma unroll
    for (int t = decltype(T0)::value; t < decltype(T1)::value; ++t) wf[t] = *(const LAS s16x8*)(size_t)(wa + off + t * 1024);
  };
  auto rdh = [&](auto Sx) {
    constexpr int off = (decltype(Sx)::value % 6) * 8192;
    hf[0] = *(const LAS s16x8*)(size_t)(ha + off); hf[1] = *(const LAS s16x8*)(size_t)(hb + off);
  };
  asm volatile("s_waitcnt vmcnt(0) lgkmcnt(0)" : "+v"(xf[3]), "+v"(xf[4]), "+v"(xf[5]) :: "memory");
  BAR();
  rdw(IC<0>{}, IC<0>{}, IC<7>{});
  rdh(IC<0>{});
  static_for<0, 18>([&](auto Sx) {
    constexpr int s = decltype(Sx)::value, p = s / 3;
    if constexpr (s % 3 == 2 && p < 5) {
      if constexpr (p < 4) wait3<k1_before(s) - k1_cum(p + 1)>(xf[3 * p + 6], xf[3 * p + 7], xf[3 * p + 8]);
      else WAIT_V(k1_before(s) - k1_cum(5));
      BAR();
    }
    if constexpr (s == 5 || s == 11) { wa += 6 * 8192; ha += 6 * 8192; hb += 6 * 8192; asm volatile("" : "+v"(wa)); pinu(ha, hb); }
    if constexpr (s == 0) static_for<k1_before(0), k1_before(1)>(issue);
    MFMA16A(wf[0], xf[s], acc[0]); MFMA16A(wf[1], xf[s], acc[1]); MFMA16A(wf[2], xf[s], acc[2]); MFMA16A(xf[s], wf[3], acc[3]);
    if constexpr (s < 17) rdw(IC<s + 1>{}, IC<0>{}, IC<4>{});
    if constexpr (s + 3 < 18) modc(xf[s + 3], cst);
    if constexpr (s + 4 < 18) rdc(cst, cs + (s + 4) * 32);
    MFMA16A(xf[s], wf[4], acc[4]); MFMA16A(wf[5], xf[s], acc[5]); MFMA16A(wf[6], xf[s], acc[6]);
    if constexpr (s < 17) rdw(IC<s + 1>{}, IC<4>{}, IC<7>{});
    if (hw) { MFMA16A(hf[0], hf[1], acc[7]); if constexpr (s < 17) rdh(IC<s + 1>{}); }
    if constexpr (s > 0) static_for<k1_before(s), k1_before(s + 1)>(issue);
  });
  asm volatile("s_nop 7" : "+v"(acc[0]), "+v"(acc[1]), "+v"(acc[2]), "+v"(acc[3]), "+v"(acc[4]), "+v"(acc[5]), "+v"(acc[6]), "+v"(acc[7]) :: "memory");
  unsigned kf[2][2], qf[2][2], vf[2][2], lf[2];
#pragma unroll
  for (int h = 0; h < 2; ++h) {
    kf[h][0] = pk_bf16(acc[h][0], acc[h][1]); kf[h][1] = pk_bf16(acc[h][2], acc[h][3]);
    qf[h][0] = pk_bf16(acc[5 + h][0], acc[5 + h][1]); qf[h][1] = pk_bf16(acc[5 + h][2], acc[5 + h][3]);
    vf[h][0] = pk_bf16(acc[3 + h][0], acc[3 + h][1]); vf[h][1] = pk_bf16(acc[3 + h][2], acc[3 + h][3]);
  }
  lf[0] = pk_bf16(acc[2][0], acc[2][1]); lf[1] = pk_bf16(acc[2][2], acc[2][3]);
#pragma unroll
  for (int h = 0; h < 2; ++h) {
    *(LAS u32x4*)(lds + K1_EK + (w * 2 + h) * 1024 + l16) = (u32x4){kf[h][0], kf[h][1], lf[1], 0u};
    *(LAS u32x2*)(lds + K1_EV + (w * 2 + h) * 512 + lane * 8) = (u32x2){vf[h][0], vf[h][1]};
  }
  if (g == 2) *(LAS u32x2*)(lds + K1_EL + (w * 16 + li) * 8) = (u32x2){lf[0], lf[1]};
  {
    const u32x2 hv = (u32x2){pk_bf16(acc[7][0], acc[7][1]), pk_bf16(acc[7][2], acc[7][3])};
    if (w < 2) *(LAS u32x2*)(lds + K1_EK + (16 + w) * 1024 + l16) = hv;
    else if (w == 2) {
      *(LAS u32x2*)(lds + K1_EK + 16 * 1024 + l16 + 8) = (u32x2){hv[1], 0u};
      *(LAS u32x2*)(lds + K1_EK + 17 * 1024 + l16 + 8) = (u32x2){hv[1], 0u};
      if (g == 2) *(LAS u32x2*)(lds + K1_EL + (128 + li) * 8) = hv;
    } else if (w < 5) *(LAS u32x2*)(lds + K1_EV + (16 + w - 3) * 512 + lane * 8) = hv;
  }
  static_for<k1_before(18), k1_before(19)>(issue);
  WAIT_L(0);
  BAR();
  {
    const int par = w & 1;
    const bool first = (w < 2);
    const int pw = first ? 8 : w - 2;
    const int pki = first ? 8 * par + (li & 7) : li;
    const int pvg = first ? 2 * par + (g & 1) : g;
    const int q4 = li >> 2;
    const int plk = first ? 8 * par + ((4 * g + q4) & 7) : 4 * g + q4;
    const s16x8 vl = cat8(tr_read(lds + K1_EL + (pw * 16 + plk) * 8), tr_read(lds + K1_EL + (w * 16 + 4 * g + q4) * 8));
    const bool prev_ok = !(first && rt == 0);
    unsigned op[2][2], ol[2];
    bool usep[4], useo[4];
#pragma unroll
    for (int j = 0; j < 4; ++j) { const int kk = 4 * g + j; usep[j] = prev_ok && (kk >= li + 9); useo[j] = (kk <= li) && (li - kk <= 7); }
    const unsigned mp01 = (usep[0] ? 0xffffu : 0u) | (usep[1] ? 0xffff0000u : 0u), mp23 = (usep[2] ? 0xffffu : 0u) | (usep[3] ? 0xffff0000u : 0u);
    static_for<k1_before(19), k1_before(20)>(issue);
#pragma unroll
    for (int h = 0; h < 2; ++h) {
      const u32x4 kp = *(const LAS u32x4*)(lds + K1_EK + (pw * 2 + h) * 1024 + (g * 16 + pki) * 16);
      const u32x2 vp = *(const LAS u32x2*)(lds + K1_EV + (pw * 2 + h) * 512 + (pvg * 16 + li) * 8);
      const u32x4 ko = (u32x4){kf[h][0], kf[h][1], lf[1], 0u};
      const u32x4 qq = (u32x4){qf[h][0], qf[h][1], (g == h) ? lf[0] : 0u, 0u};
      const f32x4 z = (f32x4){0.f, 0.f, 0.f, 0.f};
      const f32x4 so = __builtin_amdgcn_mfma_f32_16x16x32_bf16(__builtin_bit_cast(s16x8, ko), __builtin_bit_cast(s16x8, qq), z, 0, 0, 0);
      const f32x4 sp = __builtin_amdgcn_mfma_f32_16x16x32_bf16(__builtin_bit_cast(s16x8, kp), __builtin_bit_cast(s16x8, qq), z, 0, 0, 0);
      float lg[4];
#pragma unroll
      for (int j = 0; j < 4; ++j) lg[j] = (usep[j] || useo[j]) ? (usep[j] ? sp[j] : so[j]) * 0.34004648f : -3.0e38f;
      const float mx = max32(max16(fmaxf(fmaxf(lg[0], lg[1]), fmaxf(lg[2], lg[3]))));
#pragma unroll
      for (int j = 0; j < 4; ++j) lg[j] = __builtin_amdgcn_exp2f(lg[j] - mx);
      const float inv = __builtin_amdgcn_rcpf(sum32(sum16((lg[0] + lg[1]) + (lg[2] + lg[3]))));
      const unsigned p01 = pk_bf16(lg[0] * inv, lg[1] * inv), p23 = pk_bf16(lg[2] * inv, lg[3] * inv);
      const u32x4 pf = (u32x4){p01 & mp01, p23 & mp23, p01 & ~mp01, p23 & ~mp23};
      const u32x4 vv = (u32x4){vp[0], vp[1], vf[h][0], vf[h][1]};
      const f32x4 oo = __builtin_amdgcn_mfma_f32_16x16x32_bf16(__builtin_bit_cast(s16x8, vv), __builtin_bit_cast(s16x8, pf), z, 0, 0, 0);
      const f32x4 lo = __builtin_amdgcn_mfma_f32_16x16x32_bf16(vl, __builtin_bit_cast(s16x8, pf), z, 0, 0, 0);
      op[h][0] = pk_bf16(oo[0], oo[1]); op[h][1] = pk_bf16(oo[2], oo[3]);
      ol[h] = pk_bf16(lo[2 * h], lo[2 * h + 1]);
    }
    static_for<k1_before(20), k1_before(21)>(issue);
    const char* ob = ws + OFF_O + (size_t)(8 * rt + w) * (18 * 1024);
    const u32x4 o4 = (u32x4){op[0][0], op[0][1], op[1][0], op[1][1]};
    const unsigned oo0 = hp * 1024 + l16;
    asm volatile("global_store_dwordx4 %0, %1, %2 sc1\n\ts_nop 1" :: "v"(oo0), "v"(o4), "s"(ob) : "memory");
    if (g == 0) {
      const u32x2 o2 = (u32x2){ol[0], ol[1]};
      const unsigned oo1 = (16 + (hp >> 3)) * 1024 + (((hp & 7) >> 1) * 16 + li) * 16 + 8 * (hp & 1);
      asm volatile("global_store_dwordx2 %0, %1, %2 sc1\n\ts_nop 1" :: "v"(oo1), "v"(o2), "s"(ob) : "memory");
    }
  }
  WAIT_V(0);
  GAS unsigned* cnt1 = (GAS unsigned*)(ws + OFF_CNT) + rt * 64;
  arrive(lds + G1_ARR + 8, cnt1, lane);
  static_for<k1_before(21), K1_NOPS>(issue);
#pragma unroll
  for (int i = 0; i < 5; ++i) {
    const int ci = i * 64 + lane;
    const int r = ci / 18, ch = ci - r * 18;
    so[i] = r * (F * 2) + ch * 16;
  }
  if (w == 7) wait_count(cnt1, 16u);
  BAR();
  {
    const char* osrc = ws + OFF_O + (size_t)rt2 * (36 * 1024) + w * 4608;
#pragma unroll
    for (int i = 0; i < 4; ++i) glds16_sc1(osrc + i * 1024, l16, lbase + K1_O + w * 4608 + i * 1024);
    if (lane < 32) glds16_sc1(osrc + 4096, l16, lbase + K1_O + w * 4608 + 4096);
  }
  asm volatile("s_waitcnt vmcnt(5)" : "+v"(xr[0]), "+v"(xr[1]), "+v"(xr[2]), "+v"(mq3[0]), "+v"(mq3[1]), "+v"(mq3[2]) :: "memory");
  static_for<0, 18>([&](auto I) { pin(wo[decltype(I)::value]); });
  static_for<0, 5>([&](auto I) { pin(wo8[decltype(I)::value]); });
  if (tid < 144) {
    float t[4];
#pragma unroll
    for (int j = 0; j < 4; ++j) t[j] = (mq3[0][j] + mq3[1][j]) + mq3[2][j];
    rb2(t[0], t[1]); rb2(t[2], t[3]);
    if (tid >= 72 && tid < 108) {
#pragma unroll
      for (int j = 0; j < 4; ++j) t[j] += 1.f;
      rb2(t[0], t[1]); rb2(t[2], t[3]);
    }
    *(LAS f32x4*)(lds + L_C2 + tid * 16) = (f32x4){t[0], t[1], t[2], t[3]};
  }
  asm volatile("s_waitcnt vmcnt(0) lgkmcnt(0)" ::: "memory");
  BAR();
  f32x4 ac2[3];
  ac2[0] = (f32x4){0.f, 0.f, 0.f, 0.f}; ac2[1] = ac2[0]; ac2[2] = ac2[0];
  {
    unsigned oa = lbase + K1_O + l16, o8 = oa + ((w & 1) * 18 + (w >> 1)) * 1024;
    pinu(oa, o8);
    s16x8 of[2][6];
    auto rdo = [&](auto Cx) {
      constexpr int c = decltype(Cx)::value;
#pragma unroll
      for (int i = 0; i < 3; ++i) { of[c & 1][2 * i] = *(const LAS s16x8*)(size_t)(oa + (3 * c + i) * 1024); of[c & 1][2 * i + 1] = *(const LAS s16x8*)(size_t)(oa + (18 + 3 * c + i) * 1024); }
    };
    s16x8 o8f[5];
#pragma unroll
    for (int i = 0; i < 5; ++i) o8f[i] = *(const LAS s16x8*)(size_t)(o8 + ((i < 4 || w < 4) ? i : 0) * 4096);
    rdo(IC<0>{});
#pragma unroll
    for (int i = 0; i < 4; ++i) ac2[2] = __builtin_amdgcn_mfma_f32_16x16x32_bf16(__builtin_bit_cast(s16x8, wo8[i]), o8f[i], ac2[2], 0, 0, 0);
    if (w < 4) ac2[2] = __builtin_amdgcn_mfma_f32_16x16x32_bf16(__builtin_bit_cast(s16x8, wo8[4]), o8f[4], ac2[2], 0, 0, 0);
    static_for<0, 6>([&](auto Cx) {
      constexpr int c = decltype(Cx)::value;
      if constexpr (c < 5) rdo(IC<c + 1>{});
      if constexpr (c == 1) *(LAS f32x4*)(lds + L_CONST + w * 1024 + l16) = ac2[2];
#pragma unroll
      for (int i = 0; i < 3; ++i) {
        ac2[0] = __builtin_amdgcn_mfma_f32_16x16x32_bf16(__builtin_bit_cast(s16x8, wo[3 * c + i]), of[c & 1][2 * i], ac2[0], 0, 0, 0);
        ac2[1] = __builtin_amdgcn_mfma_f32_16x16x32_bf16(__builtin_bit_cast(s16x8, wo[3 * c + i]), of[c & 1][2 * i + 1], ac2[1], 0, 0, 0);
      }
      SB0();
    });
  }
  WAIT_L(0);
  BAR();
  const LAS float* c2 = (const LAS float*)(lds + L_C2);
  float v[3][4];
  f32x2 sq[3];
  LAS f32x2* st = (LAS f32x2*)(lds + L_ST);
  LAS f32x2* fin = (LAS f32x2*)(lds + L_FIN);
  {
    const f32x4 ga = *(const LAS f32x4*)(c2 + w * 16 + 4 * g);
    resid_stats(ac2[0], xr[0], ga, v[0], xr[0], sq[0]);
    resid_stats(ac2[1], xr[1], ga, v[1], xr[1], sq[1]);
    if (g < 2) st[w * 32 + g * 16 + li] = g ? sq[1] : sq[0];
    if (w < 2) {
      const LAS f32x4* t8 = (const LAS f32x4*)(lds + L_CONST + w * 1024 + l16);
      const f32x4 a8 = (t8[0] + t8[128]) + (t8[256] + t8[384]);
      resid_stats(a8, xr[2], *(const LAS f32x4*)(c2 + 128 + 4 * g), v[2], xr[2], sq[2]);
      if (g == 0) st[8 * 32 + w * 16 + li] = sq[2];
    }
  }
  WAIT_L(0);
  BAR();
  if (w == 7) {
    const int r = lane & 31;
    float mt_ = 0.f;
    f32x2 sv[9];
#pragma unroll
    for (int k = 0; k < 9; ++k) { sv[k] = st[k * 32 + r]; mt_ += sv[k][0]; }
    mt_ *= (1.f / 9.f);
    float m2 = 0.f;
#pragma unroll
    for (int k = 0; k < 9; ++k) { const float dl = sv[k][0] - mt_; m2 += sv[k][1] + 16.f * dl * dl; }
    GAS unsigned long long* xch = (GAS unsigned long long*)(ws + OFF_XCH) + (size_t)rt2 * 256;
    const float mine = (lane < 32) ? mt_ : m2;
    __hip_atomic_store(xch + nq2 * 64 + lane, ((unsigned long long)XCH_TAG << 32) | __float_as_uint(mine), __ATOMIC_RELAXED, __HIP_MEMORY_SCOPE_AGENT);
    float o[4];
    for (unsigned spin = 0; spin < (1u << 16); ++spin) {
      bool ok = true;
#pragma unroll
      for (int k = 0; k < 4; ++k) {
        const unsigned long long gv = __hip_atomic_load(xch + k * 64 + lane, __ATOMIC_RELAXED, __HIP_MEMORY_SCOPE_AGENT);
        o[k] = __uint_as_float((unsigned)gv); ok &= ((unsigned)(gv >> 32) == XCH_TAG);
      }
      if (__all(ok)) break;
      __builtin_amdgcn_s_sleep(1);
    }
    const float mean = 0.25f * ((o[0] + o[1]) + (o[2] + o[3]));
    float t = 0.f;
#pragma unroll
    for (int k = 0; k < 4; ++k) { const float dl = o[k] - mean; t += 144.f * dl * dl; }
    const auto sw = __builtin_amdgcn_permlane32_swap(__float_as_uint(mean), __float_as_uint(mean), false, false);
    const float m2s = 4.f * __uint_as_float(sw[1]);
    if (lane < 32) {
      const float var = (t + m2s) * (1.f / D);
      fin[r] = (f32x2){rb(mean), __builtin_amdgcn_rcpf(rb(__builtin_amdgcn_sqrtf(rb(rb(var) + 1e-5f))))};
    }
  }
  const f32x4 sb = *(const LAS f32x4*)(c2 + 144 + w * 16 + 4 * g), g2 = *(const LAS f32x4*)(c2 + 288 + w * 16 + 4 * g);
  WAIT_L(0);
  BAR();
  {
    const char* h2b = ws + OFF_H2 + (size_t)(rt2 * 36 + nq2 * 9) * 1024;
    const unsigned ho = ((g >> 1) * 32 + li) * 16 + (g & 1) * 8;
    gstore8_sc1(norm_mod(v[0], fin[li], g2, sb), ho + w * 1024, h2b);
    gstore8_sc1(norm_mod(v[1], fin[16 + li], g2, sb), ho + w * 1024 + 256, h2b);
    if (w < 2) gstore8_sc1(norm_mod(v[2], fin[w * 16 + li], *(const LAS f32x4*)(c2 + 288 + 128 + 4 * g), *(const LAS f32x4*)(c2 + 144 + 128 + 4 * g)), ho + 8 * 1024 + w * 256, h2b);
  }
  {
    const char* x1b = ws + OFF_X1 + (size_t)(rt2 * 72 + nq2 * 9) * 512;
    gstore8_sc1(xr[0], lane * 8 + w * 512, x1b); gstore8_sc1(xr[1], lane * 8 + (36 + w) * 512, x1b);
    if (w < 2) gstore8_sc1(xr[2], lane * 8 + (w * 36 + 8) * 512, x1b);
  }
  WAIT_V(0);
  arrive(lds + G1_ARR + 12, (GAS unsigned*)(ws + OFF_CNT) + (48 + (rt2 >> 3)) * 64, lane);
#pragma unroll
  for (int i = 0; i < 10; ++i) G1_G(i / 5, i % 5);
  }
  const GAS unsigned* cntw = (const GAS unsigned*)(ws + OFF_CNT) + (16 + (mt2 >> 1) * 4 + ksl) * 64;
  unsigned flagv;
  GAS unsigned* cntB;
  const char* wsrc2 = (const char*)w2 + (size_t)(ksl * 1152 + (w >> 2) * 144 + (w & 3) * 16) * (D * 2) + nq * 288;
  const char* wsrc2b = (const char*)w2 + (size_t)(ksl * 1152 + w * 144 + 128) * (D * 2) + nq * 288;
  const unsigned wdst2 = lbase + w * 4608;
#define G2_G(st, i) do { const char* _s = ((st) < 8) ? wsrc2 + (size_t)(((st) & 3) * 288 + ((st) >> 2) * 64) * (D * 2) : wsrc2b; const unsigned _d = wdst2 + (((st) + 1) & 3) * G1_STAGE; \
    if ((i) < 4) glds16(_s, so[i], _d + (i) * 1024); else if (lane < 32) glds16(_s, so[4], _d + 4096); } while (0)
  {
    const char* xsrc = ws + OFF_H2 + (size_t)(mt * 8 + w) * (36 * 1024);
    u32x4 xa[4], xb[4], xc[4];
#define G1_X(dst, st, i) do { const char* _p = xsrc + (st) * 4096; asm volatile("global_load_dwordx4 %0, %1, %2 offset:%3 sc1" : "=v"(dst[i]) : "v"(xvo), "s"(_p), "n"((i) * 1024) : "memory"); } while (0)
    const int grp = lane >> 4, q = (lane & 15) >> 2, p4 = lane & 3;
    const unsigned ta = (grp & 1) * G1_W3 + (8 * (grp >> 1) + q) * 288 + (((p4 & 1) << 4) | ((p4 >> 1) << 3));
    f32x16 acc[9];
#pragma unroll
    for (int t = 0; t < 9; ++t)
      for (int j = 0; j < 16; ++j) acc[t][j] = 0.f;
    s16x8 wf[4];
#define G1_RDKT(b, ks, t) cat8(tr_read((b) + (16 * (ks)) * 288 + (t) * 32), tr_read((b) + (16 * (ks) + 4) * 288 + (t) * 32))
#define G1_RD(b, i) G1_RDKT(b, (i) / 9, (i) % 9)
    const unsigned w2o = (ksl * 10368 + idx * 324 + (w * 7 + lane < 54 ? w * 7 + lane : 53)) * 128;
#define G1_STEP(xf, xn, st) do { LAS char* _b = lds + ((st) & 3) * G1_STAGE + ta; LAS char* _n = lds + (((st) + 1) & 3) * G1_STAGE + ta; \
    if ((st) >= 1) { if (lane < 7) touch((const char*)w2 + ((st) - 1) * (54 * 128), w2o, lbase + L_SCR); } \
    if ((st) == 0) { wf[0] = G1_RD(_b, 0); wf[1] = G1_RD(_b, 1); wf[2] = G1_RD(_b, 2); } \
    _Pragma("unroll") for (int i = 0; i < 36; ++i) { \
      if (i + 3 < 36) wf[(i + 3) & 3] = G1_RD(_b, i + 3); \
      else if ((st) < 6) wf[(i + 3) & 3] = G1_RD(_n, i + 3 - 36); \
      else if ((st) == 6) wf[(i + 3) & 3] = G1_RDKT(_n, i + 3 - 36, 0); \
      acc[i % 9] = __builtin_amdgcn_mfma_f32_32x32x16_bf16(wf[i & 3], __builtin_bit_cast(s16x8, xf[i / 9]), acc[i % 9], 0, 0, 0); \
      if ((st) == 0) { if ((i & 1) == 1 && (i >> 1) < 14) { const int v = i >> 1; \
        if (v < 4) G1_X(xn, 1, (v) & 3); else if (v < 9) G1_G(2, (v < 4) ? 0 : (v - 4) % 5); else G1_G(3, (v < 9) ? 0 : (v - 9) % 5); } } \
      else if ((i & 3) == 1) { const int v = i >> 2; \
        if (v < 4) G1_X(xn, (st) + 1, (v) & 3); \
        else if ((st) + 3 < 9) G1_G((st) + 3, (v - 4 < 0) ? 0 : v - 4); \
        else if ((st) == 6 && v < 8) G1_X(xc, 8, (v) & 3); } \
      SB0(); } } while (0)
    if (w == 7) wait_count((const GAS unsigned*)(ws + OFF_CNT) + (48 + mt) * 64, 32u);
    WAIT_V(0);
    BAR();
#pragma unroll
    for (int i = 0; i < 4; ++i) G1_X(xa, 0, i);
    G1_WAITX(0, xa); G1_STEP(xa, xb, 0);
    G1_WAITX(5, xb); BAR(); G1_STEP(xb, xa, 1);
    G1_WAITX(5, xa); BAR(); G1_STEP(xa, xb, 2);
    G1_WAITX(5, xb); BAR(); G1_STEP(xb, xa, 3);
    G1_WAITX(5, xa); BAR(); G1_STEP(xa, xb, 4);
    G1_WAITX(5, xb); BAR(); G1_STEP(xb, xa, 5);
    G1_WAITX(5, xa); BAR(); G1_STEP(xa, xb, 6);
    G1_WAITX(0, xb); G1_WAITX(0, xc); BAR();
#pragma unroll
    for (int i = 0; i < 5; ++i) {
      const int ci = i * 64 + lane;
      const int pr = ci / 18, ch = ci - pr * 18;
      const int r = pr ^ (((pr >> 3) & 1) << 2);
      so[i] = r * (D * 2) + ch * 16;
    }
    LAS char* b7 = lds + 3 * G1_STAGE + ta; LAS char* b8 = lds + ta;
    const int m = lane & 31, h = lane >> 5;
    const int row = mt * 256 + w * 32 + m;
    const int cl = idx >> 2;
    const unsigned dbase = (((row >> 4) * 144 + ksl * 36) * 64 + h * 16 + (row & 15)) * 16;
    const unsigned d03 = dbase + cl * 2048, d8 = dbase + (32 + (cl >> 1)) * 1024 + (cl & 1) * 512;
    GAS unsigned* cntA = (GAS unsigned*)(ws + OFF_CNT) + (16 + mt * 4 + ksl) * 64;
#define G1_RT(i) (((i) & 7) < 4 ? G1_RDKT(b7, (i) & 3, (i) >> 3) : G1_RDKT(b8, (i) & 3, (i) >> 3))
    float e[8];
    static_for<0, 80>([&](auto I) {
      constexpr int i = decltype(I)::value, t = i >> 3, j = i & 7;
      if constexpr (i + 3 < 72) wf[(i + 3) & 3] = G1_RT(i + 3);
      if constexpr (i < 72) {
        const u32x4 xx = (j < 4) ? xb[j & 3] : xc[j & 3];
        acc[t] = __builtin_amdgcn_mfma_f32_32x32x16_bf16(wf[i & 3], __builtin_bit_cast(s16x8, xx), acc[t], 0, 0, 0);
      }
      if constexpr (t >= 1) {
        e[j] = fast_silu(acc[t - 1][j]) * acc[t - 1][8 + j];
        if constexpr (j == 7) {
          constexpr int tt = t - 1;
          const u32x4 hv = (u32x4){pk_bf16(e[0], e[1]), pk_bf16(e[2], e[3]), pk_bf16(e[4], e[5]), pk_bf16(e[6], e[7])};
          if constexpr (tt < 4) gstore16_sc1<(tt >> 1) * 1024 + (tt & 1) * 512>(hv, d03, hhb);
          else if constexpr (tt < 8) gstore16_sc1<((tt & 3) >> 1) * 1024 + (tt & 1) * 512>(hv, d03, hhb + 16 * 1024);
          else gstore16_sc1<0>(hv, d8, hhb);
        }
      }
      if constexpr (i >= 2 && i < 40 && (i & 3) == 2) { constexpr int v = (i - 2) >> 2; G2_G(v / 5, v % 5); }
      if constexpr (i == 54) {
        WAIT_V(1);
        arrive(lds + G1_ARR, cntA, lane);
      }
      if constexpr (i == 74) { if (w == 7) gload4_sc1(flagv, cntw); }
      SB0();
    });
    cntB = cntA + 32;
  }
  if (w == 7) { wait_flag<1>(flagv); if (__builtin_amdgcn_readfirstlane(flagv) < 8u) wait_count(cntw, 8u); }
  const char* xs0 = hhb + ((size_t)(mt2 * 8 + mq * 2 + kg) * 144 + ksl * 36 + kg * 2) * 1024;
  const char* xs1 = hhb + ((size_t)(mt2 * 8 + mq * 2 + (kg ^ 1)) * 144 + ksl * 36 + kg * 2) * 1024;
  u32x4 xr[3][4];
#define G2_X(st, i) gload16_sc1<((i) >> 1) * 1024>(xr[(st) % 3][i], xvo, (((i) & 1) ? xs1 : xs0) + (st) * 4096)
  const int g = lane >> 4;
  unsigned tb[2][2];
  {
    const int q = (lane & 15) >> 2, p4 = lane & 3;
#pragma unroll
    for (int t = 0; t < 2; ++t) { tb[0][t] = lbase + G1_STAGE + (kg * 64 + 8 * g + ((4 * t) ^ (4 * (g & 1))) + q) * 288 + p4 * 8; tb[1][t] = tb[0][t] + G1_STAGE; }
    pinu(tb[0][0], tb[0][1]); pinu(tb[1][0], tb[1][1]);
  }
  f32x4 acc[2][9];
#pragma unroll
  for (int i = 0; i < 2; ++i)
#pragma unroll
    for (int t = 0; t < 9; ++t) { acc[i][t] = (f32x4){0.f, 0.f, 0.f, 0.f}; asm volatile("" : "+v"(acc[i][t])); }
  constexpr int PF2 = 5, RG2 = 6;
  s16x8 wf[RG2];
#define G2_RD(st, i) cat8(tr_read((LAS char*)(size_t)(tb[(st) & 1][0] + ((i) / 9) * (32 * 288) + ((i) % 9) * 32)), \
                          tr_read((LAS char*)(size_t)(tb[(st) & 1][1] + ((i) / 9) * (32 * 288) + ((i) % 9) * 32)))
  BAR();
  static_for<0, 8>([&](auto I) { constexpr int i = decltype(I)::value; G2_X(i / 4, i % 4); });
  static_for<0, PF2>([&](auto I) { constexpr int i = decltype(I)::value; wf[i % RG2] = G2_RD(0, i); });
  wait4<4>(xr[0]);
  arrive(lds + G1_ARR + 4, cntB, lane);
  const int fm = w & 1, fs = w >> 1;
  const int T = mt2 * 8 + 2 * ksl + fm;
  const int col = nq * 144 + 32 * fs + 4 * g;
  u32x2 x1v[3]; f32x4 gb[3];
  auto issue_p5 = [&]() {
    const char* xp = ws + OFF_X1 + (size_t)(T * 36 + nq * 9) * 512;
    const unsigned l8 = lane * 8;
    gload8_sc1(x1v[0], l8 + fs * 1024, xp); gload8_sc1(x1v[1], l8 + fs * 1024 + 512, xp); gload8_sc1(x1v[2], l8 + 4096, xp);
  };
  static_for<0, 162>([&](auto U) {
    constexpr int u = decltype(U)::value, st = u / 18, i = u % 18;
    if constexpr (i == 12 && st < 8) {
      wait4<g2_issued_before(u) - 9 * (st + 2)>(xr[(st + 1) % 3]);
      if constexpr (st == 1) {
        wait_flag<g2_issued_before(u) - 9 * (st + 2)>(flagv);
        if (w == 0) { if (__builtin_amdgcn_readfirstlane(flagv) < 8u) wait_count(cntw + 32, 8u); }
      }
      BAR();
      if constexpr (st >= 1) {
        constexpr unsigned d = (((st + 2) & 3) >= 2) ? 2 * G1_STAGE : (unsigned)(-2 * G1_STAGE);
        tb[(st + 1) & 1][0] += d; tb[(st + 1) & 1][1] += d;
        pinu(tb[(st + 1) & 1][0], tb[(st + 1) & 1][1]);
      }
    }
    constexpr int op = g2_op_at(u);
    if constexpr (u == 17) gload4_sc1(flagv, cntw + 32);
    if constexpr (op >= 0) { if constexpr (op % 9 < 5) G2_G(op / 9, op % 9); else G2_X(op / 9, op % 9 - 5); }
    if constexpr (u == 140) issue_p5();
    if constexpr (u + PF2 < 162) wf[(u + PF2) % RG2] = G2_RD((u + PF2) / 18, (u + PF2) % 18);
    MFMA16A(wf[u % RG2], xr[st % 3][2 * (i / 9)], acc[0][i % 9]);
    MFMA16A(wf[u % RG2], xr[st % 3][2 * (i / 9) + 1], acc[1][i % 9]);
    SB0();
  });
  asm volatile("s_nop 7" : "+v"(acc[0][0]), "+v"(acc[0][1]), "+v"(acc[0][2]), "+v"(acc[0][3]), "+v"(acc[0][4]), "+v"(acc[0][5]), "+v"(acc[0][6]), "+v"(acc[0][7]), "+v"(acc[0][8]),
               "+v"(acc[1][0]), "+v"(acc[1][1]), "+v"(acc[1][2]), "+v"(acc[1][3]), "+v"(acc[1][4]), "+v"(acc[1][5]), "+v"(acc[1][6]), "+v"(acc[1][7]), "+v"(acc[1][8]) :: "memory");
  BAR();
  LAS f32x4* ex = (LAS f32x4*)lds;
#pragma unroll
  for (int t = 0; t < 9; ++t) ex[(w * 9 + t) * 64 + lane] = acc[1][t];
  WAIT_L(0);
  BAR();
  {
    f32x4 v[9];
#pragma unroll
    for (int t = 0; t < 9; ++t) v[t] = acc[0][t] + ex[((w ^ 4) * 9 + t) * 64 + lane];
    if (mq == ksl) {
      LAS f32x4* own = ex + (72 + kg * 9) * 64 + lane;
#pragma unroll
      for (int t = 0; t < 9; ++t) own[t * 64] = v[t];
    } else {
      const char* dst = ws + OFF_PART + ((size_t)(((ksl * 16 + mt2) * 4 + nq) * 8 + mq * 2 + kg) * 5) * 1024;
      static_for<0, 4>([&](auto I) { constexpr int i = decltype(I)::value;
        gstore16_sc1<i * 1024>((u32x4){pk_bf16(v[2 * i][0], v[2 * i][1]), pk_bf16(v[2 * i][2], v[2 * i][3]), pk_bf16(v[2 * i + 1][0], v[2 * i + 1][1]), pk_bf16(v[2 * i + 1][2], v[2 * i + 1][3])}, xvo, dst); });
      gstore8_sc1((u32x2){pk_bf16(v[8][0], v[8][1]), pk_bf16(v[8][2], v[8][3])}, xvo + 4096, dst);
    }
  }
  asm volatile("s_waitcnt vmcnt(0) lgkmcnt(0)" : "+v"(x1v[0]), "+v"(x1v[1]), "+v"(x1v[2]) :: "memory");
  BAR();
  GAS unsigned* cnt4 = (GAS unsigned*)(ws + OFF_CNT) + (64 + mt2 * 4 + nq) * 64;
  if (tid == 448) __hip_atomic_fetch_add(cnt4, 1u, __ATOMIC_RELAXED, __HIP_MEMORY_SCOPE_AGENT);
  f32x4 y[3];
  {
    const LAS float* gp = (const LAS float*)(lds + L_C2) + 432 + 4 * g;
    gb[0] = *(const LAS f32x4*)(gp + 32 * fs); gb[1] = *(const LAS f32x4*)(gp + 32 * fs + 16); gb[2] = *(const LAS f32x4*)(gp + 128);
  }
  y[0] = ex[(72 + fm * 9 + 2 * fs) * 64 + lane]; y[1] = ex[(72 + fm * 9 + 2 * fs + 1) * 64 + lane]; y[2] = ex[(72 + fm * 9 + 8) * 64 + lane];
  if (w == 7) wait_count(cnt4, 4u);
  BAR();
  {
    u32x4 pa[3], pb[3];
#pragma unroll
    for (int k = 0; k < 3; ++k) {
      const int src = (ksl + 1 + k) & 3;
      const char* p = ws + OFF_PART + ((size_t)(((src * 16 + mt2) * 4 + nq) * 8 + 2 * ksl + fm) * 5) * 1024;
      gload16_sc1<0>(pa[k], xvo + fs * 1024, p);
      if (fs == 0) gload16_sc1<0>(pb[k], xvo + 4096, p);
    }
    asm volatile("s_waitcnt vmcnt(0)" : "+v"(pa[0]), "+v"(pa[1]), "+v"(pa[2]), "+v"(pb[0]), "+v"(pb[1]), "+v"(pb[2]) :: "memory");
#pragma unroll
    for (int k = 0; k < 3; ++k) {
      y[0] += (f32x4){bf_lo(pa[k][0]), bf_hi(pa[k][0]), bf_lo(pa[k][1]), bf_hi(pa[k][1])};
      y[1] += (f32x4){bf_lo(pa[k][2]), bf_hi(pa[k][2]), bf_lo(pa[k][3]), bf_hi(pa[k][3])};
      y[2] += (f32x4){bf_lo(pb[k][0]), bf_hi(pb[k][0]), bf_lo(pb[k][1]), bf_hi(pb[k][1])};
    }
    const int row = 32 * (T >> 1) + 2 * (lane & 15) + (T & 1);
    u16* op = out + (size_t)row * D;
#pragma unroll
    for (int k = 0; k < 3; ++k) {
      const u32x2 o = (u32x2){pk_bf16(y[k][0] * gb[k][0] + bf_lo(x1v[k][0]), y[k][1] * gb[k][1] + bf_hi(x1v[k][0])),
                              pk_bf16(y[k][2] * gb[k][2] + bf_lo(x1v[k][1]), y[k][3] * gb[k][3] + bf_hi(x1v[k][1]))};
      if (k < 2) *(u32x2*)(op + col + 16 * k) = o;
      else if (fs == 0) *(u32x2*)(op + nq * 144 + 128 + 4 * g) = o;
    }
  }
}

extern "C" __global__ void k_end_marker() {}

extern "C" void kernel_launch(void* const* d_in, const int* in_sizes, int n_in, void* d_out, int out_size,
                              void* d_ws, size_t ws_size, hipStream_t stream) {
  const u16* x = (const u16*)d_in[0];
  const u16* wqkv = (const u16*)d_in[1];
  const u16* wo = (const u16*)d_in[2];
  const u16* w1 = (const u16*)d_in[4];
  const u16* w3 = (const u16*)d_in[5];
  const u16* w2 = (const u16*)d_in[6];
  const u16* c = (const u16*)d_in[7];
  const u16* wmod = (const u16*)d_in[8];
  char* ws = (char*)d_ws;
  (void)hipFuncSetAttribute((const void*)km_main, hipFuncAttributeMaxDynamicSharedMemorySize, KM_LDS);
  k0_prep<<<K0_F, 384, 0, stream>>>(x, c, wmod, wqkv, wo, ws, w1, w3, w2, (const u16*)d_out);
  km_main<<<256, 512, KM_LDS, stream>>>(ws, x, w1, w3, w2, (u16*)d_out);
}
```
